```python
import torch

def _softmax(x, axis=-1):
    return torch.softmax(x, dim=axis)
def _gelu(x):
    return torch.nn.functional.gelu(x, approximate="tanh")
def _silu(x):
    return torch.nn.functional.silu(x)
def _sigmoid(x):
    return torch.sigmoid(x)
def _layernorm(x, axis=-1, eps=1e-5):
    m = x.mean(dim=axis, keepdim=True)
    v = x.var(dim=axis, keepdim=True, unbiased=False)
    return (x - m) / torch.sqrt(v + eps)
def _scatter_add(tgt, idx, src, axis=0):
    return tgt.clone().index_add_(axis, idx, src)
def _segment_sum(src, seg, n):
    out = torch.zeros((n,) + src.shape[1:], dtype=src.dtype)
    return out.index_add_(0, seg, src)
def _pad(x, pw):
    flat = tuple(v for pair in reversed(pw) for v in pair)
    return torch.nn.functional.pad(x, flat)

kernel_name = 'parallel_sum__sliding_local_dilated__swiglu_ffn__prompt'

def setup_inputs():

    _w = (lambda a: (lambda t: t.to(torch.bfloat16) if t.is_floating_point() else t)(torch.as_tensor(a)))
    def _A_setup():
        B, S, D = 1, 2048, 576
        return {
            "x": _w((lambda *s: torch.randn(*s, dtype=torch.bfloat16) * 0.05)(B, S, D)),
            "wqkv": _w((lambda *s: torch.randn(*s, dtype=torch.bfloat16) * 0.1)(D, 3 * D)),
            "wo": _w((lambda *s: torch.randn(*s, dtype=torch.bfloat16) * 0.05)(D, D)),
        }
    _A_in = _A_setup()

    def _B_setup():
        B, S, D, F = 1, 2048, 576, 4608
        return {
            "x": _w((lambda *s: torch.randn(*s, dtype=torch.bfloat16) * 0.05)(B, S, D)),
            "w_gate": _w((lambda *s: torch.randn(*s, dtype=torch.bfloat16) * 0.05)(D, F)),
            "w_up": _w((lambda *s: torch.randn(*s, dtype=torch.bfloat16) * 0.05)(D, F)),
            "w_down": _w((lambda *s: torch.randn(*s, dtype=torch.bfloat16) * 0.05)(F, D)),
        }
    _B_in = _B_setup()

    return {
        "A_x": _A_in["x"],
        "A_wqkv": _A_in["wqkv"],
        "A_wo": _A_in["wo"],
        "B_x": _B_in["x"],
        "B_w_gate": _B_in["w_gate"],
        "B_w_up": _B_in["w_up"],
        "B_w_down": _B_in["w_down"],
    }

def reference(A_x, A_wqkv, A_wo, B_x, B_w_gate, B_w_up, B_w_down):
    def _A_ref(x, wqkv, wo):
        B, S, D = x.shape
        H = 32; Dh = D // H
        W, dil = 8, 2
        qkv = (lambda x, *d: x.permute(*d))((x @ wqkv).reshape(B, S, 3, H, Dh), 2, 0, 3, 1, 4)
        q, k, v = qkv[0], qkv[1], qkv[2]
        scale = Dh ** -0.5
        pos = torch.arange(S)
        off = torch.arange(W) * dil
        idx = pos[:, None] - off[None, :]
        mask = idx >= 0
        idx_c = torch.where(mask, idx, 0).reshape(-1)
        kg = k[:, :, idx_c, :].reshape(B, H, S, W, Dh)
        vg = v[:, :, idx_c, :].reshape(B, H, S, W, Dh)
        logits = torch.einsum("bhsd,bhswd->bhsw", q, kg) * scale
        logits = torch.where(mask[None, None, :, :], logits, -1e9)
        attn = _softmax(logits, axis=-1)
        o = torch.einsum("bhsw,bhswd->bhsd", attn, vg)
        return (lambda x, *d: x.permute(*d))(o, 0, 2, 1, 3).reshape(B, S, D) @ wo

    def _B_ref(x, w_gate, w_up, w_down):
        g = _silu(x @ w_gate)
        u = x @ w_up
        return (g * u) @ w_down

    x = A_x
    xn = _layernorm(x, axis=-1)
    return x + _A_ref(xn, A_wqkv, A_wo) + _B_ref(xn, B_w_gate, B_w_up, B_w_down)
```

```cpp
#include <hip/hip_runtime.h>
#include <hip/hip_bf16.h>

__global__ __launch_bounds__(256, 1) void fused_main(const void* x, const void* wqkv, const void* wo, const void* wg,
                                                     const void* wu, const void* wd, void* ws, void* out) {
  __shared__ __attribute__((aligned(16))) char smem[163840];
  unsigned wid = __builtin_amdgcn_readfirstlane(threadIdx.x >> 6);
  asm volatile(
"s_load_dwordx2 s[20:21], %0, 0\n"
"s_load_dwordx2 s[32:33], %0, 8\n"
"s_load_dwordx2 s[40:41], %0, 16\n"
"s_load_dwordx2 s[24:25], %0, 24\n"
"s_load_dwordx2 s[28:29], %0, 32\n"
"s_load_dwordx2 s[36:37], %0, 40\n"
"s_load_dwordx2 s[44:45], %0, 48\n"
"s_load_dwordx2 s[96:97], %0, 56\n"
"s_mov_b32 s52, %1\n"
"s_mov_b32 s53, %2\n"
"v_mbcnt_lo_u32_b32 v233, -1, 0\n"
"v_mbcnt_hi_u32_b32 v233, -1, v233\n"
"s_getpc_b64 s[14:15]\n"
"L_pc_%=:\n"
"v_and_b32 v243, 31, v233\n"
"s_lshl_b32 s67, s53, 12\n"
"v_lshl_add_u32 v243, v243, 7, s67\n"
"s_mov_b32 exec_hi, 0\n"
"global_load_dword v215, v243, s[14:15]\n"
"s_mov_b32 exec_hi, -1\n"
"s_lshr_b32 s57, s52, 4\n"
"s_lshl_b32 s73, s57, 2\n"
"s_add_u32 s73, s73, s53\n"
"s_and_b32 s54, s53, 1\n"
"s_lshr_b32 s55, s53, 1\n"
"v_lshrrev_b32 v140, 3, v233\n"
"v_and_b32 v141, 7, v233\n"
"v_mul_u32_u24 v254, 0x480, v140\n"
"v_lshl_add_u32 v254, v141, 4, v254\n"
"s_lshl_b32 s67, s57, 2\n"
"s_lshl_b32 s68, s54, 1\n"
"s_add_u32 s67, s67, s68\n"
"s_add_u32 s67, s67, s55\n"
"s_mul_i32 s17, s67, 0x9000\n"
"s_lshr_b32 s68, s52, 3\n"
"s_and_b32 s58, s68, 1\n"
"s_and_b32 s68, s68, 1\n"
"s_and_b32 s56, s52, 7\n"
"s_lshl_b32 s56, s56, 1\n"
"s_add_u32 s56, s56, s68\n"
"v_and_b32 v242, 15, v233\n"
"s_and_b32 s68, s73, 31\n"
"s_mul_i32 s68, s68, 0x600\n"
"s_add_u32 s68, s68, 0x4000\n"
"v_lshl_add_u32 v242, v242, 7, s68\n"
"v_min_u32 v242, L_end_%=-L_pc_%=-4, v242\n"
"s_mul_i32 s60, s56, 0x240\n"
"s_lshl_b32 s68, s58, 6\n"
"s_add_u32 s60, s60, s68\n"
"s_mul_i32 s67, s73, 9\n"
"v_and_b32 v142, 63, v233\n"
"v_add_u32 v142, s67, v142\n"
"v_mul_u32_u24 v209, 0x2400, v142\n"
"v_mov_b32 v143, 0x40000000\n"
"v_add_u32 v209, s60, v209\n"
"v_mul_u32_u24 v142, 43, v233\n"
"v_lshrrev_b32 v142, 8, v142\n"
"v_mul_u32_u24 v228, 6, v142\n"
"v_sub_u32 v228, v233, v228\n"
"v_add_u32 v142, s67, v142\n"
"v_mul_u32_u24 v142, 0xd80, v142\n"
"v_and_b32 v235, 1, v228\n"
"v_lshl_add_u32 v142, v235, 6, v142\n"
"v_lshrrev_b32 v228, 1, v228\n"
"v_mul_u32_u24 v228, 0x480, v228\n"
"s_mul_i32 s68, s56, 0x48\n"
"s_and_b32 s68, s68, 0xffffffc0\n"
"s_lshr_b32 s68, s56, 1\n"
"s_add_u32 s68, s68, s58\n"
"s_lshl_b32 s68, s68, 7\n"
"v_cmp_gt_u32 vcc, 54, v233\n"
"v_add3_u32 v228, v228, v142, s68\n"
"s_nop 0\n"
"v_cndmask_b32 v228, v143, v228, vcc\n"
"s_lshl_b32 s67, s57, 8\n"
"s_add_u32 s18, s67, 0x1800000\n"
"v_mov_b32 v236, 1\n"
"v_mov_b32 v237, 0\n"
"v_and_b32 v142, 3, v141\n"
"v_xor_b32 v140, v140, v142\n"
"v_lshlrev_b32 v140, 4, v140\n"
"v_lshl_add_u32 v140, v142, 8, v140\n"
"s_waitcnt lgkmcnt(0)\n"
"s_and_b32 s21, s21, 0xffff\n"
"s_mov_b32 s22, 0x240000\n"
"s_mov_b32 s23, 0x20000\n"
"buffer_load_dwordx4 v[0:3], v254, s[20:23], s17 offen offset:0\n"
"buffer_load_dwordx4 v[4:7], v254, s[20:23], s17 offen offset:128\n"
"buffer_load_dwordx4 v[8:11], v254, s[20:23], s17 offen offset:256\n"
"buffer_load_dwordx4 v[12:15], v254, s[20:23], s17 offen offset:384\n"
"buffer_load_dwordx4 v[16:19], v254, s[20:23], s17 offen offset:512\n"
"buffer_load_dwordx4 v[20:23], v254, s[20:23], s17 offen offset:640\n"
"buffer_load_dwordx4 v[24:27], v254, s[20:23], s17 offen offset:768\n"
"buffer_load_dwordx4 v[28:31], v254, s[20:23], s17 offen offset:896\n"
"buffer_load_dwordx4 v[32:35], v254, s[20:23], s17 offen offset:1024\n"
"s_add_u32 s17, s17, 0x2400\n"
"s_and_b32 s25, s25, 0xffff\n"
"s_mov_b32 s26, 0x510000\n"
"s_mov_b32 s27, 0x20000\n"
"s_and_b32 s29, s29, 0xffff\n"
"s_mov_b32 s30, 0x510000\n"
"s_mov_b32 s31, 0x20000\n"
"s_and_b32 s33, s33, 0xffff\n"
"s_mov_b32 s34, 0x1e6000\n"
"s_mov_b32 s35, 0x20000\n"
"s_and_b32 s45, s45, 0xffff\n"
"s_mov_b32 s46, 0x3200000\n"
"s_mov_b32 s47, 0x20000\n"
"v_lshrrev_b32 v141, 2, v141\n"
"v_mul_u32_u24 v141, 0x440, v141\n"
"v_add_u32 v255, v140, v141\n"
"v_and_b32 v140, 15, v233\n"
"v_lshrrev_b32 v141, 4, v233\n"
"s_mul_i32 s59, s53, 0x420\n"
"s_mul_i32 s68, s53, 0x2400\n"
"s_add_u32 s60, s60, s68\n"
"v_bfe_u32 v142, v233, 3, 1\n"
"v_lshlrev_b32 v142, 3, v142\n"
"v_bfe_u32 v143, v233, 5, 1\n"
"v_lshl_add_u32 v142, v143, 4, v142\n"
"v_bfe_u32 v143, v233, 4, 1\n"
"v_lshl_add_u32 v142, v143, 2, v142\n"
"v_and_b32 v143, 7, v233\n"
"v_lshlrev_b32 v143, 4, v143\n"
"v_mul_u32_u24 v207, 0x2400, v142\n"
"v_add_u32 v207, v207, v143\n"
"s_and_b32 s41, s41, 0xffff\n"
"s_mov_b32 s42, 0xa2000\n"
"s_mov_b32 s43, 0x20000\n"
"s_and_b32 s37, s37, 0xffff\n"
"s_mov_b32 s38, 0x510000\n"
"s_mov_b32 s39, 0x20000\n"
"s_and_b32 s97, s97, 0xffff\n"
"s_mov_b32 s98, 0x240000\n"
"s_mov_b32 s99, 0x20000\n"
"s_cmp_eq_u32 s55, 0\n"
"s_cselect_b32 s48, s24, s28\n"
"s_cselect_b32 s49, s25, s29\n"
"s_mov_b32 s50, s26\n"
"s_mov_b32 s51, s27\n"
"v_mul_u32_u24 v244, 0xd80, v142\n"
"v_add_u32 v208, v244, v143\n"
"s_mul_i32 s61, s56, 0x48\n"
"s_and_b32 s69, s61, 63\n"
"s_sub_u32 s61, s61, s69\n"
"s_mul_i32 s68, s53, 0xd80\n"
"s_add_u32 s61, s61, s68\n"
"v_bfe_u32 v244, v233, 3, 1\n"
"v_bfe_u32 v252, v233, 5, 1\n"
"v_lshl_add_u32 v244, v252, 1, v244\n"
"v_bfe_u32 v253, v233, 4, 1\n"
"v_lshlrev_b32 v246, 2, v244\n"
"v_lshl_add_u32 v247, v253, 4, v246\n"
"v_mul_u32_u24 v211, 0x480, v247\n"
"v_lshl_add_u32 v247, v253, 2, v247\n"
"v_mul_u32_u24 v212, 0x480, v247\n"
"v_mul_u32_u24 v247, 0x900, v253\n"
"v_mov_b32 v248, 0x40000000\n"
"buffer_load_dwordx4 v[36:39], v254, s[20:23], s17 offen offset:0\n"
"buffer_load_dwordx4 v[40:43], v254, s[20:23], s17 offen offset:128\n"
"buffer_load_dwordx4 v[44:47], v254, s[20:23], s17 offen offset:256\n"
"buffer_load_dwordx4 v[48:51], v254, s[20:23], s17 offen offset:384\n"
"buffer_load_dwordx4 v[52:55], v254, s[20:23], s17 offen offset:512\n"
"buffer_load_dwordx4 v[56:59], v254, s[20:23], s17 offen offset:640\n"
"buffer_load_dwordx4 v[60:63], v254, s[20:23], s17 offen offset:768\n"
"buffer_load_dwordx4 v[64:67], v254, s[20:23], s17 offen offset:896\n"
"buffer_load_dwordx4 v[68:71], v254, s[20:23], s17 offen offset:1024\n"
"s_add_u32 s17, s17, 0x2400\n"
"v_cmp_eq_u32 vcc, 0, v244\n"
"s_nop 1\n"
"v_cndmask_b32 v213, v248, v247, vcc\n"
"v_bfe_u32 v244, v233, 2, 3\n"
"v_lshl_add_u32 v244, v252, 4, v244\n"
"v_mul_u32_u24 v244, 0x2400, v244\n"
"v_and_b32 v250, 3, v233\n"
"v_lshl_add_u32 v210, v250, 4, v244\n"
"s_cmp_eq_u32 s58, 0\n"
"s_cselect_b32 s67, 0x100, 0\n"
"s_mul_i32 s68, s56, 0x120\n"
"s_add_u32 s67, s67, s68\n"
"s_lshl_b32 s62, s67, 1\n"
"s_add_u32 s64, s67, s53\n"
"s_mul_i32 s64, s64, 0x480\n"
"s_mul_i32 s67, s54, 0x12000\n"
"s_add_u32 s62, s62, s67\n"
"s_lshl_b32 s67, s58, 5\n"
"s_add_u32 s63, s68, s67\n"
"s_add_u32 s63, s63, s53\n"
"s_mul_i32 s63, s63, 0x480\n"
"s_mul_i32 s65, s56, 36\n"
"s_add_u32 s65, s65, s53\n"
"s_mul_i32 s65, s65, 0x480\n"
"v_bfe_u32 v251, v233, 2, 2\n"
"v_mul_u32_u24 v244, 0x420, v251\n"
"v_lshl_add_u32 v244, v253, 7, v244\n"
"v_lshl_add_u32 v244, v252, 9, v244\n"
"v_lshl_add_u32 v247, v250, 3, v244\n"
"s_lshl_b32 s67, s55, 6\n"
"v_add_u32 v200, s67, v247\n"
"v_add_u32 v201, 0xe700, v200\n"
"v_add_u32 v202, 0x1ce00, v200\n"
"s_mul_i32 s67, s55, 40\n"
"s_add_u32 s67, s67, s69\n"
"v_add_u32 v214, s67, v247\n"
"s_mul_i32 s68, s54, 0x1080\n"
"s_add_u32 s68, s68, 0x1080\n"
"v_add_u32 v217, s68, v214\n"
"v_min_u32 v248, 2, v250\n"
"v_mul_u32_u24 v248, 0x1080, v248\n"
"s_add_u32 s68, s69, 32\n"
"v_add3_u32 v216, v244, v248, s68\n"
"v_mul_u32_u24 v244, 0x420, v253\n"
"v_lshl_add_u32 v244, v251, 6, v244\n"
"v_lshl_add_u32 v244, v252, 9, v244\n"
"v_lshl_add_u32 v244, v250, 3, v244\n"
"s_lshl_b32 s67, s55, 5\n"
"v_add_u32 v219, s67, v244\n"
"v_lshlrev_b32 v139, 4, v233\n"
"v_xor_b32 v244, v233, v141\n"
"v_lshlrev_b32 v244, 4, v244\n"
"v_add_u32 v218, 0x23100, v244\n"
"s_mul_i32 s67, s54, 0x1080\n"
"s_add_u32 s67, s67, 0xe700\n"
"s_mul_i32 s68, s55, 0x840\n"
"s_add_u32 s69, s67, s68\n"
"v_add_u32 v203, s69, v244\n"
"v_add_u32 v255, s69, v255\n"
"s_xor_b32 s68, s68, 0x840\n"
"s_add_u32 s69, s67, s68\n"
"v_add_u32 v204, s69, v244\n"
"s_mul_i32 s67, s55, 0x2100\n"
"v_add_u32 v205, s67, v203\n"
"v_add_u32 v206, s67, v204\n"
"v_lshlrev_b32 v234, 2, v140\n"
"s_lshl_b32 s67, s57, 7\n"
"s_lshl_b32 s68, s53, 2\n"
"s_add_u32 s67, s67, s68\n"
"s_sub_u32 s67, s67, 16\n"
"s_mul_i32 s67, s67, 0x480\n"
"s_cmp_eq_u32 s57, 0\n"
"s_cselect_b32 s67, 0x40000000, s67\n"
"v_mul_u32_u24 v244, 0x480, v141\n"
"v_add_u32 v244, s67, v244\n"
"v_lshl_add_u32 v240, v140, 4, v244\n"
"v_lshl_add_u32 v241, v140, 3, v244\n"
"s_lshl_b32 s68, s53, 6\n"
"s_add_u32 s68, s68, 0x23100\n"
"v_xor_b32 v244, v141, v250\n"
"v_lshlrev_b32 v244, 4, v244\n"
"v_lshl_add_u32 v244, v250, 8, v244\n"
"v_mul_u32_u24 v248, 0x420, v251\n"
"v_add3_u32 v244, v244, v248, s68\n"
"v_bfe_u32 v248, v233, 2, 1\n"
"v_lshl_add_u32 v238, v248, 5, v244\n"
"v_bfe_u32 v248, v233, 1, 2\n"
"v_xor_b32 v244, v141, v248\n"
"v_lshlrev_b32 v244, 4, v244\n"
"v_lshl_add_u32 v244, v248, 8, v244\n"
"v_bfe_u32 v248, v233, 3, 1\n"
"v_mul_u32_u24 v248, 0x440, v248\n"
"v_add3_u32 v244, v244, v248, s68\n"
"v_and_b32 v248, 1, v140\n"
"v_lshl_add_u32 v239, v248, 3, v244\n"
"s_lshl_b32 s67, s54, 1\n"
"s_add_u32 s68, s67, s55\n"
"s_mul_i32 s68, s68, 0x1500\n"
"s_add_u32 s68, s68, 0x18c00\n"
"s_cmp_eq_u32 s55, 0\n"
"s_cselect_b32 s19, 1, -1\n"
"s_mul_i32 s19, s19, 0x1500\n"
"v_mul_u32_u24 v244, 0xa8, v140\n"
"v_lshl_add_u32 v248, v141, 3, v244\n"
"v_add_u32 v131, s68, v244\n"
"v_add_u32 v130, s68, v248\n"
"s_lshl_b32 s67, s55, 5\n"
"v_add_u32 v128, s67, v130\n"
"v_add_u32 v129, s19, v128\n"
"v_add_u32 v135, 0x18c00, v248\n"
"s_lshl_b32 s69, s54, 6\n"
"s_add_u32 s69, s69, s67\n"
"v_add_u32 v134, s69, v135\n"
"v_lshl_add_u32 v249, v141, 2, v251\n"
"v_mul_u32_u24 v249, 0xa8, v249\n"
"v_add_u32 v249, s68, v249\n"
"v_lshl_add_u32 v132, v250, 3, v249\n"
"v_add_u32 v132, s67, v132\n"
"s_lshl_b32 s67, s55, 3\n"
"v_add_u32 v133, s67, v249\n"
"v_and_b32 v244, 1, v140\n"
"v_cmp_eq_u32 s[74:75], 1, v244\n"
"v_lshl_add_u32 v248, v141, 2, v244\n"
"v_cmp_le_u32 s[76:77], v248, v140\n"
"v_add_u32 v248, 2, v248\n"
"v_cmp_le_u32 s[78:79], v248, v140\n"
"v_cmp_eq_u32 s[92:93], 3, v141\n"
"v_cmp_le_u32 s[94:95], 2, v141\n"
"v_cmp_eq_u32 vcc, 0, v141\n"
"v_mov_b32 v248, 0xffff\n"
"v_mov_b32 v249, 0xffff0000\n"
"v_cndmask_b32 v248, v248, v249, s[74:75]\n"
"v_cndmask_b32 v229, v248, 0, s[76:77]\n"
"v_cndmask_b32 v230, v248, 0, s[78:79]\n"
"v_cndmask_b32 v231, 0, v248, s[76:77]\n"
"v_cndmask_b32 v232, 0, v248, s[78:79]\n"
"s_cmp_eq_u32 s55, 0\n"
"s_cselect_b64 s[88:89], vcc, 0\n"
"s_cselect_b64 s[90:91], 0, vcc\n"
"s_not_b64 vcc, s[76:77]\n"
"s_not_b64 s[70:71], s[78:79]\n"
"s_lshl_b32 s67, s57, 2\n"
"s_add_u32 s67, s67, s53\n"
"s_cmp_eq_u32 s67, 0\n"
"s_cselect_b64 s[80:81], vcc, 0\n"
"s_cselect_b64 s[82:83], s[70:71], 0\n"
"s_cmp_eq_u32 s67, 2\n"
"s_cselect_b64 s[84:85], vcc, 0\n"
"s_cselect_b64 s[86:87], s[70:71], 0\n"
"v_mov_b32 v137, 0xf149f2ca\n"
"v_mov_b32 v138, 0x3f803f80\n"
"s_mov_b32 s70, 0x3f803f80\n"
"s_mov_b32 s71, 0x3f80\n"
"s_mov_b32 s72, 0x3f800000\n"
"s_mov_b32 s16, 0x3e274c22\n"
"v_cmp_eq_u32 vcc, v251, v141\n"
"v_and_b32 v244, 1, v233\n"
"v_lshlrev_b32 v244, 4, v244\n"
"v_mov_b32 v248, 0x3f80\n"
"v_lshlrev_b32 v244, v244, v248\n"
"v_cndmask_b32 v244, 0, v244, vcc\n"
"v_cmp_gt_u32 vcc, 2, v250\n"
"s_nop 1\n"
"v_cndmask_b32 v248, 0, v244, vcc\n"
"v_cndmask_b32 v249, v244, 0, vcc\n"
"v_accvgpr_write_b32 a4, v248\n"
"v_accvgpr_write_b32 a14, v248\n"
"v_accvgpr_write_b32 a5, v249\n"
"v_accvgpr_write_b32 a15, v249\n"
"v_accvgpr_write_b32 a6, 0\n"
"v_accvgpr_write_b32 a12, 0\n"
"v_accvgpr_write_b32 a7, 0\n"
"v_accvgpr_write_b32 a13, 0\n"
"s_waitcnt vmcnt(17)\n"
"v_dot2_f32_bf16 v244, v0, s70, 0\n"
"v_dot2_f32_bf16 v245, v1, s70, 0\n"
"v_dot2_f32_bf16 v246, v2, s70, 0\n"
"v_dot2_f32_bf16 v247, v3, s70, 0\n"
"s_waitcnt vmcnt(16)\n"
"v_dot2_f32_bf16 v244, v4, s70, v244\n"
"v_dot2_f32_bf16 v245, v5, s70, v245\n"
"v_dot2_f32_bf16 v246, v6, s70, v246\n"
"v_dot2_f32_bf16 v247, v7, s70, v247\n"
"s_waitcnt vmcnt(15)\n"
"v_dot2_f32_bf16 v244, v8, s70, v244\n"
"v_dot2_f32_bf16 v245, v9, s70, v245\n"
"v_dot2_f32_bf16 v246, v10, s70, v246\n"
"v_dot2_f32_bf16 v247, v11, s70, v247\n"
"s_waitcnt vmcnt(14)\n"
"v_dot2_f32_bf16 v244, v12, s70, v244\n"
"v_dot2_f32_bf16 v245, v13, s70, v245\n"
"v_dot2_f32_bf16 v246, v14, s70, v246\n"
"v_dot2_f32_bf16 v247, v15, s70, v247\n"
"s_waitcnt vmcnt(13)\n"
"v_dot2_f32_bf16 v244, v16, s70, v244\n"
"v_dot2_f32_bf16 v245, v17, s70, v245\n"
"v_dot2_f32_bf16 v246, v18, s70, v246\n"
"v_dot2_f32_bf16 v247, v19, s70, v247\n"
"s_waitcnt vmcnt(12)\n"
"v_dot2_f32_bf16 v244, v20, s70, v244\n"
"v_dot2_f32_bf16 v245, v21, s70, v245\n"
"v_dot2_f32_bf16 v246, v22, s70, v246\n"
"v_dot2_f32_bf16 v247, v23, s70, v247\n"
"s_waitcnt vmcnt(11)\n"
"v_dot2_f32_bf16 v244, v24, s70, v244\n"
"v_dot2_f32_bf16 v245, v25, s70, v245\n"
"v_dot2_f32_bf16 v246, v26, s70, v246\n"
"v_dot2_f32_bf16 v247, v27, s70, v247\n"
"s_waitcnt vmcnt(10)\n"
"v_dot2_f32_bf16 v244, v28, s70, v244\n"
"v_dot2_f32_bf16 v245, v29, s70, v245\n"
"v_dot2_f32_bf16 v246, v30, s70, v246\n"
"v_dot2_f32_bf16 v247, v31, s70, v247\n"
"s_waitcnt vmcnt(9)\n"
"v_dot2_f32_bf16 v244, v32, s70, v244\n"
"v_dot2_f32_bf16 v245, v33, s70, v245\n"
"v_dot2_f32_bf16 v246, v34, s70, v246\n"
"v_dot2_f32_bf16 v247, v35, s70, v247\n"
"s_nop 0\n"
"v_add_f32 v244, v244, v245\n"
"s_nop 0\n"
"v_add_f32 v246, v246, v247\n"
"v_add_f32 v244, v244, v246\n"
"s_nop 1\n"
"v_add_f32_dpp v244, v244, v244 quad_perm:[1,0,3,2] row_mask:0xf bank_mask:0xf\n"
"s_nop 1\n"
"v_add_f32_dpp v244, v244, v244 quad_perm:[2,3,0,1] row_mask:0xf bank_mask:0xf\n"
"s_nop 1\n"
"v_add_f32_dpp v244, v244, v244 row_half_mirror row_mask:0xf bank_mask:0xf\n"
"v_mul_f32 v244, 0xbae38e39, v244\n"
"v_cvt_pk_bf16_f32 v248, v244, v244\n"
"v_lshlrev_b32 v248, 16, v248\n"
"v_sub_f32 v224, v244, v248\n"
"v_mov_b32 v244, v248\n"
"v_mov_b32 v245, v248\n"
"v_mov_b32 v246, v248\n"
"v_mov_b32 v247, v248\n"
"s_nop 1\n"
"v_mfma_f32_16x16x32_bf16 v[192:195], a[4:7], v[0:3], v[244:247]\n"
"v_mfma_f32_16x16x32_bf16 v[196:199], a[12:15], v[0:3], v[244:247]\n"
"buffer_load_dwordx4 v[72:75], v254, s[20:23], s17 offen offset:0\n"
"buffer_load_dwordx4 v[76:79], v254, s[20:23], s17 offen offset:128\n"
"v_mfma_f32_16x16x32_bf16 v[148:151], a[4:7], v[4:7], v[244:247]\n"
"v_mfma_f32_16x16x32_bf16 v[156:159], a[12:15], v[4:7], v[244:247]\n"
"buffer_load_dwordx4 v[80:83], v254, s[20:23], s17 offen offset:256\n"
"v_mfma_f32_16x16x32_bf16 v[164:167], a[4:7], v[8:11], v[244:247]\n"
"s_nop 0\n"
"v_cvt_pk_bf16_f32 v0, v192, v193\n"
"v_cvt_pk_bf16_f32 v1, v194, v195\n"
"v_mfma_f32_16x16x32_bf16 v[172:175], a[12:15], v[8:11], v[244:247]\n"
"buffer_load_dwordx4 v[84:87], v254, s[20:23], s17 offen offset:384\n"
"v_cvt_pk_bf16_f32 v2, v196, v197\n"
"v_cvt_pk_bf16_f32 v3, v198, v199\n"
"v_mfma_f32_16x16x32_bf16 v[192:195], a[4:7], v[12:15], v[244:247]\n"
"ds_write_b128 v255, v[0:3] offset:0\n"
"v_cvt_pk_bf16_f32 v4, v148, v149\n"
"v_cvt_pk_bf16_f32 v5, v150, v151\n"
"v_mfma_f32_16x16x32_bf16 v[196:199], a[12:15], v[12:15], v[244:247]\n"
"buffer_load_dwordx4 v[88:91], v254, s[20:23], s17 offen offset:512\n"
"v_cvt_pk_bf16_f32 v6, v156, v157\n"
"v_cvt_pk_bf16_f32 v7, v158, v159\n"
"v_mfma_f32_16x16x32_bf16 v[148:151], a[4:7], v[16:19], v[244:247]\n"
"ds_write_b128 v255, v[4:7] offset:8448\n"
"v_cvt_pk_bf16_f32 v8, v164, v165\n"
"v_cvt_pk_bf16_f32 v9, v166, v167\n"
"v_mfma_f32_16x16x32_bf16 v[156:159], a[12:15], v[16:19], v[244:247]\n"
"buffer_load_dwordx4 v[92:95], v254, s[20:23], s17 offen offset:640\n"
"buffer_load_dwordx4 v[96:99], v254, s[20:23], s17 offen offset:768\n"
"v_cvt_pk_bf16_f32 v10, v172, v173\n"
"v_cvt_pk_bf16_f32 v11, v174, v175\n"
"v_mfma_f32_16x16x32_bf16 v[164:167], a[4:7], v[20:23], v[244:247]\n"
"ds_write_b128 v255, v[8:11] offset:16896\n"
"v_cvt_pk_bf16_f32 v12, v192, v193\n"
"v_cvt_pk_bf16_f32 v13, v194, v195\n"
"v_mfma_f32_16x16x32_bf16 v[172:175], a[12:15], v[20:23], v[244:247]\n"
"buffer_load_dwordx4 v[100:103], v254, s[20:23], s17 offen offset:896\n"
"v_cvt_pk_bf16_f32 v14, v196, v197\n"
"v_cvt_pk_bf16_f32 v15, v198, v199\n"
"v_mfma_f32_16x16x32_bf16 v[192:195], a[4:7], v[24:27], v[244:247]\n"
"ds_write_b128 v255, v[12:15] offset:25344\n"
"v_cvt_pk_bf16_f32 v16, v148, v149\n"
"v_cvt_pk_bf16_f32 v17, v150, v151\n"
"v_mfma_f32_16x16x32_bf16 v[196:199], a[12:15], v[24:27], v[244:247]\n"
"buffer_load_dwordx4 v[104:107], v254, s[20:23], s17 offen offset:1024\n"
"s_add_u32 s17, s17, 0x2400\n"
"v_cvt_pk_bf16_f32 v18, v156, v157\n"
"v_cvt_pk_bf16_f32 v19, v158, v159\n"
"v_mfma_f32_16x16x32_bf16 v[148:151], a[4:7], v[28:31], v[244:247]\n"
"ds_write_b128 v255, v[16:19] offset:33792\n"
"v_cvt_pk_bf16_f32 v20, v164, v165\n"
"v_cvt_pk_bf16_f32 v21, v166, v167\n"
"v_mfma_f32_16x16x32_bf16 v[156:159], a[12:15], v[28:31], v[244:247]\n"
"s_add_u32 s66, s60, 0\n"
"s_add_u32 m0, s59, 0\n"
"s_nop 0\n"
"buffer_load_dwordx4 v207, s[24:27], s66 offen lds\n"
"v_cvt_pk_bf16_f32 v22, v172, v173\n"
"v_cvt_pk_bf16_f32 v23, v174, v175\n"
"v_mfma_f32_16x16x32_bf16 v[164:167], a[4:7], v[32:35], v[244:247]\n"
"ds_write_b128 v255, v[20:23] offset:42240\n"
"v_cvt_pk_bf16_f32 v24, v192, v193\n"
"v_cvt_pk_bf16_f32 v25, v194, v195\n"
"v_mfma_f32_16x16x32_bf16 v[172:175], a[12:15], v[32:35], v[244:247]\n"
"s_add_u32 m0, s59, 0x1080\n"
"s_nop 0\n"
"buffer_load_dwordx4 v207, s[28:31], s66 offen lds\n"
"v_cvt_pk_bf16_f32 v26, v196, v197\n"
"v_cvt_pk_bf16_f32 v27, v198, v199\n"
"ds_write_b128 v255, v[24:27] offset:50688\n"
"v_cvt_pk_bf16_f32 v28, v148, v149\n"
"v_cvt_pk_bf16_f32 v29, v150, v151\n"
"v_cvt_pk_bf16_f32 v30, v156, v157\n"
"v_cvt_pk_bf16_f32 v31, v158, v159\n"
"ds_write_b128 v255, v[28:31] offset:59136\n"
"v_cvt_pk_bf16_f32 v32, v164, v165\n"
"v_cvt_pk_bf16_f32 v33, v166, v167\n"
"v_cvt_pk_bf16_f32 v34, v172, v173\n"
"v_cvt_pk_bf16_f32 v35, v174, v175\n"
"v_add_u32 v255, 0xe700, v255\n"
"ds_write_b128 v255, v[32:35] offset:8448\n"
"v_dot2_f32_bf16 v250, v0, v0, 0\n"
"v_dot2_f32_bf16 v251, v1, v1, 0\n"
"v_dot2_f32_bf16 v252, v2, v2, 0\n"
"v_dot2_f32_bf16 v253, v3, v3, 0\n"
"v_dot2_f32_bf16 v250, v4, v4, v250\n"
"v_dot2_f32_bf16 v251, v5, v5, v251\n"
"v_dot2_f32_bf16 v252, v6, v6, v252\n"
"v_dot2_f32_bf16 v253, v7, v7, v253\n"
"v_dot2_f32_bf16 v250, v8, v8, v250\n"
"v_dot2_f32_bf16 v251, v9, v9, v251\n"
"v_dot2_f32_bf16 v252, v10, v10, v252\n"
"v_dot2_f32_bf16 v253, v11, v11, v253\n"
"v_dot2_f32_bf16 v250, v12, v12, v250\n"
"v_dot2_f32_bf16 v251, v13, v13, v251\n"
"v_dot2_f32_bf16 v252, v14, v14, v252\n"
"v_dot2_f32_bf16 v253, v15, v15, v253\n"
"v_dot2_f32_bf16 v250, v16, v16, v250\n"
"v_dot2_f32_bf16 v251, v17, v17, v251\n"
"v_dot2_f32_bf16 v252, v18, v18, v252\n"
"v_dot2_f32_bf16 v253, v19, v19, v253\n"
"v_dot2_f32_bf16 v250, v20, v20, v250\n"
"v_dot2_f32_bf16 v251, v21, v21, v251\n"
"v_dot2_f32_bf16 v252, v22, v22, v252\n"
"v_dot2_f32_bf16 v253, v23, v23, v253\n"
"v_dot2_f32_bf16 v250, v24, v24, v250\n"
"v_dot2_f32_bf16 v251, v25, v25, v251\n"
"v_dot2_f32_bf16 v252, v26, v26, v252\n"
"v_dot2_f32_bf16 v253, v27, v27, v253\n"
"v_dot2_f32_bf16 v250, v28, v28, v250\n"
"v_dot2_f32_bf16 v251, v29, v29, v251\n"
"v_dot2_f32_bf16 v252, v30, v30, v252\n"
"v_dot2_f32_bf16 v253, v31, v31, v253\n"
"v_dot2_f32_bf16 v250, v32, v32, v250\n"
"v_dot2_f32_bf16 v251, v33, v33, v251\n"
"v_dot2_f32_bf16 v252, v34, v34, v252\n"
"v_dot2_f32_bf16 v253, v35, v35, v253\n"
"s_nop 0\n"
"v_add_f32 v250, v250, v251\n"
"s_nop 0\n"
"v_add_f32 v252, v252, v253\n"
"v_add_f32 v220, v250, v252\n"
"s_waitcnt vmcnt(19)\n"
"v_dot2_f32_bf16 v244, v36, s70, 0\n"
"v_dot2_f32_bf16 v245, v37, s70, 0\n"
"v_dot2_f32_bf16 v246, v38, s70, 0\n"
"v_dot2_f32_bf16 v247, v39, s70, 0\n"
"s_waitcnt vmcnt(18)\n"
"v_dot2_f32_bf16 v244, v40, s70, v244\n"
"v_dot2_f32_bf16 v245, v41, s70, v245\n"
"v_dot2_f32_bf16 v246, v42, s70, v246\n"
"v_dot2_f32_bf16 v247, v43, s70, v247\n"
"s_waitcnt vmcnt(17)\n"
"v_dot2_f32_bf16 v244, v44, s70, v244\n"
"v_dot2_f32_bf16 v245, v45, s70, v245\n"
"v_dot2_f32_bf16 v246, v46, s70, v246\n"
"v_dot2_f32_bf16 v247, v47, s70, v247\n"
"s_waitcnt vmcnt(16)\n"
"v_dot2_f32_bf16 v244, v48, s70, v244\n"
"v_dot2_f32_bf16 v245, v49, s70, v245\n"
"v_dot2_f32_bf16 v246, v50, s70, v246\n"
"v_dot2_f32_bf16 v247, v51, s70, v247\n"
"s_waitcnt vmcnt(15)\n"
"v_dot2_f32_bf16 v244, v52, s70, v244\n"
"v_dot2_f32_bf16 v245, v53, s70, v245\n"
"v_dot2_f32_bf16 v246, v54, s70, v246\n"
"v_dot2_f32_bf16 v247, v55, s70, v247\n"
"s_waitcnt vmcnt(14)\n"
"v_dot2_f32_bf16 v244, v56, s70, v244\n"
"v_dot2_f32_bf16 v245, v57, s70, v245\n"
"v_dot2_f32_bf16 v246, v58, s70, v246\n"
"v_dot2_f32_bf16 v247, v59, s70, v247\n"
"s_waitcnt vmcnt(13)\n"
"v_dot2_f32_bf16 v244, v60, s70, v244\n"
"v_dot2_f32_bf16 v245, v61, s70, v245\n"
"v_dot2_f32_bf16 v246, v62, s70, v246\n"
"v_dot2_f32_bf16 v247, v63, s70, v247\n"
"s_waitcnt vmcnt(12)\n"
"v_dot2_f32_bf16 v244, v64, s70, v244\n"
"v_dot2_f32_bf16 v245, v65, s70, v245\n"
"v_dot2_f32_bf16 v246, v66, s70, v246\n"
"v_dot2_f32_bf16 v247, v67, s70, v247\n"
"s_waitcnt vmcnt(11)\n"
"v_dot2_f32_bf16 v244, v68, s70, v244\n"
"v_dot2_f32_bf16 v245, v69, s70, v245\n"
"v_dot2_f32_bf16 v246, v70, s70, v246\n"
"v_dot2_f32_bf16 v247, v71, s70, v247\n"
"s_nop 0\n"
"v_add_f32 v244, v244, v245\n"
"s_nop 0\n"
"v_add_f32 v246, v246, v247\n"
"v_add_f32 v244, v244, v246\n"
"s_nop 1\n"
"v_add_f32_dpp v244, v244, v244 quad_perm:[1,0,3,2] row_mask:0xf bank_mask:0xf\n"
"s_nop 1\n"
"v_add_f32_dpp v244, v244, v244 quad_perm:[2,3,0,1] row_mask:0xf bank_mask:0xf\n"
"s_nop 1\n"
"v_add_f32_dpp v244, v244, v244 row_half_mirror row_mask:0xf bank_mask:0xf\n"
"v_mul_f32 v244, 0xbae38e39, v244\n"
"v_cvt_pk_bf16_f32 v248, v244, v244\n"
"v_lshlrev_b32 v248, 16, v248\n"
"v_sub_f32 v225, v244, v248\n"
"v_mov_b32 v244, v248\n"
"v_mov_b32 v245, v248\n"
"v_mov_b32 v246, v248\n"
"v_mov_b32 v247, v248\n"
"s_nop 1\n"
"v_mfma_f32_16x16x32_bf16 v[192:195], a[4:7], v[36:39], v[244:247]\n"
"v_mfma_f32_16x16x32_bf16 v[196:199], a[12:15], v[36:39], v[244:247]\n"
"s_add_u32 s66, s60, 0x48000\n"
"s_add_u32 m0, s59, 0x2100\n"
"s_nop 0\n"
"buffer_load_dwordx4 v207, s[24:27], s66 offen lds\n"
"s_add_u32 m0, s59, 0x3180\n"
"s_nop 0\n"
"buffer_load_dwordx4 v207, s[28:31], s66 offen lds\n"
"v_mfma_f32_16x16x32_bf16 v[148:151], a[4:7], v[40:43], v[244:247]\n"
"v_mfma_f32_16x16x32_bf16 v[156:159], a[12:15], v[40:43], v[244:247]\n"
"buffer_load_dwordx4 v[108:111], v254, s[20:23], s17 offen offset:0\n"
"v_mfma_f32_16x16x32_bf16 v[164:167], a[4:7], v[44:47], v[244:247]\n"
"v_cvt_pk_bf16_f32 v36, v192, v193\n"
"v_cvt_pk_bf16_f32 v37, v194, v195\n"
"v_mfma_f32_16x16x32_bf16 v[172:175], a[12:15], v[44:47], v[244:247]\n"
"buffer_load_dwordx4 v[112:115], v254, s[20:23], s17 offen offset:128\n"
"v_cvt_pk_bf16_f32 v38, v196, v197\n"
"v_cvt_pk_bf16_f32 v39, v198, v199\n"
"v_mfma_f32_16x16x32_bf16 v[192:195], a[4:7], v[48:51], v[244:247]\n"
"v_add_u32 v255, 0xffff1900, v255\n"
"ds_write_b128 v255, v[36:39] offset:128\n"
"v_cvt_pk_bf16_f32 v40, v148, v149\n"
"v_cvt_pk_bf16_f32 v41, v150, v151\n"
"v_mfma_f32_16x16x32_bf16 v[196:199], a[12:15], v[48:51], v[244:247]\n"
"buffer_load_dwordx4 v[116:119], v254, s[20:23], s17 offen offset:256\n"
"buffer_load_dwordx4 v[120:123], v254, s[20:23], s17 offen offset:384\n"
"v_cvt_pk_bf16_f32 v42, v156, v157\n"
"v_cvt_pk_bf16_f32 v43, v158, v159\n"
"v_mfma_f32_16x16x32_bf16 v[148:151], a[4:7], v[52:55], v[244:247]\n"
"ds_write_b128 v255, v[40:43] offset:8576\n"
"v_cvt_pk_bf16_f32 v44, v164, v165\n"
"v_cvt_pk_bf16_f32 v45, v166, v167\n"
"v_mfma_f32_16x16x32_bf16 v[156:159], a[12:15], v[52:55], v[244:247]\n"
"buffer_load_dwordx4 v[124:127], v254, s[20:23], s17 offen offset:512\n"
"v_cvt_pk_bf16_f32 v46, v172, v173\n"
"v_cvt_pk_bf16_f32 v47, v174, v175\n"
"v_mfma_f32_16x16x32_bf16 v[164:167], a[4:7], v[56:59], v[244:247]\n"
"ds_write_b128 v255, v[44:47] offset:17024\n"
"v_cvt_pk_bf16_f32 v48, v192, v193\n"
"v_cvt_pk_bf16_f32 v49, v194, v195\n"
"v_mfma_f32_16x16x32_bf16 v[172:175], a[12:15], v[56:59], v[244:247]\n"
"buffer_load_dwordx4 v[176:179], v254, s[20:23], s17 offen offset:640\n"
"v_cvt_pk_bf16_f32 v50, v196, v197\n"
"v_cvt_pk_bf16_f32 v51, v198, v199\n"
"v_mfma_f32_16x16x32_bf16 v[192:195], a[4:7], v[60:63], v[244:247]\n"
"ds_write_b128 v255, v[48:51] offset:25472\n"
"v_cvt_pk_bf16_f32 v52, v148, v149\n"
"v_cvt_pk_bf16_f32 v53, v150, v151\n"
"v_mfma_f32_16x16x32_bf16 v[196:199], a[12:15], v[60:63], v[244:247]\n"
"buffer_load_dwordx4 v[180:183], v254, s[20:23], s17 offen offset:768\n"
"buffer_load_dwordx4 v[184:187], v254, s[20:23], s17 offen offset:896\n"
"v_cvt_pk_bf16_f32 v54, v156, v157\n"
"v_cvt_pk_bf16_f32 v55, v158, v159\n"
"v_mfma_f32_16x16x32_bf16 v[148:151], a[4:7], v[64:67], v[244:247]\n"
"ds_write_b128 v255, v[52:55] offset:33920\n"
"v_cvt_pk_bf16_f32 v56, v164, v165\n"
"v_cvt_pk_bf16_f32 v57, v166, v167\n"
"v_mfma_f32_16x16x32_bf16 v[156:159], a[12:15], v[64:67], v[244:247]\n"
"buffer_load_dwordx4 v[188:191], v254, s[20:23], s17 offen offset:1024\n"
"s_add_u32 s17, s17, 0x2400\n"
"v_cvt_pk_bf16_f32 v58, v172, v173\n"
"v_cvt_pk_bf16_f32 v59, v174, v175\n"
"v_mfma_f32_16x16x32_bf16 v[164:167], a[4:7], v[68:71], v[244:247]\n"
"ds_write_b128 v255, v[56:59] offset:42368\n"
"v_cvt_pk_bf16_f32 v60, v192, v193\n"
"v_cvt_pk_bf16_f32 v61, v194, v195\n"
"v_mfma_f32_16x16x32_bf16 v[172:175], a[12:15], v[68:71], v[244:247]\n"
"s_mov_b64 exec, 0xfff\n"
"global_load_dword v215, v242, s[14:15]\n"
"s_mov_b64 exec, -1\n"
"v_cvt_pk_bf16_f32 v62, v196, v197\n"
"v_cvt_pk_bf16_f32 v63, v198, v199\n"
"ds_write_b128 v255, v[60:63] offset:50816\n"
"v_cvt_pk_bf16_f32 v64, v148, v149\n"
"v_cvt_pk_bf16_f32 v65, v150, v151\n"
"v_cvt_pk_bf16_f32 v66, v156, v157\n"
"v_cvt_pk_bf16_f32 v67, v158, v159\n"
"ds_write_b128 v255, v[64:67] offset:59264\n"
"v_cvt_pk_bf16_f32 v68, v164, v165\n"
"v_cvt_pk_bf16_f32 v69, v166, v167\n"
"v_cvt_pk_bf16_f32 v70, v172, v173\n"
"v_cvt_pk_bf16_f32 v71, v174, v175\n"
"v_add_u32 v255, 0xe700, v255\n"
"ds_write_b128 v255, v[68:71] offset:8576\n"
"v_dot2_f32_bf16 v250, v36, v36, 0\n"
"v_dot2_f32_bf16 v251, v37, v37, 0\n"
"v_dot2_f32_bf16 v252, v38, v38, 0\n"
"v_dot2_f32_bf16 v253, v39, v39, 0\n"
"v_dot2_f32_bf16 v250, v40, v40, v250\n"
"v_dot2_f32_bf16 v251, v41, v41, v251\n"
"v_dot2_f32_bf16 v252, v42, v42, v252\n"
"v_dot2_f32_bf16 v253, v43, v43, v253\n"
"v_dot2_f32_bf16 v250, v44, v44, v250\n"
"v_dot2_f32_bf16 v251, v45, v45, v251\n"
"v_dot2_f32_bf16 v252, v46, v46, v252\n"
"v_dot2_f32_bf16 v253, v47, v47, v253\n"
"v_dot2_f32_bf16 v250, v48, v48, v250\n"
"v_dot2_f32_bf16 v251, v49, v49, v251\n"
"v_dot2_f32_bf16 v252, v50, v50, v252\n"
"v_dot2_f32_bf16 v253, v51, v51, v253\n"
"v_dot2_f32_bf16 v250, v52, v52, v250\n"
"v_dot2_f32_bf16 v251, v53, v53, v251\n"
"v_dot2_f32_bf16 v252, v54, v54, v252\n"
"v_dot2_f32_bf16 v253, v55, v55, v253\n"
"v_dot2_f32_bf16 v250, v56, v56, v250\n"
"v_dot2_f32_bf16 v251, v57, v57, v251\n"
"v_dot2_f32_bf16 v252, v58, v58, v252\n"
"v_dot2_f32_bf16 v253, v59, v59, v253\n"
"v_dot2_f32_bf16 v250, v60, v60, v250\n"
"v_dot2_f32_bf16 v251, v61, v61, v251\n"
"v_dot2_f32_bf16 v252, v62, v62, v252\n"
"v_dot2_f32_bf16 v253, v63, v63, v253\n"
"v_dot2_f32_bf16 v250, v64, v64, v250\n"
"v_dot2_f32_bf16 v251, v65, v65, v251\n"
"v_dot2_f32_bf16 v252, v66, v66, v252\n"
"v_dot2_f32_bf16 v253, v67, v67, v253\n"
"v_dot2_f32_bf16 v250, v68, v68, v250\n"
"v_dot2_f32_bf16 v251, v69, v69, v251\n"
"v_dot2_f32_bf16 v252, v70, v70, v252\n"
"v_dot2_f32_bf16 v253, v71, v71, v253\n"
"s_nop 0\n"
"v_add_f32 v250, v250, v251\n"
"s_nop 0\n"
"v_add_f32 v252, v252, v253\n"
"v_add_f32 v221, v250, v252\n"
"s_waitcnt vmcnt(22)\n"
"v_dot2_f32_bf16 v244, v72, s70, 0\n"
"v_dot2_f32_bf16 v245, v73, s70, 0\n"
"v_dot2_f32_bf16 v246, v74, s70, 0\n"
"v_dot2_f32_bf16 v247, v75, s70, 0\n"
"s_waitcnt vmcnt(21)\n"
"v_dot2_f32_bf16 v244, v76, s70, v244\n"
"v_dot2_f32_bf16 v245, v77, s70, v245\n"
"v_dot2_f32_bf16 v246, v78, s70, v246\n"
"v_dot2_f32_bf16 v247, v79, s70, v247\n"
"s_waitcnt vmcnt(20)\n"
"v_dot2_f32_bf16 v244, v80, s70, v244\n"
"v_dot2_f32_bf16 v245, v81, s70, v245\n"
"v_dot2_f32_bf16 v246, v82, s70, v246\n"
"v_dot2_f32_bf16 v247, v83, s70, v247\n"
"s_waitcnt vmcnt(19)\n"
"v_dot2_f32_bf16 v244, v84, s70, v244\n"
"v_dot2_f32_bf16 v245, v85, s70, v245\n"
"v_dot2_f32_bf16 v246, v86, s70, v246\n"
"v_dot2_f32_bf16 v247, v87, s70, v247\n"
"s_waitcnt vmcnt(18)\n"
"v_dot2_f32_bf16 v244, v88, s70, v244\n"
"v_dot2_f32_bf16 v245, v89, s70, v245\n"
"v_dot2_f32_bf16 v246, v90, s70, v246\n"
"v_dot2_f32_bf16 v247, v91, s70, v247\n"
"s_waitcnt vmcnt(17)\n"
"v_dot2_f32_bf16 v244, v92, s70, v244\n"
"v_dot2_f32_bf16 v245, v93, s70, v245\n"
"v_dot2_f32_bf16 v246, v94, s70, v246\n"
"v_dot2_f32_bf16 v247, v95, s70, v247\n"
"s_waitcnt vmcnt(16)\n"
"v_dot2_f32_bf16 v244, v96, s70, v244\n"
"v_dot2_f32_bf16 v245, v97, s70, v245\n"
"v_dot2_f32_bf16 v246, v98, s70, v246\n"
"v_dot2_f32_bf16 v247, v99, s70, v247\n"
"s_waitcnt vmcnt(15)\n"
"v_dot2_f32_bf16 v244, v100, s70, v244\n"
"v_dot2_f32_bf16 v245, v101, s70, v245\n"
"v_dot2_f32_bf16 v246, v102, s70, v246\n"
"v_dot2_f32_bf16 v247, v103, s70, v247\n"
"s_waitcnt vmcnt(14)\n"
"v_dot2_f32_bf16 v244, v104, s70, v244\n"
"v_dot2_f32_bf16 v245, v105, s70, v245\n"
"v_dot2_f32_bf16 v246, v106, s70, v246\n"
"v_dot2_f32_bf16 v247, v107, s70, v247\n"
"s_nop 0\n"
"v_add_f32 v244, v244, v245\n"
"s_nop 0\n"
"v_add_f32 v246, v246, v247\n"
"v_add_f32 v244, v244, v246\n"
"s_nop 1\n"
"v_add_f32_dpp v244, v244, v244 quad_perm:[1,0,3,2] row_mask:0xf bank_mask:0xf\n"
"s_nop 1\n"
"v_add_f32_dpp v244, v244, v244 quad_perm:[2,3,0,1] row_mask:0xf bank_mask:0xf\n"
"s_nop 1\n"
"v_add_f32_dpp v244, v244, v244 row_half_mirror row_mask:0xf bank_mask:0xf\n"
"v_mul_f32 v244, 0xbae38e39, v244\n"
"v_cvt_pk_bf16_f32 v248, v244, v244\n"
"v_lshlrev_b32 v248, 16, v248\n"
"v_sub_f32 v226, v244, v248\n"
"s_waitcnt lgkmcnt(0)\n"
"s_barrier\n"
"v_mov_b32 v244, v248\n"
"v_mov_b32 v245, v248\n"
"v_mov_b32 v246, v248\n"
"v_mov_b32 v247, v248\n"
"s_nop 1\n"
"v_mfma_f32_16x16x32_bf16 v[192:195], a[4:7], v[72:75], v[244:247]\n"
"ds_read_b128 a[0:3], v203 offset:0\n"
"ds_read_b128 a[8:11], v204 offset:0\n"
"v_mfma_f32_16x16x32_bf16 v[196:199], a[12:15], v[72:75], v[244:247]\n"
"s_add_u32 s66, s60, 0x90000\n"
"s_add_u32 m0, s59, 0x4200\n"
"s_nop 0\n"
"buffer_load_dwordx4 v207, s[24:27], s66 offen lds\n"
"s_add_u32 m0, s59, 0x5280\n"
"s_nop 0\n"
"buffer_load_dwordx4 v207, s[28:31], s66 offen lds\n"
"ds_read_b128 a[16:19], v203 offset:1088\n"
"ds_read_b128 a[24:27], v204 offset:1088\n"
"v_mfma_f32_16x16x32_bf16 v[148:151], a[4:7], v[76:79], v[244:247]\n"
"ds_read_b128 a[32:35], v203 offset:8448\n"
"ds_read_b128 a[40:43], v204 offset:8448\n"
"v_mfma_f32_16x16x32_bf16 v[156:159], a[12:15], v[76:79], v[244:247]\n"
"s_add_u32 s66, s60, 0xd8000\n"
"s_add_u32 m0, s59, 0x6300\n"
"s_nop 0\n"
"buffer_load_dwordx4 v207, s[24:27], s66 offen lds\n"
"ds_read_b128 a[48:51], v203 offset:9536\n"
"ds_read_b128 a[56:59], v204 offset:9536\n"
"v_mfma_f32_16x16x32_bf16 v[164:167], a[4:7], v[80:83], v[244:247]\n"
"ds_read_b128 a[64:67], v203 offset:16896\n"
"ds_read_b128 a[72:75], v204 offset:16896\n"
"v_cvt_pk_bf16_f32 v72, v192, v193\n"
"v_cvt_pk_bf16_f32 v73, v194, v195\n"
"v_mfma_f32_16x16x32_bf16 v[172:175], a[12:15], v[80:83], v[244:247]\n"
"s_add_u32 m0, s59, 0x7380\n"
"s_nop 0\n"
"buffer_load_dwordx4 v207, s[28:31], s66 offen lds\n"
"ds_read_b128 a[80:83], v203 offset:17984\n"
"ds_read_b128 a[88:91], v204 offset:17984\n"
"v_cvt_pk_bf16_f32 v74, v196, v197\n"
"v_cvt_pk_bf16_f32 v75, v198, v199\n"
"v_mfma_f32_16x16x32_bf16 v[192:195], a[4:7], v[84:87], v[244:247]\n"
"ds_read_b128 a[96:99], v203 offset:25344\n"
"ds_read_b128 a[104:107], v204 offset:25344\n"
"v_cvt_pk_bf16_f32 v76, v148, v149\n"
"v_cvt_pk_bf16_f32 v77, v150, v151\n"
"v_mfma_f32_16x16x32_bf16 v[196:199], a[12:15], v[84:87], v[244:247]\n"
"s_mov_b64 exec, 0x1ff\n buffer_load_dword v215, v209, s[24:27], 0 offen offset:0\n s_mov_b64 exec, -1\n"
"ds_read_b128 a[112:115], v203 offset:26432\n"
"ds_read_b128 a[120:123], v204 offset:26432\n"
"v_cvt_pk_bf16_f32 v78, v156, v157\n"
"v_cvt_pk_bf16_f32 v79, v158, v159\n"
"v_mfma_f32_16x16x32_bf16 v[148:151], a[4:7], v[88:91], v[244:247]\n"
"ds_read_b128 a[128:131], v203 offset:33792\n"
"ds_read_b128 a[136:139], v204 offset:33792\n"
"v_cvt_pk_bf16_f32 v80, v164, v165\n"
"v_cvt_pk_bf16_f32 v81, v166, v167\n"
"v_mfma_f32_16x16x32_bf16 v[156:159], a[12:15], v[88:91], v[244:247]\n"
"s_mov_b64 exec, 0x1ff\n buffer_load_dword v215, v209, s[28:31], 0 offen offset:0\n s_mov_b64 exec, -1\n"
"ds_read_b128 a[144:147], v203 offset:34880\n"
"ds_read_b128 a[152:155], v204 offset:34880\n"
"v_cvt_pk_bf16_f32 v82, v172, v173\n"
"v_cvt_pk_bf16_f32 v83, v174, v175\n"
"v_mfma_f32_16x16x32_bf16 v[164:167], a[4:7], v[92:95], v[244:247]\n"
"ds_read_b128 a[160:163], v203 offset:42240\n"
"ds_read_b128 a[168:171], v204 offset:42240\n"
"v_cvt_pk_bf16_f32 v84, v192, v193\n"
"v_cvt_pk_bf16_f32 v85, v194, v195\n"
"v_mfma_f32_16x16x32_bf16 v[172:175], a[12:15], v[92:95], v[244:247]\n"
"s_add_u32 s66, s60, 0x120000\n"
"s_add_u32 m0, s59, 0x8400\n"
"s_nop 0\n"
"buffer_load_dwordx4 v207, s[24:27], s66 offen lds\n"
"ds_read_b128 a[176:179], v203 offset:43328\n"
"ds_read_b128 a[184:187], v204 offset:43328\n"
"v_cvt_pk_bf16_f32 v86, v196, v197\n"
"v_cvt_pk_bf16_f32 v87, v198, v199\n"
"v_mfma_f32_16x16x32_bf16 v[192:195], a[4:7], v[96:99], v[244:247]\n"
"ds_read_b128 a[192:195], v203 offset:50688\n"
"ds_read_b128 a[200:203], v204 offset:50688\n"
"v_cvt_pk_bf16_f32 v88, v148, v149\n"
"v_cvt_pk_bf16_f32 v89, v150, v151\n"
"v_mfma_f32_16x16x32_bf16 v[196:199], a[12:15], v[96:99], v[244:247]\n"
"s_add_u32 m0, s59, 0x9480\n"
"s_nop 0\n"
"buffer_load_dwordx4 v207, s[28:31], s66 offen lds\n"
"ds_read_b128 a[208:211], v203 offset:51776\n"
"ds_read_b128 a[216:219], v204 offset:51776\n"
"v_cvt_pk_bf16_f32 v90, v156, v157\n"
"v_cvt_pk_bf16_f32 v91, v158, v159\n"
"v_mfma_f32_16x16x32_bf16 v[148:151], a[4:7], v[100:103], v[244:247]\n"
"ds_read_b128 a[224:227], v203 offset:59136\n"
"ds_read_b128 a[232:235], v204 offset:59136\n"
"v_cvt_pk_bf16_f32 v92, v164, v165\n"
"v_cvt_pk_bf16_f32 v93, v166, v167\n"
"v_mfma_f32_16x16x32_bf16 v[156:159], a[12:15], v[100:103], v[244:247]\n"
"s_add_u32 s66, s60, 0x168000\n"
"s_add_u32 m0, s59, 0xa500\n"
"s_nop 0\n"
"buffer_load_dwordx4 v207, s[24:27], s66 offen lds\n"
"ds_read_b128 a[240:243], v203 offset:60224\n"
"ds_read_b128 a[248:251], v204 offset:60224\n"
"v_cvt_pk_bf16_f32 v94, v172, v173\n"
"v_cvt_pk_bf16_f32 v95, v174, v175\n"
"v_mfma_f32_16x16x32_bf16 v[164:167], a[4:7], v[104:107], v[244:247]\n"
"v_add_u32 v203, 0xe700, v203\n"
"ds_read_b128 v[144:147], v203 offset:8448\n"
"v_add_u32 v204, 0xe700, v204\n"
"ds_read_b128 v[152:155], v204 offset:8448\n"
"v_cvt_pk_bf16_f32 v96, v192, v193\n"
"v_cvt_pk_bf16_f32 v97, v194, v195\n"
"v_mfma_f32_16x16x32_bf16 v[172:175], a[12:15], v[104:107], v[244:247]\n"
"s_add_u32 m0, s59, 0xb580\n"
"s_nop 0\n"
"buffer_load_dwordx4 v207, s[28:31], s66 offen lds\n"
"ds_read_b128 v[160:163], v203 offset:9536\n"
"ds_read_b128 v[168:171], v204 offset:9536\n"
"v_cvt_pk_bf16_f32 v98, v196, v197\n"
"v_cvt_pk_bf16_f32 v99, v198, v199\n"
"v_cvt_pk_bf16_f32 v100, v148, v149\n"
"v_cvt_pk_bf16_f32 v101, v150, v151\n"
"v_cvt_pk_bf16_f32 v102, v156, v157\n"
"v_cvt_pk_bf16_f32 v103, v158, v159\n"
"v_cvt_pk_bf16_f32 v104, v164, v165\n"
"v_cvt_pk_bf16_f32 v105, v166, v167\n"
"v_cvt_pk_bf16_f32 v106, v172, v173\n"
"v_cvt_pk_bf16_f32 v107, v174, v175\n"
"s_waitcnt vmcnt(19)\n"
"v_dot2_f32_bf16 v244, v108, s70, 0\n"
"v_dot2_f32_bf16 v245, v109, s70, 0\n"
"v_dot2_f32_bf16 v246, v110, s70, 0\n"
"v_dot2_f32_bf16 v247, v111, s70, 0\n"
"s_waitcnt vmcnt(18)\n"
"v_dot2_f32_bf16 v244, v112, s70, v244\n"
"v_dot2_f32_bf16 v245, v113, s70, v245\n"
"v_dot2_f32_bf16 v246, v114, s70, v246\n"
"v_dot2_f32_bf16 v247, v115, s70, v247\n"
"s_waitcnt vmcnt(17)\n"
"v_dot2_f32_bf16 v244, v116, s70, v244\n"
"v_dot2_f32_bf16 v245, v117, s70, v245\n"
"v_dot2_f32_bf16 v246, v118, s70, v246\n"
"v_dot2_f32_bf16 v247, v119, s70, v247\n"
"s_waitcnt vmcnt(16)\n"
"v_dot2_f32_bf16 v244, v120, s70, v244\n"
"v_dot2_f32_bf16 v245, v121, s70, v245\n"
"v_dot2_f32_bf16 v246, v122, s70, v246\n"
"v_dot2_f32_bf16 v247, v123, s70, v247\n"
"s_waitcnt vmcnt(15)\n"
"v_dot2_f32_bf16 v244, v124, s70, v244\n"
"v_dot2_f32_bf16 v245, v125, s70, v245\n"
"v_dot2_f32_bf16 v246, v126, s70, v246\n"
"v_dot2_f32_bf16 v247, v127, s70, v247\n"
"s_waitcnt vmcnt(14)\n"
"v_dot2_f32_bf16 v244, v176, s70, v244\n"
"v_dot2_f32_bf16 v245, v177, s70, v245\n"
"v_dot2_f32_bf16 v246, v178, s70, v246\n"
"v_dot2_f32_bf16 v247, v179, s70, v247\n"
"s_waitcnt vmcnt(13)\n"
"v_dot2_f32_bf16 v244, v180, s70, v244\n"
"v_dot2_f32_bf16 v245, v181, s70, v245\n"
"v_dot2_f32_bf16 v246, v182, s70, v246\n"
"v_dot2_f32_bf16 v247, v183, s70, v247\n"
"s_waitcnt vmcnt(12)\n"
"v_dot2_f32_bf16 v244, v184, s70, v244\n"
"v_dot2_f32_bf16 v245, v185, s70, v245\n"
"v_dot2_f32_bf16 v246, v186, s70, v246\n"
"v_dot2_f32_bf16 v247, v187, s70, v247\n"
"s_waitcnt vmcnt(11)\n"
"v_dot2_f32_bf16 v244, v188, s70, v244\n"
"v_dot2_f32_bf16 v245, v189, s70, v245\n"
"v_dot2_f32_bf16 v246, v190, s70, v246\n"
"v_dot2_f32_bf16 v247, v191, s70, v247\n"
"s_nop 0\n"
"v_add_f32 v244, v244, v245\n"
"s_nop 0\n"
"v_add_f32 v246, v246, v247\n"
"v_add_f32 v244, v244, v246\n"
"s_nop 1\n"
"v_add_f32_dpp v244, v244, v244 quad_perm:[1,0,3,2] row_mask:0xf bank_mask:0xf\n"
"s_nop 1\n"
"v_add_f32_dpp v244, v244, v244 quad_perm:[2,3,0,1] row_mask:0xf bank_mask:0xf\n"
"s_nop 1\n"
"v_add_f32_dpp v244, v244, v244 row_half_mirror row_mask:0xf bank_mask:0xf\n"
"v_mul_f32 v244, 0xbae38e39, v244\n"
"v_cvt_pk_bf16_f32 v248, v244, v244\n"
"v_lshlrev_b32 v248, 16, v248\n"
"v_sub_f32 v227, v244, v248\n"
"s_waitcnt lgkmcnt(0)\n"
"s_barrier\n"
"v_add_u32 v255, 0xffff1900, v255\n"
"ds_write_b128 v255, v[72:75] offset:0\n"
"v_dot2_f32_bf16 v250, v72, v72, 0\n"
"v_dot2_f32_bf16 v251, v73, v73, 0\n"
"v_dot2_f32_bf16 v252, v74, v74, 0\n"
"v_dot2_f32_bf16 v253, v75, v75, 0\n"
"ds_write_b128 v255, v[76:79] offset:8448\n"
"v_dot2_f32_bf16 v250, v76, v76, v250\n"
"v_dot2_f32_bf16 v251, v77, v77, v251\n"
"v_dot2_f32_bf16 v252, v78, v78, v252\n"
"v_dot2_f32_bf16 v253, v79, v79, v253\n"
"ds_write_b128 v255, v[80:83] offset:16896\n"
"v_dot2_f32_bf16 v250, v80, v80, v250\n"
"v_dot2_f32_bf16 v251, v81, v81, v251\n"
"v_dot2_f32_bf16 v252, v82, v82, v252\n"
"v_dot2_f32_bf16 v253, v83, v83, v253\n"
"ds_write_b128 v255, v[84:87] offset:25344\n"
"v_dot2_f32_bf16 v250, v84, v84, v250\n"
"v_dot2_f32_bf16 v251, v85, v85, v251\n"
"v_dot2_f32_bf16 v252, v86, v86, v252\n"
"v_dot2_f32_bf16 v253, v87, v87, v253\n"
"ds_write_b128 v255, v[88:91] offset:33792\n"
"v_dot2_f32_bf16 v250, v88, v88, v250\n"
"v_dot2_f32_bf16 v251, v89, v89, v251\n"
"v_dot2_f32_bf16 v252, v90, v90, v252\n"
"v_dot2_f32_bf16 v253, v91, v91, v253\n"
"ds_write_b128 v255, v[92:95] offset:42240\n"
"v_dot2_f32_bf16 v250, v92, v92, v250\n"
"v_dot2_f32_bf16 v251, v93, v93, v251\n"
"v_dot2_f32_bf16 v252, v94, v94, v252\n"
"v_dot2_f32_bf16 v253, v95, v95, v253\n"
"ds_write_b128 v255, v[96:99] offset:50688\n"
"v_dot2_f32_bf16 v250, v96, v96, v250\n"
"v_dot2_f32_bf16 v251, v97, v97, v251\n"
"v_dot2_f32_bf16 v252, v98, v98, v252\n"
"v_dot2_f32_bf16 v253, v99, v99, v253\n"
"ds_write_b128 v255, v[100:103] offset:59136\n"
"v_dot2_f32_bf16 v250, v100, v100, v250\n"
"v_dot2_f32_bf16 v251, v101, v101, v251\n"
"v_dot2_f32_bf16 v252, v102, v102, v252\n"
"v_dot2_f32_bf16 v253, v103, v103, v253\n"
"v_add_u32 v255, 0xe700, v255\n"
"ds_write_b128 v255, v[104:107] offset:8448\n"
"v_dot2_f32_bf16 v250, v104, v104, v250\n"
"v_dot2_f32_bf16 v251, v105, v105, v251\n"
"v_dot2_f32_bf16 v252, v106, v106, v252\n"
"v_dot2_f32_bf16 v253, v107, v107, v253\n"
"s_nop 0\n"
"v_add_f32 v250, v250, v251\n"
"s_nop 0\n"
"v_add_f32 v252, v252, v253\n"
"v_add_f32 v222, v250, v252\n"
"v_mov_b32 v244, v248\n"
"v_mov_b32 v245, v248\n"
"v_mov_b32 v246, v248\n"
"v_mov_b32 v247, v248\n"
"s_nop 1\n"
"v_mfma_f32_16x16x32_bf16 v[192:195], a[4:7], v[108:111], v[244:247]\n"
"v_mfma_f32_16x16x32_bf16 v[196:199], a[12:15], v[108:111], v[244:247]\n"
"s_mov_b32 exec_lo, 0x55555555\n s_mov_b32 exec_hi, 0x155555\n buffer_load_dword v215, v228, s[32:35], 0 offen offset:0\n s_mov_b64 exec, -1\n"
"v_mfma_f32_16x16x32_bf16 v[148:151], a[4:7], v[112:115], v[244:247]\n"
"v_mfma_f32_16x16x32_bf16 v[156:159], a[12:15], v[112:115], v[244:247]\n"
"s_add_u32 s66, s60, 0x1b0000\n"
"s_add_u32 m0, s59, 0xc600\n"
"s_nop 0\n"
"buffer_load_dwordx4 v207, s[24:27], s66 offen lds\n"
"v_mfma_f32_16x16x32_bf16 v[164:167], a[4:7], v[116:119], v[244:247]\n"
"v_cvt_pk_bf16_f32 v108, v192, v193\n"
"v_cvt_pk_bf16_f32 v109, v194, v195\n"
"v_mfma_f32_16x16x32_bf16 v[172:175], a[12:15], v[116:119], v[244:247]\n"
"s_add_u32 m0, s59, 0xd680\n"
"s_nop 0\n"
"buffer_load_dwordx4 v207, s[28:31], s66 offen lds\n"
"v_cvt_pk_bf16_f32 v110, v196, v197\n"
"v_cvt_pk_bf16_f32 v111, v198, v199\n"
"v_mfma_f32_16x16x32_bf16 v[192:195], a[4:7], v[120:123], v[244:247]\n"
"v_add_u32 v255, 0xffff1900, v255\n"
"ds_write_b128 v255, v[108:111] offset:128\n"
"v_cvt_pk_bf16_f32 v112, v148, v149\n"
"v_cvt_pk_bf16_f32 v113, v150, v151\n"
"v_mfma_f32_16x16x32_bf16 v[196:199], a[12:15], v[120:123], v[244:247]\n"
"s_add_u32 s66, s60, 0x1f8000\n"
"s_add_u32 m0, s59, 0x21000\n"
"s_nop 0\n"
"buffer_load_dwordx4 v207, s[24:27], s66 offen lds\n"
"v_cvt_pk_bf16_f32 v114, v156, v157\n"
"v_cvt_pk_bf16_f32 v115, v158, v159\n"
"v_mfma_f32_16x16x32_bf16 v[148:151], a[4:7], v[124:127], v[244:247]\n"
"ds_write_b128 v255, v[112:115] offset:8576\n"
"v_cvt_pk_bf16_f32 v116, v164, v165\n"
"v_cvt_pk_bf16_f32 v117, v166, v167\n"
"v_mfma_f32_16x16x32_bf16 v[156:159], a[12:15], v[124:127], v[244:247]\n"
"s_add_u32 m0, s59, 0x22080\n"
"s_nop 0\n"
"buffer_load_dwordx4 v207, s[28:31], s66 offen lds\n"
"v_cvt_pk_bf16_f32 v118, v172, v173\n"
"v_cvt_pk_bf16_f32 v119, v174, v175\n"
"v_mfma_f32_16x16x32_bf16 v[164:167], a[4:7], v[176:179], v[244:247]\n"
"ds_write_b128 v255, v[116:119] offset:17024\n"
"v_cvt_pk_bf16_f32 v120, v192, v193\n"
"v_cvt_pk_bf16_f32 v121, v194, v195\n"
"v_mfma_f32_16x16x32_bf16 v[172:175], a[12:15], v[176:179], v[244:247]\n"
"v_cvt_pk_bf16_f32 v122, v196, v197\n"
"v_cvt_pk_bf16_f32 v123, v198, v199\n"
"v_mfma_f32_16x16x32_bf16 v[192:195], a[4:7], v[180:183], v[244:247]\n"
"ds_write_b128 v255, v[120:123] offset:25472\n"
"v_cvt_pk_bf16_f32 v124, v148, v149\n"
"v_cvt_pk_bf16_f32 v125, v150, v151\n"
"v_mfma_f32_16x16x32_bf16 v[196:199], a[12:15], v[180:183], v[244:247]\n"
"v_cvt_pk_bf16_f32 v126, v156, v157\n"
"v_cvt_pk_bf16_f32 v127, v158, v159\n"
"v_mfma_f32_16x16x32_bf16 v[148:151], a[4:7], v[184:187], v[244:247]\n"
"ds_write_b128 v255, v[124:127] offset:33920\n"
"v_cvt_pk_bf16_f32 v176, v164, v165\n"
"v_cvt_pk_bf16_f32 v177, v166, v167\n"
"v_mfma_f32_16x16x32_bf16 v[156:159], a[12:15], v[184:187], v[244:247]\n"
"v_cvt_pk_bf16_f32 v178, v172, v173\n"
"v_cvt_pk_bf16_f32 v179, v174, v175\n"
"v_mfma_f32_16x16x32_bf16 v[164:167], a[4:7], v[188:191], v[244:247]\n"
"ds_write_b128 v255, v[176:179] offset:42368\n"
"v_cvt_pk_bf16_f32 v180, v192, v193\n"
"v_cvt_pk_bf16_f32 v181, v194, v195\n"
"v_mfma_f32_16x16x32_bf16 v[172:175], a[12:15], v[188:191], v[244:247]\n"
"v_cvt_pk_bf16_f32 v182, v196, v197\n"
"v_cvt_pk_bf16_f32 v183, v198, v199\n"
"ds_write_b128 v255, v[180:183] offset:50816\n"
"v_cvt_pk_bf16_f32 v184, v148, v149\n"
"v_cvt_pk_bf16_f32 v185, v150, v151\n"
"v_cvt_pk_bf16_f32 v186, v156, v157\n"
"v_cvt_pk_bf16_f32 v187, v158, v159\n"
"ds_write_b128 v255, v[184:187] offset:59264\n"
"v_cvt_pk_bf16_f32 v188, v164, v165\n"
"v_cvt_pk_bf16_f32 v189, v166, v167\n"
"v_cvt_pk_bf16_f32 v190, v172, v173\n"
"v_cvt_pk_bf16_f32 v191, v174, v175\n"
"v_add_u32 v255, 0xe700, v255\n"
"ds_write_b128 v255, v[188:191] offset:8576\n"
"v_dot2_f32_bf16 v250, v108, v108, 0\n"
"v_dot2_f32_bf16 v251, v109, v109, 0\n"
"v_dot2_f32_bf16 v252, v110, v110, 0\n"
"v_dot2_f32_bf16 v253, v111, v111, 0\n"
"v_dot2_f32_bf16 v250, v112, v112, v250\n"
"v_dot2_f32_bf16 v251, v113, v113, v251\n"
"v_dot2_f32_bf16 v252, v114, v114, v252\n"
"v_dot2_f32_bf16 v253, v115, v115, v253\n"
"v_dot2_f32_bf16 v250, v116, v116, v250\n"
"v_dot2_f32_bf16 v251, v117, v117, v251\n"
"v_dot2_f32_bf16 v252, v118, v118, v252\n"
"v_dot2_f32_bf16 v253, v119, v119, v253\n"
"v_dot2_f32_bf16 v250, v120, v120, v250\n"
"v_dot2_f32_bf16 v251, v121, v121, v251\n"
"v_dot2_f32_bf16 v252, v122, v122, v252\n"
"v_dot2_f32_bf16 v253, v123, v123, v253\n"
"v_dot2_f32_bf16 v250, v124, v124, v250\n"
"v_dot2_f32_bf16 v251, v125, v125, v251\n"
"v_dot2_f32_bf16 v252, v126, v126, v252\n"
"v_dot2_f32_bf16 v253, v127, v127, v253\n"
"v_dot2_f32_bf16 v250, v176, v176, v250\n"
"v_dot2_f32_bf16 v251, v177, v177, v251\n"
"v_dot2_f32_bf16 v252, v178, v178, v252\n"
"v_dot2_f32_bf16 v253, v179, v179, v253\n"
"v_dot2_f32_bf16 v250, v180, v180, v250\n"
"v_dot2_f32_bf16 v251, v181, v181, v251\n"
"v_dot2_f32_bf16 v252, v182, v182, v252\n"
"v_dot2_f32_bf16 v253, v183, v183, v253\n"
"v_dot2_f32_bf16 v250, v184, v184, v250\n"
"v_dot2_f32_bf16 v251, v185, v185, v251\n"
"v_dot2_f32_bf16 v252, v186, v186, v252\n"
"v_dot2_f32_bf16 v253, v187, v187, v253\n"
"v_dot2_f32_bf16 v250, v188, v188, v250\n"
"v_dot2_f32_bf16 v251, v189, v189, v251\n"
"v_dot2_f32_bf16 v252, v190, v190, v252\n"
"v_dot2_f32_bf16 v253, v191, v191, v253\n"
"s_nop 0\n"
"v_add_f32 v250, v250, v251\n"
"s_nop 0\n"
"v_add_f32 v252, v252, v253\n"
"v_add_f32 v223, v250, v252\n"
"s_mov_b32 s9, 0x3ae38e39\n"
"s_mov_b64 exec, 0\n s_cmp_eq_u32 s53, 1\n s_cbranch_scc1 L_st1_%=\n s_cmp_eq_u32 s53, 2\n s_cbranch_scc1 L_st2_%=\n s_cmp_eq_u32 s53, 3\n s_cbranch_scc1 L_st3_%=\n s_branch L_st0_%=\n L_ret_%=:\n s_mov_b64 exec, -1\n"
"s_waitcnt lgkmcnt(0)\n"
"s_barrier\n"
"s_branch L_st0_%=\n .p2align 12\n"
"L_st0_%=:\n s_cbranch_execz L_st4_%=\n"
"v_add_u32 v203, 0xffff1900, v203\n"
"v_add_u32 v204, 0xffff1900, v204\n"
"ds_read_b64_tr_b16 v[176:177], v200 offset:0\n"
"ds_read_b64_tr_b16 v[178:179], v200 offset:256\n"
"ds_read_b128 a[4:7], v203 offset:0\n"
"ds_read_b128 a[12:15], v204 offset:0\n"
"ds_read_b64_tr_b16 v[180:181], v200 offset:32\n"
"ds_read_b64_tr_b16 v[182:183], v200 offset:288\n"
"ds_read_b64_tr_b16 v[184:185], v200 offset:4224\n"
"ds_read_b64_tr_b16 v[186:187], v200 offset:4480\n"
"s_waitcnt lgkmcnt(6)\n"
"v_mfma_f32_16x16x32_bf16 v[0:3], v[176:179], a[0:3], 0\n"
"s_add_u32 s66, s60, 0x240000\n"
"ds_read_b64_tr_b16 v[188:189], v200 offset:4256\n"
"v_add_u32 v122, 0x27dc0, v234\n"
"v_mfma_f32_16x16x32_bf16 v[8:11], v[176:179], a[8:11], 0\n"
"s_waitcnt lgkmcnt(6)\n"
"ds_read_b64_tr_b16 v[190:191], v200 offset:4512\n"
"v_lshrrev_b32 v235, 3, v233\n"
"v_mfma_f32_16x16x32_bf16 v[4:7], v[176:179], a[4:7], 0\n"
"s_waitcnt lgkmcnt(6)\n"
"ds_read_b64_tr_b16 v[192:193], v200 offset:8448\n"
"v_add_f32_dpp v220, v220, v220 quad_perm:[1,0,3,2] row_mask:0xf bank_mask:0xf\n"
"v_mfma_f32_16x16x32_bf16 v[12:15], v[176:179], a[12:15], 0\n"
"s_waitcnt lgkmcnt(3)\n"
"ds_read_b64_tr_b16 v[194:195], v200 offset:8704\n"
"v_add_f32_dpp v220, v220, v220 quad_perm:[2,3,0,1] row_mask:0xf bank_mask:0xf\n"
"v_mfma_f32_16x16x32_bf16 v[28:31], v[180:183], a[12:15], 0\n"
"s_add_u32 m0, s59, 0\n"
"ds_read_b128 a[20:23], v203 offset:1088\n"
"v_add_f32_dpp v220, v220, v220 row_half_mirror row_mask:0xf bank_mask:0xf\n"
"v_mfma_f32_16x16x32_bf16 v[20:23], v[180:183], a[4:7], 0\n"
"s_lshl_b32 s67, s54, 1\n"
"ds_read_b128 a[28:31], v204 offset:1088\n"
"v_mul_f32 v224, v224, v224\n"
"v_mfma_f32_16x16x32_bf16 v[24:27], v[180:183], a[8:11], 0\n"
"s_add_u32 s67, s67, s55\n"
"ds_read_b64_tr_b16 v[196:197], v200 offset:8480\n"
"v_fma_f32 v220, v220, s9, -v224\n"
"v_mfma_f32_16x16x32_bf16 v[16:19], v[180:183], a[0:3], 0\n"
"s_lshl_b32 s67, s67, 7\n"
"ds_read_b64_tr_b16 v[198:199], v200 offset:8736\n"
"v_max_f32 v220, 0, v220\n"
"v_mfma_f32_16x16x32_bf16 v[32:35], v[184:187], a[0:3], 0\n"
"s_xor_b32 s68, s67, 0x80\n"
"ds_read_b64_tr_b16 v[176:177], v200 offset:12672\n"
"v_cvt_pk_bf16_f32 v220, v220, v220\n"
"v_mfma_f32_16x16x32_bf16 v[40:43], v[184:187], a[8:11], 0\n"
"s_add_u32 s67, s67, 0x27e00\n"
"ds_read_b64_tr_b16 v[178:179], v200 offset:12928\n"
"v_lshlrev_b32 v220, 16, v220\n"
"v_mfma_f32_16x16x32_bf16 v[36:39], v[184:187], a[4:7], 0\n"
"s_lshl_b32 s69, s53, 4\n"
"ds_read_b64_tr_b16 v[180:181], v200 offset:12704\n"
"v_add_u32 v120, s67, v234\n"
"v_mfma_f32_16x16x32_bf16 v[44:47], v[184:187], a[12:15], 0\n"
"s_waitcnt lgkmcnt(5)\n"
"ds_read_b64_tr_b16 v[182:183], v200 offset:12960\n"
"v_lshl_add_u32 v235, v235, 2, s67\n"
"v_mfma_f32_16x16x32_bf16 v[60:63], v[188:191], a[12:15], 0\n"
"s_add_u32 s69, s69, 0x27dc0\n"
"ds_read_b128 a[36:39], v203 offset:8448\n"
"v_add_f32 v220, 0x3727c5ac, v220\n"
"v_mfma_f32_16x16x32_bf16 v[52:55], v[188:191], a[4:7], 0\n"
"s_add_u32 s68, s68, 0x27e00\n"
"ds_read_b128 a[44:47], v204 offset:8448\n"
"v_cvt_pk_bf16_f32 v220, v220, v220\n"
"v_mfma_f32_16x16x32_bf16 v[56:59], v[188:191], a[8:11], 0\n"
"s_lshl_b32 s15, s54, 1\n"
"ds_read_b128 a[52:55], v203 offset:9536\n"
"v_add_u32 v121, s68, v234\n"
"v_mfma_f32_16x16x32_bf16 v[48:51], v[188:191], a[0:3], 0\n"
"s_add_u32 s15, s15, s55\n"
"ds_read_b128 a[60:63], v204 offset:9536\n"
"v_lshlrev_b32 v220, 16, v220\n"
"v_mfma_f32_16x16x32_bf16 v[0:3], v[192:195], a[16:19], v[0:3]\n"
"s_lshl_b32 s67, s57, 4\n"
"v_sqrt_f32 v220, v220\n"
"v_mfma_f32_16x16x32_bf16 v[8:11], v[192:195], a[24:27], v[8:11]\n"
"s_add_u32 s67, s67, s56\n"
"ds_read_b128 a[68:71], v203 offset:16896\n"
"v_cvt_pk_bf16_f32 v220, v220, v220\n"
"v_mfma_f32_16x16x32_bf16 v[4:7], v[192:195], a[20:23], v[4:7]\n"
"s_mul_i32 s68, s67, 0x2000\n"
"ds_read_b128 a[76:79], v204 offset:16896\n"
"v_lshlrev_b32 v220, 16, v220\n"
"v_mfma_f32_16x16x32_bf16 v[12:15], v[192:195], a[28:31], v[12:15]\n"
"s_waitcnt lgkmcnt(4)\n"
"ds_write_b32 v235, v220 offset:0\n"
"v_add_f32_dpp v221, v221, v221 quad_perm:[1,0,3,2] row_mask:0xf bank_mask:0xf\n"
"v_mfma_f32_16x16x32_bf16 v[28:31], v[196:199], a[28:31], v[28:31]\n"
"s_add_u32 s68, s68, 0x1900000\n"
"s_xor_b32 s67, s67, 1\n"
"buffer_load_dwordx4 v[148:151], v240, s[20:23], 0 offen offset:0\n"
"v_mfma_f32_16x16x32_bf16 v[20:23], v[196:199], a[20:23], v[20:23]\n"
"s_waitcnt vmcnt(12)\n"
"ds_read_b128 a[84:87], v203 offset:17984\n"
"v_add_f32_dpp v221, v221, v221 quad_perm:[2,3,0,1] row_mask:0xf bank_mask:0xf\n"
"v_mfma_f32_16x16x32_bf16 v[24:27], v[196:199], a[24:27], v[24:27]\n"
"s_barrier\n"
"ds_read_b128 a[92:95], v204 offset:17984\n"
"v_add_f32_dpp v221, v221, v221 row_half_mirror row_mask:0xf bank_mask:0xf\n"
"v_mfma_f32_16x16x32_bf16 v[16:19], v[196:199], a[16:19], v[16:19]\n"
"s_lshl_b32 s67, s67, 1\n"
"ds_read_b64_tr_b16 v[184:185], v200 offset:16896\n"
"v_mul_f32 v225, v225, v225\n"
"v_mfma_f32_16x16x32_bf16 v[32:35], v[176:179], a[16:19], v[32:35]\n"
"s_add_u32 s67, s67, s54\n"
"s_mul_i32 s17, s67, 0x2100\n"
"buffer_load_dwordx4 v[156:159], v240, s[20:23], 0 offen offset:256\n"
"v_mfma_f32_16x16x32_bf16 v[40:43], v[176:179], a[24:27], v[40:43]\n"
"s_lshl_b32 s67, s55, 3\n"
"ds_read_b64_tr_b16 v[186:187], v200 offset:17152\n"
"v_fma_f32 v221, v221, s9, -v225\n"
"v_mfma_f32_16x16x32_bf16 v[36:39], v[176:179], a[20:23], v[36:39]\n"
"s_mov_b32 s72, s18\n"
"ds_read_b64_tr_b16 v[188:189], v200 offset:16928\n"
"v_max_f32 v221, 0, v221\n"
"v_mfma_f32_16x16x32_bf16 v[44:47], v[176:179], a[28:31], v[44:47]\n"
"s_mov_b32 s8, 0x40000\n"
"ds_read_b64_tr_b16 v[190:191], v200 offset:17184\n"
"v_cvt_pk_bf16_f32 v221, v221, v221\n"
"v_mfma_f32_16x16x32_bf16 v[60:63], v[180:183], a[28:31], v[60:63]\n"
"buffer_load_dwordx4 v[164:167], v240, s[20:23], 0 offen offset:512\n"
"v_mfma_f32_16x16x32_bf16 v[52:55], v[180:183], a[20:23], v[52:55]\n"
"ds_read_b64_tr_b16 v[192:193], v200 offset:21120\n"
"v_lshlrev_b32 v221, 16, v221\n"
"v_mfma_f32_16x16x32_bf16 v[56:59], v[180:183], a[24:27], v[56:59]\n"
"buffer_load_dwordx4 v[172:175], v240, s[20:23], 0 offen offset:768\n"
"v_mfma_f32_16x16x32_bf16 v[48:51], v[180:183], a[16:19], v[48:51]\n"
"s_waitcnt lgkmcnt(3)\n"
"ds_read_b64_tr_b16 v[194:195], v200 offset:21376\n"
"v_add_f32 v221, 0x3727c5ac, v221\n"
"v_mfma_f32_16x16x32_bf16 v[0:3], v[184:187], a[32:35], v[0:3]\n"
"ds_read_b64_tr_b16 v[196:197], v200 offset:21152\n"
"v_cvt_pk_bf16_f32 v221, v221, v221\n"
"v_mfma_f32_16x16x32_bf16 v[8:11], v[184:187], a[40:43], v[8:11]\n"
"buffer_load_dwordx2 v[140:141], v241, s[20:23], 0 offen offset:1024\n"
"v_mfma_f32_16x16x32_bf16 v[4:7], v[184:187], a[36:39], v[4:7]\n"
"ds_read_b64_tr_b16 v[198:199], v200 offset:21408\n"
"v_lshlrev_b32 v221, 16, v221\n"
"v_mfma_f32_16x16x32_bf16 v[12:15], v[184:187], a[44:47], v[12:15]\n"
"s_waitcnt lgkmcnt(3)\n"
"v_sqrt_f32 v221, v221\n"
"v_mfma_f32_16x16x32_bf16 v[28:31], v[188:191], a[44:47], v[28:31]\n"
"ds_read_b64_tr_b16 v[176:177], v200 offset:25344\n"
"v_cvt_pk_bf16_f32 v221, v221, v221\n"
"v_mfma_f32_16x16x32_bf16 v[20:23], v[188:191], a[36:39], v[20:23]\n"
"ds_read_b64_tr_b16 v[178:179], v200 offset:25600\n"
"v_lshlrev_b32 v221, 16, v221\n"
"v_mfma_f32_16x16x32_bf16 v[24:27], v[188:191], a[40:43], v[24:27]\n"
"ds_write_b32 v235, v221 offset:32\n"
"v_add_f32_dpp v222, v222, v222 quad_perm:[1,0,3,2] row_mask:0xf bank_mask:0xf\n"
"v_mfma_f32_16x16x32_bf16 v[16:19], v[188:191], a[32:35], v[16:19]\n"
"s_waitcnt lgkmcnt(4)\n"
"ds_read_b64_tr_b16 v[180:181], v200 offset:25376\n"
"v_add_f32_dpp v222, v222, v222 quad_perm:[2,3,0,1] row_mask:0xf bank_mask:0xf\n"
"v_mfma_f32_16x16x32_bf16 v[32:35], v[192:195], a[32:35], v[32:35]\n"
"ds_read_b64_tr_b16 v[182:183], v200 offset:25632\n"
"v_add_f32_dpp v222, v222, v222 row_half_mirror row_mask:0xf bank_mask:0xf\n"
"v_mfma_f32_16x16x32_bf16 v[40:43], v[192:195], a[40:43], v[40:43]\n"
"ds_read_b64_tr_b16 v[184:185], v200 offset:29568\n"
"v_mul_f32 v226, v226, v226\n"
"v_mfma_f32_16x16x32_bf16 v[36:39], v[192:195], a[36:39], v[36:39]\n"
"buffer_load_dwordx4 v207, s[24:27], s66 offen lds\n"
"v_mfma_f32_16x16x32_bf16 v[44:47], v[192:195], a[44:47], v[44:47]\n"
"s_waitcnt lgkmcnt(4)\n"
"s_add_u32 m0, s59, 0x1080\n"
"ds_read_b64_tr_b16 v[186:187], v200 offset:29824\n"
"v_fma_f32 v222, v222, s9, -v226\n"
"v_mfma_f32_16x16x32_bf16 v[60:63], v[196:199], a[44:47], v[60:63]\n"
"ds_read_b64_tr_b16 v[188:189], v200 offset:29600\n"
"v_max_f32 v222, 0, v222\n"
"v_mfma_f32_16x16x32_bf16 v[52:55], v[196:199], a[36:39], v[52:55]\n"
"ds_read_b64_tr_b16 v[190:191], v200 offset:29856\n"
"v_cvt_pk_bf16_f32 v222, v222, v222\n"
"v_mfma_f32_16x16x32_bf16 v[56:59], v[196:199], a[40:43], v[56:59]\n"
"ds_read_b128 a[100:103], v203 offset:25344\n"
"v_lshlrev_b32 v222, 16, v222\n"
"v_mfma_f32_16x16x32_bf16 v[48:51], v[196:199], a[32:35], v[48:51]\n"
"ds_read_b128 a[108:111], v204 offset:25344\n"
"v_add_f32 v222, 0x3727c5ac, v222\n"
"v_mfma_f32_16x16x32_bf16 v[0:3], v[176:179], a[48:51], v[0:3]\n"
"buffer_load_dwordx4 v207, s[28:31], s66 offen lds\n"
"v_mfma_f32_16x16x32_bf16 v[8:11], v[176:179], a[56:59], v[8:11]\n"
"s_add_u32 s66, s60, 0x288000\n"
"s_add_u32 m0, s59, 0x2100\n"
"ds_read_b128 a[116:119], v203 offset:26432\n"
"v_cvt_pk_bf16_f32 v222, v222, v222\n"
"v_mfma_f32_16x16x32_bf16 v[4:7], v[176:179], a[52:55], v[4:7]\n"
"ds_read_b128 a[124:127], v204 offset:26432\n"
"v_lshlrev_b32 v222, 16, v222\n"
"v_mfma_f32_16x16x32_bf16 v[12:15], v[176:179], a[60:63], v[12:15]\n"
"s_waitcnt lgkmcnt(4)\n"
"v_sqrt_f32 v222, v222\n"
"v_mfma_f32_16x16x32_bf16 v[28:31], v[180:183], a[60:63], v[28:31]\n"
"ds_read_b128 a[132:135], v203 offset:33792\n"
"v_cvt_pk_bf16_f32 v222, v222, v222\n"
"v_mfma_f32_16x16x32_bf16 v[20:23], v[180:183], a[52:55], v[20:23]\n"
"s_waitcnt vmcnt(12)\n"
"ds_read_b128 a[140:143], v204 offset:33792\n"
"v_lshlrev_b32 v222, 16, v222\n"
"v_mfma_f32_16x16x32_bf16 v[24:27], v[180:183], a[56:59], v[24:27]\n"
"s_barrier\n"
"ds_write_b32 v235, v222 offset:64\n"
"v_add_f32_dpp v223, v223, v223 quad_perm:[1,0,3,2] row_mask:0xf bank_mask:0xf\n"
"v_mfma_f32_16x16x32_bf16 v[16:19], v[180:183], a[48:51], v[16:19]\n"
"buffer_load_dwordx4 v207, s[24:27], s66 offen lds\n"
"v_mfma_f32_16x16x32_bf16 v[32:35], v[184:187], a[48:51], v[32:35]\n"
"s_add_u32 m0, s59, 0x3180\n"
"ds_read_b64_tr_b16 v[192:193], v200 offset:33792\n"
"v_add_f32_dpp v223, v223, v223 quad_perm:[2,3,0,1] row_mask:0xf bank_mask:0xf\n"
"v_mfma_f32_16x16x32_bf16 v[40:43], v[184:187], a[56:59], v[40:43]\n"
"ds_read_b64_tr_b16 v[194:195], v200 offset:34048\n"
"v_add_f32_dpp v223, v223, v223 row_half_mirror row_mask:0xf bank_mask:0xf\n"
"v_mfma_f32_16x16x32_bf16 v[36:39], v[184:187], a[52:55], v[36:39]\n"
"ds_read_b64_tr_b16 v[196:197], v200 offset:33824\n"
"v_mul_f32 v227, v227, v227\n"
"v_mfma_f32_16x16x32_bf16 v[44:47], v[184:187], a[60:63], v[44:47]\n"
"buffer_load_dwordx4 v207, s[28:31], s66 offen lds\n"
"v_mfma_f32_16x16x32_bf16 v[60:63], v[188:191], a[60:63], v[60:63]\n"
"s_add_u32 s66, s60, 0x2d0000\n"
"s_add_u32 m0, s59, 0x4200\n"
"ds_read_b64_tr_b16 v[198:199], v200 offset:34080\n"
"v_fma_f32 v223, v223, s9, -v227\n"
"v_mfma_f32_16x16x32_bf16 v[52:55], v[188:191], a[52:55], v[52:55]\n"
"ds_read_b64_tr_b16 v[176:177], v200 offset:38016\n"
"v_max_f32 v223, 0, v223\n"
"v_mfma_f32_16x16x32_bf16 v[56:59], v[188:191], a[56:59], v[56:59]\n"
"ds_read_b64_tr_b16 v[178:179], v200 offset:38272\n"
"v_cvt_pk_bf16_f32 v223, v223, v223\n"
"v_mfma_f32_16x16x32_bf16 v[48:51], v[188:191], a[48:51], v[48:51]\n"
"s_waitcnt lgkmcnt(4)\n"
"ds_read_b64_tr_b16 v[180:181], v200 offset:38048\n"
"v_lshlrev_b32 v223, 16, v223\n"
"v_mfma_f32_16x16x32_bf16 v[0:3], v[192:195], a[64:67], v[0:3]\n"
"s_mov_b64 exec, 1\n buffer_atomic_add v236, v237, s[44:47], s18 offen sc0 sc1\n s_mov_b64 exec, -1\n"
"v_mfma_f32_16x16x32_bf16 v[8:11], v[192:195], a[72:75], v[8:11]\n"
"ds_read_b64_tr_b16 v[182:183], v200 offset:38304\n"
"v_add_f32 v223, 0x3727c5ac, v223\n"
"v_mfma_f32_16x16x32_bf16 v[4:7], v[192:195], a[68:71], v[4:7]\n"
"buffer_load_dwordx4 v207, s[24:27], s66 offen lds\n"
"v_mfma_f32_16x16x32_bf16 v[12:15], v[192:195], a[76:79], v[12:15]\n"
"s_waitcnt lgkmcnt(3)\n"
"s_add_u32 m0, s59, 0x5280\n"
"ds_read_b64_tr_b16 v[184:185], v200 offset:42240\n"
"v_cvt_pk_bf16_f32 v223, v223, v223\n"
"v_mfma_f32_16x16x32_bf16 v[28:31], v[196:199], a[76:79], v[28:31]\n"
"ds_read_b64_tr_b16 v[186:187], v200 offset:42496\n"
"v_lshlrev_b32 v223, 16, v223\n"
"v_mfma_f32_16x16x32_bf16 v[20:23], v[196:199], a[68:71], v[20:23]\n"
"v_sqrt_f32 v223, v223\n"
"v_mfma_f32_16x16x32_bf16 v[24:27], v[196:199], a[72:75], v[24:27]\n"
"ds_read_b64_tr_b16 v[188:189], v200 offset:42272\n"
"v_cvt_pk_bf16_f32 v223, v223, v223\n"
"v_mfma_f32_16x16x32_bf16 v[16:19], v[196:199], a[64:67], v[16:19]\n"
"s_waitcnt lgkmcnt(3)\n"
"ds_read_b64_tr_b16 v[190:191], v200 offset:42528\n"
"v_lshlrev_b32 v223, 16, v223\n"
"v_mfma_f32_16x16x32_bf16 v[32:35], v[176:179], a[64:67], v[32:35]\n"
"buffer_load_dwordx4 v207, s[28:31], s66 offen lds\n"
"v_mfma_f32_16x16x32_bf16 v[40:43], v[176:179], a[72:75], v[40:43]\n"
"s_add_u32 s66, s60, 0x318000\n"
"s_add_u32 m0, s59, 0xe700\n"
"ds_write_b32 v235, v223 offset:96\n"
"v_lshrrev_b32 v142, 4, v233\n"
"v_mfma_f32_16x16x32_bf16 v[36:39], v[176:179], a[68:71], v[36:39]\n"
"ds_read_b128 a[148:151], v203 offset:34880\n"
"v_cmp_gt_u32 s[10:11], 2, v142\n"
"v_mfma_f32_16x16x32_bf16 v[44:47], v[176:179], a[76:79], v[44:47]\n"
"ds_read_b128 a[156:159], v204 offset:34880\n"
"v_mul_u32_u24 v142, 0x1080, v142\n"
"v_mfma_f32_16x16x32_bf16 v[60:63], v[180:183], a[76:79], v[60:63]\n"
"ds_read_b128 a[164:167], v203 offset:42240\n"
"v_lshl_add_u32 v142, v234, 1, v142\n"
"v_mfma_f32_16x16x32_bf16 v[52:55], v[180:183], a[68:71], v[52:55]\n"
"ds_read_b128 a[172:175], v204 offset:42240\n"
"v_mov_b32 v119, 0x40000000\n"
"v_mfma_f32_16x16x32_bf16 v[56:59], v[180:183], a[72:75], v[56:59]\n"
"buffer_load_dwordx4 v207, s[24:27], s66 offen lds\n"
"v_mfma_f32_16x16x32_bf16 v[48:51], v[180:183], a[64:67], v[48:51]\n"
"s_waitcnt lgkmcnt(4)\n"
"s_add_u32 m0, s59, 0xf780\n"
"ds_read_b128 a[180:183], v203 offset:43328\n"
"v_cndmask_b32 v142, v119, v142, s[10:11]\n"
"v_mfma_f32_16x16x32_bf16 v[0:3], v[184:187], a[80:83], v[0:3]\n"
"ds_read_b128 a[188:191], v204 offset:43328\n"
"v_add_u32 v214, 0xf780, v214\n"
"v_mfma_f32_16x16x32_bf16 v[8:11], v[184:187], a[88:91], v[8:11]\n"
"ds_read_b64_tr_b16 v[192:193], v200 offset:46464\n"
"v_add_u32 v216, 0xf780, v216\n"
"v_mfma_f32_16x16x32_bf16 v[4:7], v[184:187], a[84:87], v[4:7]\n"
"ds_read_b64_tr_b16 v[194:195], v200 offset:46720\n"
"v_add_u32 v217, 0xf780, v217\n"
"v_mfma_f32_16x16x32_bf16 v[12:15], v[184:187], a[92:95], v[12:15]\n"
"ds_read_b64_tr_b16 v[196:197], v200 offset:46496\n"
"v_mfma_f32_16x16x32_bf16 v[28:31], v[188:191], a[92:95], v[28:31]\n"
"buffer_load_dwordx4 v207, s[28:31], s66 offen lds\n"
"v_mfma_f32_16x16x32_bf16 v[20:23], v[188:191], a[84:87], v[20:23]\n"
"s_waitcnt vmcnt(14) lgkmcnt(3)\n"
"s_add_u32 s66, s60, 0x360000\n"
"ds_read_b64_tr_b16 v[198:199], v200 offset:46752\n"
"v_mfma_f32_16x16x32_bf16 v[24:27], v[188:191], a[88:91], v[24:27]\n"
"s_barrier\n"
"s_add_u32 m0, s59, 0x10800\n"
"ds_read_b128 a[196:199], v203 offset:50688\n"
"v_mfma_f32_16x16x32_bf16 v[16:19], v[188:191], a[80:83], v[16:19]\n"
"s_waitcnt lgkmcnt(3)\n"
"ds_read_b64_tr_b16 v[176:177], v200 offset:50688\n"
"v_mfma_f32_16x16x32_bf16 v[32:35], v[192:195], a[80:83], v[32:35]\n"
"ds_read_b64_tr_b16 v[178:179], v200 offset:50944\n"
"v_mfma_f32_16x16x32_bf16 v[40:43], v[192:195], a[88:91], v[40:43]\n"
"buffer_load_dwordx4 v207, s[24:27], s66 offen lds\n"
"v_mfma_f32_16x16x32_bf16 v[36:39], v[192:195], a[84:87], v[36:39]\n"
"s_waitcnt vmcnt(14)\n"
"s_add_u32 m0, s59, 0x11880\n"
"ds_read_b64_tr_b16 v[180:181], v200 offset:50720\n"
"v_lshlrev_b32 v64, 16, v148\n"
"v_mfma_f32_16x16x32_bf16 v[44:47], v[192:195], a[92:95], v[44:47]\n"
"s_waitcnt lgkmcnt(4)\n"
"ds_read_b64_tr_b16 v[182:183], v200 offset:50976\n"
"v_and_b32 v65, 0xffff0000, v148\n"
"v_mfma_f32_16x16x32_bf16 v[60:63], v[196:199], a[92:95], v[60:63]\n"
"ds_read_b64_tr_b16 v[184:185], v200 offset:54912\n"
"v_lshlrev_b32 v66, 16, v149\n"
"v_mfma_f32_16x16x32_bf16 v[52:55], v[196:199], a[84:87], v[52:55]\n"
"ds_read_b64_tr_b16 v[186:187], v200 offset:55168\n"
"v_and_b32 v67, 0xffff0000, v149\n"
"v_mfma_f32_16x16x32_bf16 v[56:59], v[196:199], a[88:91], v[56:59]\n"
"buffer_load_dwordx4 v207, s[28:31], s66 offen lds\n"
"v_mfma_f32_16x16x32_bf16 v[48:51], v[196:199], a[80:83], v[48:51]\n"
"s_waitcnt lgkmcnt(4)\n"
"ds_read_b64_tr_b16 v[188:189], v200 offset:54944\n"
"v_lshlrev_b32 v68, 16, v150\n"
"v_mfma_f32_16x16x32_bf16 v[0:3], v[176:179], a[96:99], v[0:3]\n"
"s_add_u32 s66, s60, 0x3a8000\n"
"ds_read_b64_tr_b16 v[190:191], v200 offset:55200\n"
"v_and_b32 v69, 0xffff0000, v150\n"
"v_mfma_f32_16x16x32_bf16 v[8:11], v[176:179], a[104:107], v[8:11]\n"
"s_add_u32 m0, s59, 0x12900\n"
"ds_read_b64_tr_b16 v[192:193], v202 offset:16896\n"
"v_lshlrev_b32 v70, 16, v151\n"
"v_mfma_f32_16x16x32_bf16 v[4:7], v[176:179], a[100:103], v[4:7]\n"
"ds_read_b64_tr_b16 v[194:195], v202 offset:17152\n"
"v_and_b32 v71, 0xffff0000, v151\n"
"v_mfma_f32_16x16x32_bf16 v[12:15], v[176:179], a[108:111], v[12:15]\n"
"s_waitcnt lgkmcnt(4)\n"
"buffer_load_dwordx4 v207, s[24:27], s66 offen lds\n"
"v_mfma_f32_16x16x32_bf16 v[28:31], v[180:183], a[108:111], v[28:31]\n"
"s_waitcnt vmcnt(15)\n"
"s_add_u32 m0, s59, 0x13980\n"
"ds_read_b64_tr_b16 v[196:197], v202 offset:16928\n"
"v_lshlrev_b32 v72, 16, v156\n"
"v_mfma_f32_16x16x32_bf16 v[20:23], v[180:183], a[100:103], v[20:23]\n"
"ds_read_b64_tr_b16 v[198:199], v202 offset:17184\n"
"v_and_b32 v73, 0xffff0000, v156\n"
"v_mfma_f32_16x16x32_bf16 v[24:27], v[180:183], a[104:107], v[24:27]\n"
"ds_read_b128 a[204:207], v204 offset:50688\n"
"v_lshlrev_b32 v74, 16, v157\n"
"v_mfma_f32_16x16x32_bf16 v[16:19], v[180:183], a[96:99], v[16:19]\n"
"ds_read_b128 a[212:215], v203 offset:51776\n"
"v_and_b32 v75, 0xffff0000, v157\n"
"v_mfma_f32_16x16x32_bf16 v[32:35], v[184:187], a[96:99], v[32:35]\n"
"ds_read_b128 a[220:223], v204 offset:51776\n"
"v_lshlrev_b32 v76, 16, v158\n"
"v_mfma_f32_16x16x32_bf16 v[40:43], v[184:187], a[104:107], v[40:43]\n"
"s_mov_b64 exec, 0x1ff\n buffer_load_dword v215, v209, s[24:27], 0 offen offset:128\n s_mov_b64 exec, -1\n"
"v_mfma_f32_16x16x32_bf16 v[36:39], v[184:187], a[100:103], v[36:39]\n"
"ds_read_b128 a[228:231], v203 offset:59136\n"
"v_and_b32 v77, 0xffff0000, v158\n"
"v_mfma_f32_16x16x32_bf16 v[44:47], v[184:187], a[108:111], v[44:47]\n"
"s_waitcnt lgkmcnt(4)\n"
"ds_read_b128 a[236:239], v204 offset:59136\n"
"v_lshlrev_b32 v78, 16, v159\n"
"v_mfma_f32_16x16x32_bf16 v[60:63], v[188:191], a[108:111], v[60:63]\n"
"ds_read_b128 a[244:247], v203 offset:60224\n"
"v_add_u32 v203, 0xe700, v203\n"
"v_mfma_f32_16x16x32_bf16 v[52:55], v[188:191], a[100:103], v[52:55]\n"
"s_mov_b64 exec, 0x1ff\n buffer_load_dword v215, v209, s[28:31], 0 offen offset:128\n s_mov_b64 exec, -1\n"
"v_mfma_f32_16x16x32_bf16 v[56:59], v[188:191], a[104:107], v[56:59]\n"
"ds_read_b128 a[252:255], v204 offset:60224\n"
"v_add_u32 v204, 0xe700, v204\n"
"v_mfma_f32_16x16x32_bf16 v[48:51], v[188:191], a[96:99], v[48:51]\n"
"ds_read_b64_tr_b16 v[176:177], v202 offset:21120\n"
"v_and_b32 v79, 0xffff0000, v159\n"
"v_mfma_f32_16x16x32_bf16 v[0:3], v[192:195], a[112:115], v[0:3]\n"
"s_waitcnt vmcnt(16)\n"
"ds_read_b64_tr_b16 v[178:179], v202 offset:21376\n"
"v_lshlrev_b32 v80, 16, v164\n"
"v_mfma_f32_16x16x32_bf16 v[8:11], v[192:195], a[120:123], v[8:11]\n"
"ds_read_b64_tr_b16 v[180:181], v202 offset:21152\n"
"v_and_b32 v81, 0xffff0000, v164\n"
"v_mfma_f32_16x16x32_bf16 v[4:7], v[192:195], a[116:119], v[4:7]\n"
"ds_read_b64_tr_b16 v[182:183], v202 offset:21408\n"
"v_lshlrev_b32 v82, 16, v165\n"
"v_mfma_f32_16x16x32_bf16 v[12:15], v[192:195], a[124:127], v[12:15]\n"
"buffer_load_dwordx4 v207, s[28:31], s66 offen lds\n"
"v_mfma_f32_16x16x32_bf16 v[28:31], v[196:199], a[124:127], v[28:31]\n"
"s_add_u32 s66, s60, 0x3f0000\n"
"s_add_u32 m0, s59, 0x6300\n"
"ds_read_b128 v[148:151], v203 offset:8448\n"
"v_and_b32 v83, 0xffff0000, v165\n"
"v_mfma_f32_16x16x32_bf16 v[20:23], v[196:199], a[116:119], v[20:23]\n"
"s_waitcnt vmcnt(11) lgkmcnt(4)\n"
"ds_read_b128 v[156:159], v204 offset:8448\n"
"v_lshlrev_b32 v84, 16, v166\n"
"v_mfma_f32_16x16x32_bf16 v[24:27], v[196:199], a[120:123], v[24:27]\n"
"s_barrier\n"
"v_and_b32 v85, 0xffff0000, v166\n"
"v_lshlrev_b32 v86, 16, v167\n"
"v_mfma_f32_16x16x32_bf16 v[16:19], v[196:199], a[112:115], v[16:19]\n"
"s_waitcnt lgkmcnt(3)\n"
"ds_read_b64_tr_b16 v[184:185], v200 offset:0\n"
"v_and_b32 v87, 0xffff0000, v167\n"
"v_mfma_f32_16x16x32_bf16 v[32:35], v[176:179], a[112:115], v[32:35]\n"
"ds_read_b64_tr_b16 v[186:187], v200 offset:256\n"
"v_lshlrev_b32 v88, 16, v172\n"
"v_mfma_f32_16x16x32_bf16 v[40:43], v[176:179], a[120:123], v[40:43]\n"
"buffer_load_dwordx4 v207, s[24:27], s66 offen lds\n"
"v_mfma_f32_16x16x32_bf16 v[36:39], v[176:179], a[116:119], v[36:39]\n"
"s_add_u32 m0, s59, 0x7380\n"
"ds_read_b64_tr_b16 v[188:189], v200 offset:32\n"
"v_and_b32 v89, 0xffff0000, v172\n"
"v_mfma_f32_16x16x32_bf16 v[44:47], v[176:179], a[124:127], v[44:47]\n"
"s_waitcnt lgkmcnt(3)\n"
"ds_read_b64_tr_b16 v[190:191], v200 offset:288\n"
"v_lshlrev_b32 v90, 16, v173\n"
"v_mfma_f32_16x16x32_bf16 v[60:63], v[180:183], a[124:127], v[60:63]\n"
"ds_read_b64_tr_b16 v[192:193], v200 offset:4224\n"
"v_and_b32 v91, 0xffff0000, v173\n"
"v_mfma_f32_16x16x32_bf16 v[52:55], v[180:183], a[116:119], v[52:55]\n"
"ds_read_b64_tr_b16 v[194:195], v200 offset:4480\n"
"v_lshlrev_b32 v92, 16, v174\n"
"v_mfma_f32_16x16x32_bf16 v[56:59], v[180:183], a[120:123], v[56:59]\n"
"ds_read_b64_tr_b16 v[196:197], v200 offset:4256\n"
"v_and_b32 v93, 0xffff0000, v174\n"
"v_mfma_f32_16x16x32_bf16 v[48:51], v[180:183], a[112:115], v[48:51]\n"
"s_waitcnt lgkmcnt(5)\n"
"buffer_load_dwordx4 v207, s[28:31], s66 offen lds\n"
"v_mfma_f32_16x16x32_bf16 v[0:3], v[184:187], a[128:131], v[0:3]\n"
"s_add_u32 s66, s60, 0x438000\n"
"s_add_u32 m0, s59, 0x8400\n"
"ds_read_b64_tr_b16 v[198:199], v200 offset:4512\n"
"v_lshlrev_b32 v94, 16, v175\n"
"v_mfma_f32_16x16x32_bf16 v[8:11], v[184:187], a[136:139], v[8:11]\n"
"ds_read_b64_tr_b16 v[176:177], v200 offset:8448\n"
"v_and_b32 v95, 0xffff0000, v175\n"
"v_mfma_f32_16x16x32_bf16 v[4:7], v[184:187], a[132:135], v[4:7]\n"
"ds_read_b64_tr_b16 v[178:179], v200 offset:8704\n"
"v_lshlrev_b32 v96, 16, v140\n"
"v_mfma_f32_16x16x32_bf16 v[12:15], v[184:187], a[140:143], v[12:15]\n"
"s_waitcnt lgkmcnt(4)\n"
"ds_read_b64_tr_b16 v[180:181], v200 offset:8480\n"
"v_and_b32 v97, 0xffff0000, v140\n"
"v_mfma_f32_16x16x32_bf16 v[28:31], v[188:191], a[140:143], v[28:31]\n"
"buffer_load_dwordx4 v207, s[24:27], s66 offen lds\n"
"v_mfma_f32_16x16x32_bf16 v[20:23], v[188:191], a[132:135], v[20:23]\n"
"s_add_u32 m0, s59, 0x9480\n"
"ds_read_b64_tr_b16 v[182:183], v200 offset:8736\n"
"v_lshlrev_b32 v98, 16, v141\n"
"v_mfma_f32_16x16x32_bf16 v[24:27], v[188:191], a[136:139], v[24:27]\n"
"ds_read_b128 v[164:167], v203 offset:9536\n"
"v_and_b32 v99, 0xffff0000, v141\n"
"v_mfma_f32_16x16x32_bf16 v[16:19], v[188:191], a[128:131], v[16:19]\n"
"ds_read_b128 v[172:175], v204 offset:9536\n"
"v_add_f32 v123, v68, v64\n"
"v_mfma_f32_16x16x32_bf16 v[32:35], v[192:195], a[128:131], v[32:35]\n"
"ds_read_b64_tr_b16 v[184:185], v200 offset:12672\n"
"v_add_f32 v124, v69, v65\n"
"v_mfma_f32_16x16x32_bf16 v[40:43], v[192:195], a[136:139], v[40:43]\n"
"buffer_load_dwordx4 v207, s[28:31], s66 offen lds\n"
"v_mfma_f32_16x16x32_bf16 v[36:39], v[192:195], a[132:135], v[36:39]\n"
"s_add_u32 s66, s60, 0x480000\n"
"ds_read_b64_tr_b16 v[186:187], v200 offset:12928\n"
"v_add_f32 v125, v70, v66\n"
"v_mfma_f32_16x16x32_bf16 v[44:47], v[192:195], a[140:143], v[44:47]\n"
"s_waitcnt lgkmcnt(4)\n"
"ds_read_b64_tr_b16 v[188:189], v200 offset:12704\n"
"v_add_f32 v126, v71, v67\n"
"v_mfma_f32_16x16x32_bf16 v[60:63], v[196:199], a[140:143], v[60:63]\n"
"s_add_u32 m0, s59, 0x14a00\n"
"ds_read_b64_tr_b16 v[190:191], v200 offset:12960\n"
"v_add_f32 v123, v72, v123\n"
"v_mfma_f32_16x16x32_bf16 v[52:55], v[196:199], a[132:135], v[52:55]\n"
"v_add_f32 v124, v73, v124\n"
"v_add_f32 v125, v74, v125\n"
"v_mfma_f32_16x16x32_bf16 v[56:59], v[196:199], a[136:139], v[56:59]\n"
"buffer_load_dwordx4 v207, s[24:27], s66 offen lds\n"
"v_mfma_f32_16x16x32_bf16 v[48:51], v[196:199], a[128:131], v[48:51]\n"
"s_add_u32 m0, s59, 0x15a80\n"
"v_add_f32 v126, v75, v126\n"
"v_add_f32 v123, v76, v123\n"
"v_mfma_f32_16x16x32_bf16 v[0:3], v[176:179], a[144:147], v[0:3]\n"
"v_add_f32 v124, v77, v124\n"
"v_add_f32 v125, v78, v125\n"
"v_mfma_f32_16x16x32_bf16 v[8:11], v[176:179], a[152:155], v[8:11]\n"
"v_add_f32 v126, v79, v126\n"
"v_add_f32 v123, v80, v123\n"
"v_mfma_f32_16x16x32_bf16 v[4:7], v[176:179], a[148:151], v[4:7]\n"
"v_add_f32 v124, v81, v124\n"
"v_add_f32 v125, v82, v125\n"
"v_mfma_f32_16x16x32_bf16 v[12:15], v[176:179], a[156:159], v[12:15]\n"
"v_add_f32 v126, v83, v126\n"
"v_add_f32 v123, v84, v123\n"
"v_mfma_f32_16x16x32_bf16 v[28:31], v[180:183], a[156:159], v[28:31]\n"
"buffer_load_dwordx4 v207, s[28:31], s66 offen lds\n"
"v_mfma_f32_16x16x32_bf16 v[20:23], v[180:183], a[148:151], v[20:23]\n"
"s_waitcnt vmcnt(10) lgkmcnt(0)\n"
"s_add_u32 s66, s60, 0x4c8000\n"
"v_add_f32 v124, v85, v124\n"
"v_add_f32 v125, v86, v125\n"
"v_mfma_f32_16x16x32_bf16 v[24:27], v[180:183], a[152:155], v[24:27]\n"
"s_barrier\n"
"s_add_u32 m0, s59, 0x16b00\n"
"v_add_f32 v126, v87, v126\n"
"v_add_f32 v123, v88, v123\n"
"v_mfma_f32_16x16x32_bf16 v[16:19], v[180:183], a[144:147], v[16:19]\n"
"ds_read_b64_tr_b16 v[192:193], v200 offset:16896\n"
"v_add_f32 v124, v89, v124\n"
"v_mfma_f32_16x16x32_bf16 v[32:35], v[184:187], a[144:147], v[32:35]\n"
"ds_read_b64_tr_b16 v[194:195], v200 offset:17152\n"
"v_add_f32 v125, v90, v125\n"
"v_mfma_f32_16x16x32_bf16 v[40:43], v[184:187], a[152:155], v[40:43]\n"
"ds_read_b64_tr_b16 v[196:197], v200 offset:16928\n"
"v_add_f32 v126, v91, v126\n"
"v_mfma_f32_16x16x32_bf16 v[36:39], v[184:187], a[148:151], v[36:39]\n"
"buffer_load_dwordx4 v207, s[24:27], s66 offen lds\n"
"v_mfma_f32_16x16x32_bf16 v[44:47], v[184:187], a[156:159], v[44:47]\n"
"s_add_u32 m0, s59, 0x17b80\n"
"ds_read_b64_tr_b16 v[198:199], v200 offset:17184\n"
"v_add_f32 v123, v92, v123\n"
"v_mfma_f32_16x16x32_bf16 v[60:63], v[188:191], a[156:159], v[60:63]\n"
"ds_read_b64_tr_b16 v[176:177], v200 offset:21120\n"
"v_add_f32 v124, v93, v124\n"
"v_mfma_f32_16x16x32_bf16 v[52:55], v[188:191], a[148:151], v[52:55]\n"
"ds_read_b64_tr_b16 v[178:179], v200 offset:21376\n"
"v_add_f32 v125, v94, v125\n"
"v_mfma_f32_16x16x32_bf16 v[56:59], v[188:191], a[152:155], v[56:59]\n"
"ds_read_b64_tr_b16 v[180:181], v200 offset:21152\n"
"v_add_f32 v126, v95, v126\n"
"v_mfma_f32_16x16x32_bf16 v[48:51], v[188:191], a[144:147], v[48:51]\n"
"s_waitcnt lgkmcnt(4)\n"
"ds_read_b64_tr_b16 v[182:183], v200 offset:21408\n"
"v_add_f32 v123, v96, v123\n"
"v_mfma_f32_16x16x32_bf16 v[0:3], v[192:195], a[160:163], v[0:3]\n"
"buffer_load_dwordx4 v207, s[28:31], s66 offen lds\n"
"v_mfma_f32_16x16x32_bf16 v[8:11], v[192:195], a[168:171], v[8:11]\n"
"s_add_u32 s66, s61, 0\n"
"ds_read_b64_tr_b16 v[184:185], v201 offset:0\n"
"v_add_f32 v124, v97, v124\n"
"v_mfma_f32_16x16x32_bf16 v[4:7], v[192:195], a[164:167], v[4:7]\n"
"s_add_u32 m0, s59, 0x18c00\n"
"ds_read_b64_tr_b16 v[186:187], v201 offset:256\n"
"v_add_f32 v125, v98, v125\n"
"v_mfma_f32_16x16x32_bf16 v[12:15], v[192:195], a[172:175], v[12:15]\n"
"s_waitcnt lgkmcnt(4)\n"
"ds_read_b64_tr_b16 v[188:189], v201 offset:32\n"
"v_add_f32 v126, v99, v126\n"
"v_mfma_f32_16x16x32_bf16 v[28:31], v[196:199], a[172:175], v[28:31]\n"
"ds_read_b64_tr_b16 v[190:191], v201 offset:288\n"
"v_add_f32 v123, v123, v124\n"
"v_mfma_f32_16x16x32_bf16 v[20:23], v[196:199], a[164:167], v[20:23]\n"
"ds_read_b64_tr_b16 v[192:193], v201 offset:4224\n"
"v_add_f32 v125, v125, v126\n"
"v_mfma_f32_16x16x32_bf16 v[24:27], v[196:199], a[168:171], v[24:27]\n"
"buffer_load_dwordx4 v208, s[32:35], s66 offen lds\n"
"v_mfma_f32_16x16x32_bf16 v[16:19], v[196:199], a[160:163], v[16:19]\n"
"s_add_u32 s66, s61, 0x480\n"
"ds_read_b64_tr_b16 v[194:195], v201 offset:4480\n"
"v_add_f32 v123, v123, v125\n"
"v_mfma_f32_16x16x32_bf16 v[32:35], v[176:179], a[160:163], v[32:35]\n"
"s_add_u32 m0, s59, 0x19c80\n"
"ds_read_b64_tr_b16 v[196:197], v201 offset:4256\n"
"v_add_f32_dpp v123, v123, v123 row_ror:8 row_mask:0xf bank_mask:0xf\n"
"v_mfma_f32_16x16x32_bf16 v[40:43], v[176:179], a[168:171], v[40:43]\n"
"ds_read_b64_tr_b16 v[198:199], v201 offset:4512\n"
"v_add_f32_dpp v123, v123, v123 row_ror:4 row_mask:0xf bank_mask:0xf\n"
"v_mfma_f32_16x16x32_bf16 v[36:39], v[176:179], a[164:167], v[36:39]\n"
"L_st1_%=:\n s_cbranch_execz L_st5_%=\n"
"v_mfma_f32_16x16x32_bf16 v[44:47], v[176:179], a[172:175], v[44:47]\n"
"s_waitcnt lgkmcnt(3)\n"
"ds_read_b64_tr_b16 v[176:177], v201 offset:8448\n"
"v_add_f32_dpp v123, v123, v123 row_ror:2 row_mask:0xf bank_mask:0xf\n"
"v_mfma_f32_16x16x32_bf16 v[60:63], v[180:183], a[172:175], v[60:63]\n"
"buffer_load_dwordx4 v208, s[32:35], s66 offen lds\n"
"v_mfma_f32_16x16x32_bf16 v[52:55], v[180:183], a[164:167], v[52:55]\n"
"s_add_u32 s66, s61, 0x900\n"
"ds_read_b64_tr_b16 v[178:179], v201 offset:8704\n"
"v_add_f32_dpp v123, v123, v123 row_ror:1 row_mask:0xf bank_mask:0xf\n"
"v_mfma_f32_16x16x32_bf16 v[56:59], v[180:183], a[168:171], v[56:59]\n"
"s_add_u32 m0, s59, 0x1ad00\n"
"v_mul_f32 v123, s9, v123\n"
"v_cvt_pk_bf16_f32 v127, v123, v123\n"
"v_mfma_f32_16x16x32_bf16 v[48:51], v[180:183], a[160:163], v[48:51]\n"
"ds_read_b64_tr_b16 v[180:181], v201 offset:8480\n"
"v_lshlrev_b32 v127, 16, v127\n"
"v_mfma_f32_16x16x32_bf16 v[0:3], v[184:187], a[176:179], v[0:3]\n"
"ds_read_b64_tr_b16 v[182:183], v201 offset:8736\n"
"v_sub_f32 v118, v123, v127\n"
"v_mfma_f32_16x16x32_bf16 v[8:11], v[184:187], a[184:187], v[8:11]\n"
"v_sub_f32 v64, v64, v127\n"
"v_mul_f32 v123, v64, v64\n"
"v_mfma_f32_16x16x32_bf16 v[4:7], v[184:187], a[180:183], v[4:7]\n"
"s_mov_b64 exec, 0x1ff\n buffer_load_dword v215, v209, s[24:27], 0 offen offset:256\n s_mov_b64 exec, -1\n"
"v_mfma_f32_16x16x32_bf16 v[12:15], v[184:187], a[188:191], v[12:15]\n"
"ds_read_b64_tr_b16 v[184:185], v201 offset:12672\n"
"v_sub_f32 v65, v65, v127\n"
"v_mfma_f32_16x16x32_bf16 v[28:31], v[188:191], a[188:191], v[28:31]\n"
"ds_read_b64_tr_b16 v[186:187], v201 offset:12928\n"
"v_mul_f32 v124, v65, v65\n"
"v_mfma_f32_16x16x32_bf16 v[20:23], v[188:191], a[180:183], v[20:23]\n"
"v_cvt_pk_bf16_f32 v100, v64, v65\n"
"v_sub_f32 v66, v66, v127\n"
"v_mfma_f32_16x16x32_bf16 v[24:27], v[188:191], a[184:187], v[24:27]\n"
"v_mul_f32 v125, v66, v66\n"
"v_sub_f32 v67, v67, v127\n"
"v_mfma_f32_16x16x32_bf16 v[16:19], v[188:191], a[176:179], v[16:19]\n"
"s_waitcnt lgkmcnt(2)\n"
"s_mov_b64 exec, 0x1ff\n buffer_load_dword v215, v209, s[28:31], 0 offen offset:256\n s_mov_b64 exec, -1\n"
"v_mfma_f32_16x16x32_bf16 v[32:35], v[192:195], a[176:179], v[32:35]\n"
"ds_read_b64_tr_b16 v[188:189], v201 offset:12704\n"
"v_mul_f32 v126, v67, v67\n"
"v_mfma_f32_16x16x32_bf16 v[40:43], v[192:195], a[184:187], v[40:43]\n"
"ds_read_b64_tr_b16 v[190:191], v201 offset:12960\n"
"v_cvt_pk_bf16_f32 v101, v66, v67\n"
"v_mfma_f32_16x16x32_bf16 v[36:39], v[192:195], a[180:183], v[36:39]\n"
"v_sub_f32 v68, v68, v127\n"
"v_fmac_f32 v123, v68, v68\n"
"v_mfma_f32_16x16x32_bf16 v[44:47], v[192:195], a[188:191], v[44:47]\n"
"v_sub_f32 v69, v69, v127\n"
"v_fmac_f32 v124, v69, v69\n"
"v_mfma_f32_16x16x32_bf16 v[60:63], v[196:199], a[188:191], v[60:63]\n"
"v_cvt_pk_bf16_f32 v102, v68, v69\n"
"v_sub_f32 v70, v70, v127\n"
"v_mfma_f32_16x16x32_bf16 v[52:55], v[196:199], a[180:183], v[52:55]\n"
"v_fmac_f32 v125, v70, v70\n"
"v_sub_f32 v71, v71, v127\n"
"v_mfma_f32_16x16x32_bf16 v[56:59], v[196:199], a[184:187], v[56:59]\n"
"v_fmac_f32 v126, v71, v71\n"
"v_cvt_pk_bf16_f32 v103, v70, v71\n"
"v_mfma_f32_16x16x32_bf16 v[48:51], v[196:199], a[176:179], v[48:51]\n"
"ds_write_b128 v238, v[100:103] offset:0\n"
"v_mfma_f32_16x16x32_bf16 v[0:3], v[176:179], a[192:195], v[0:3]\n"
"v_sub_f32 v72, v72, v127\n"
"v_fmac_f32 v123, v72, v72\n"
"v_mfma_f32_16x16x32_bf16 v[8:11], v[176:179], a[200:203], v[8:11]\n"
"v_sub_f32 v73, v73, v127\n"
"v_fmac_f32 v124, v73, v73\n"
"v_mfma_f32_16x16x32_bf16 v[4:7], v[176:179], a[196:199], v[4:7]\n"
"v_cvt_pk_bf16_f32 v104, v72, v73\n"
"v_sub_f32 v74, v74, v127\n"
"v_mfma_f32_16x16x32_bf16 v[12:15], v[176:179], a[204:207], v[12:15]\n"
"v_fmac_f32 v125, v74, v74\n"
"v_sub_f32 v75, v75, v127\n"
"v_mfma_f32_16x16x32_bf16 v[28:31], v[180:183], a[204:207], v[28:31]\n"
"v_fmac_f32 v126, v75, v75\n"
"v_cvt_pk_bf16_f32 v105, v74, v75\n"
"v_mfma_f32_16x16x32_bf16 v[20:23], v[180:183], a[196:199], v[20:23]\n"
"s_waitcnt vmcnt(8)\n"
"v_sub_f32 v76, v76, v127\n"
"v_fmac_f32 v123, v76, v76\n"
"v_mfma_f32_16x16x32_bf16 v[24:27], v[180:183], a[200:203], v[24:27]\n"
"s_barrier\n"
"v_sub_f32 v77, v77, v127\n"
"v_fmac_f32 v124, v77, v77\n"
"v_mfma_f32_16x16x32_bf16 v[16:19], v[180:183], a[192:195], v[16:19]\n"
"s_waitcnt lgkmcnt(0)\n"
"ds_read_b64_tr_b16 v[192:193], v201 offset:16896\n"
"v_cvt_pk_bf16_f32 v106, v76, v77\n"
"v_mfma_f32_16x16x32_bf16 v[32:35], v[184:187], a[192:195], v[32:35]\n"
"ds_read_b64_tr_b16 v[194:195], v201 offset:17152\n"
"v_sub_f32 v78, v78, v127\n"
"v_mfma_f32_16x16x32_bf16 v[40:43], v[184:187], a[200:203], v[40:43]\n"
"ds_read_b64_tr_b16 v[196:197], v201 offset:16928\n"
"v_sub_f32 v79, v79, v127\n"
"v_mfma_f32_16x16x32_bf16 v[36:39], v[184:187], a[196:199], v[36:39]\n"
"ds_read_b64_tr_b16 v[198:199], v201 offset:17184\n"
"v_fmac_f32 v125, v78, v78\n"
"v_mfma_f32_16x16x32_bf16 v[44:47], v[184:187], a[204:207], v[44:47]\n"
"ds_read_b64_tr_b16 v[176:177], v201 offset:21120\n"
"v_fmac_f32 v126, v79, v79\n"
"v_mfma_f32_16x16x32_bf16 v[60:63], v[188:191], a[204:207], v[60:63]\n"
"ds_read_b64_tr_b16 v[178:179], v201 offset:21376\n"
"v_sub_f32 v80, v80, v127\n"
"v_mfma_f32_16x16x32_bf16 v[52:55], v[188:191], a[196:199], v[52:55]\n"
"ds_read_b64_tr_b16 v[180:181], v201 offset:21152\n"
"v_sub_f32 v81, v81, v127\n"
"v_mfma_f32_16x16x32_bf16 v[56:59], v[188:191], a[200:203], v[56:59]\n"
"ds_read_b64_tr_b16 v[182:183], v201 offset:21408\n"
"v_sub_f32 v82, v82, v127\n"
"v_mfma_f32_16x16x32_bf16 v[48:51], v[188:191], a[192:195], v[48:51]\n"
"s_waitcnt lgkmcnt(5)\n"
"ds_read_b64_tr_b16 v[184:185], v200 offset:25344\n"
"v_sub_f32 v83, v83, v127\n"
"v_mfma_f32_16x16x32_bf16 v[0:3], v[192:195], a[208:211], v[0:3]\n"
"ds_read_b64_tr_b16 v[186:187], v200 offset:25600\n"
"v_fmac_f32 v123, v80, v80\n"
"v_mfma_f32_16x16x32_bf16 v[8:11], v[192:195], a[216:219], v[8:11]\n"
"ds_read_b64_tr_b16 v[188:189], v200 offset:25376\n"
"v_fmac_f32 v124, v81, v81\n"
"v_mfma_f32_16x16x32_bf16 v[4:7], v[192:195], a[212:215], v[4:7]\n"
"ds_read_b64_tr_b16 v[190:191], v200 offset:25632\n"
"v_fmac_f32 v125, v82, v82\n"
"v_mfma_f32_16x16x32_bf16 v[12:15], v[192:195], a[220:223], v[12:15]\n"
"s_waitcnt lgkmcnt(5)\n"
"ds_read_b64_tr_b16 v[192:193], v200 offset:29568\n"
"v_fmac_f32 v126, v83, v83\n"
"v_mfma_f32_16x16x32_bf16 v[28:31], v[196:199], a[220:223], v[28:31]\n"
"ds_read_b64_tr_b16 v[194:195], v200 offset:29824\n"
"v_sub_f32 v84, v84, v127\n"
"v_mfma_f32_16x16x32_bf16 v[20:23], v[196:199], a[212:215], v[20:23]\n"
"v_sub_f32 v85, v85, v127\n"
"v_mfma_f32_16x16x32_bf16 v[24:27], v[196:199], a[216:219], v[24:27]\n"
"v_sub_f32 v86, v86, v127\n"
"v_mfma_f32_16x16x32_bf16 v[16:19], v[196:199], a[208:211], v[16:19]\n"
"ds_read_b64_tr_b16 v[196:197], v200 offset:29600\n"
"v_sub_f32 v87, v87, v127\n"
"v_mfma_f32_16x16x32_bf16 v[32:35], v[176:179], a[208:211], v[32:35]\n"
"ds_read_b64_tr_b16 v[198:199], v200 offset:29856\n"
"v_fmac_f32 v123, v84, v84\n"
"v_mfma_f32_16x16x32_bf16 v[40:43], v[176:179], a[216:219], v[40:43]\n"
"v_fmac_f32 v124, v85, v85\n"
"v_mfma_f32_16x16x32_bf16 v[36:39], v[176:179], a[212:215], v[36:39]\n"
"v_fmac_f32 v125, v86, v86\n"
"v_mfma_f32_16x16x32_bf16 v[44:47], v[176:179], a[220:223], v[44:47]\n"
"s_waitcnt lgkmcnt(2)\n"
"ds_read_b64_tr_b16 v[176:177], v200 offset:33792\n"
"v_fmac_f32 v126, v87, v87\n"
"v_mfma_f32_16x16x32_bf16 v[60:63], v[180:183], a[220:223], v[60:63]\n"
"ds_read_b64_tr_b16 v[178:179], v200 offset:34048\n"
"v_sub_f32 v88, v88, v127\n"
"v_mfma_f32_16x16x32_bf16 v[52:55], v[180:183], a[212:215], v[52:55]\n"
"v_sub_f32 v89, v89, v127\n"
"v_mfma_f32_16x16x32_bf16 v[56:59], v[180:183], a[216:219], v[56:59]\n"
"v_sub_f32 v90, v90, v127\n"
"v_mfma_f32_16x16x32_bf16 v[48:51], v[180:183], a[208:211], v[48:51]\n"
"buffer_load_dwordx4 v208, s[32:35], s66 offen lds\n"
"v_mfma_f32_16x16x32_bf16 v[0:3], v[184:187], a[224:227], v[0:3]\n"
"s_add_u32 s66, s61, 0x1b000\n"
"s_add_u32 m0, s59, 0x1bd80\n"
"ds_read_b64_tr_b16 v[180:181], v200 offset:33824\n"
"v_sub_f32 v91, v91, v127\n"
"v_mfma_f32_16x16x32_bf16 v[8:11], v[184:187], a[232:235], v[8:11]\n"
"ds_read_b64_tr_b16 v[182:183], v200 offset:34080\n"
"v_fmac_f32 v123, v88, v88\n"
"v_mfma_f32_16x16x32_bf16 v[4:7], v[184:187], a[228:231], v[4:7]\n"
"v_fmac_f32 v124, v89, v89\n"
"v_mfma_f32_16x16x32_bf16 v[12:15], v[184:187], a[236:239], v[12:15]\n"
"ds_read_b64_tr_b16 v[184:185], v200 offset:38016\n"
"v_fmac_f32 v125, v90, v90\n"
"v_mfma_f32_16x16x32_bf16 v[28:31], v[188:191], a[236:239], v[28:31]\n"
"buffer_load_dwordx4 v208, s[32:35], s66 offen lds\n"
"v_mfma_f32_16x16x32_bf16 v[20:23], v[188:191], a[228:231], v[20:23]\n"
"s_add_u32 s66, s61, 0x1b480\n"
"s_add_u32 m0, s59, 0x1ce00\n"
"ds_read_b64_tr_b16 v[186:187], v200 offset:38272\n"
"v_fmac_f32 v126, v91, v91\n"
"v_mfma_f32_16x16x32_bf16 v[24:27], v[188:191], a[232:235], v[24:27]\n"
"v_sub_f32 v92, v92, v127\n"
"v_mfma_f32_16x16x32_bf16 v[16:19], v[188:191], a[224:227], v[16:19]\n"
"ds_read_b64_tr_b16 v[188:189], v200 offset:38048\n"
"v_sub_f32 v93, v93, v127\n"
"v_mfma_f32_16x16x32_bf16 v[32:35], v[192:195], a[224:227], v[32:35]\n"
"buffer_load_dwordx4 v208, s[32:35], s66 offen lds\n"
"v_mfma_f32_16x16x32_bf16 v[40:43], v[192:195], a[232:235], v[40:43]\n"
"s_add_u32 s66, s61, 0x1b900\n"
"s_add_u32 m0, s59, 0x1de80\n"
"ds_read_b64_tr_b16 v[190:191], v200 offset:38304\n"
"v_sub_f32 v94, v94, v127\n"
"v_mfma_f32_16x16x32_bf16 v[36:39], v[192:195], a[228:231], v[36:39]\n"
"v_sub_f32 v95, v95, v127\n"
"v_mfma_f32_16x16x32_bf16 v[44:47], v[192:195], a[236:239], v[44:47]\n"
"s_waitcnt lgkmcnt(2)\n"
"v_fmac_f32 v123, v92, v92\n"
"v_mfma_f32_16x16x32_bf16 v[60:63], v[196:199], a[236:239], v[60:63]\n"
"buffer_load_dwordx4 v208, s[32:35], s66 offen lds\n"
"v_mfma_f32_16x16x32_bf16 v[52:55], v[196:199], a[228:231], v[52:55]\n"
"s_add_u32 s66, s61, 0x36000\n"
"s_add_u32 m0, s59, 0\n"
"v_fmac_f32 v124, v93, v93\n"
"v_fmac_f32 v125, v94, v94\n"
"v_mfma_f32_16x16x32_bf16 v[56:59], v[196:199], a[232:235], v[56:59]\n"
"v_fmac_f32 v126, v95, v95\n"
"v_sub_f32 v96, v96, v127\n"
"v_mfma_f32_16x16x32_bf16 v[48:51], v[196:199], a[224:227], v[48:51]\n"
"v_sub_f32 v97, v97, v127\n"
"v_mfma_f32_16x16x32_bf16 v[0:3], v[176:179], a[240:243], v[0:3]\n"
"v_sub_f32 v98, v98, v127\n"
"v_sub_f32 v99, v99, v127\n"
"v_mfma_f32_16x16x32_bf16 v[8:11], v[176:179], a[248:251], v[8:11]\n"
"v_fmac_f32 v123, v96, v96\n"
"v_mfma_f32_16x16x32_bf16 v[4:7], v[176:179], a[244:247], v[4:7]\n"
"v_fmac_f32 v124, v97, v97\n"
"v_fmac_f32 v125, v98, v98\n"
"v_mfma_f32_16x16x32_bf16 v[12:15], v[176:179], a[252:255], v[12:15]\n"
"buffer_load_dwordx4 v208, s[32:35], s66 offen lds\n"
"v_mfma_f32_16x16x32_bf16 v[28:31], v[180:183], a[252:255], v[28:31]\n"
"s_add_u32 s66, s61, 0x36480\n"
"s_add_u32 m0, s59, 0x1080\n"
"v_fmac_f32 v126, v99, v99\n"
"v_add_f32 v123, v123, v124\n"
"v_mfma_f32_16x16x32_bf16 v[20:23], v[180:183], a[244:247], v[20:23]\n"
"s_waitcnt vmcnt(9)\n"
"v_add_f32 v125, v125, v126\n"
"v_add_f32 v123, v123, v125\n"
"v_mfma_f32_16x16x32_bf16 v[24:27], v[180:183], a[248:251], v[24:27]\n"
"s_barrier\n"
"v_mul_f32 v118, v118, v118\n"
"v_add_f32_dpp v123, v123, v123 row_ror:8 row_mask:0xf bank_mask:0xf\n"
"v_mfma_f32_16x16x32_bf16 v[16:19], v[180:183], a[240:243], v[16:19]\n"
"ds_read_b64_tr_b16 v[192:193], v201 offset:25344\n"
"v_add_f32_dpp v123, v123, v123 row_ror:4 row_mask:0xf bank_mask:0xf\n"
"v_mfma_f32_16x16x32_bf16 v[32:35], v[184:187], a[240:243], v[32:35]\n"
"buffer_load_dwordx4 v208, s[32:35], s66 offen lds\n"
"v_mfma_f32_16x16x32_bf16 v[40:43], v[184:187], a[248:251], v[40:43]\n"
"s_add_u32 s66, s61, 0x36900\n"
"s_add_u32 m0, s59, 0x2100\n"
"ds_read_b64_tr_b16 v[194:195], v201 offset:25600\n"
"v_add_f32_dpp v123, v123, v123 row_ror:2 row_mask:0xf bank_mask:0xf\n"
"v_mfma_f32_16x16x32_bf16 v[36:39], v[184:187], a[244:247], v[36:39]\n"
"ds_read_b64_tr_b16 v[196:197], v201 offset:25376\n"
"v_add_f32_dpp v123, v123, v123 row_ror:1 row_mask:0xf bank_mask:0xf\n"
"v_mfma_f32_16x16x32_bf16 v[44:47], v[184:187], a[252:255], v[44:47]\n"
"s_waitcnt lgkmcnt(3)\n"
"ds_read_b64_tr_b16 v[198:199], v201 offset:25632\n"
"v_fma_f32 v123, v123, s9, -v118\n"
"v_mfma_f32_16x16x32_bf16 v[60:63], v[188:191], a[252:255], v[60:63]\n"
"ds_read_b64_tr_b16 v[176:177], v201 offset:29568\n"
"v_max_f32 v123, 0, v123\n"
"v_mfma_f32_16x16x32_bf16 v[52:55], v[188:191], a[244:247], v[52:55]\n"
"buffer_load_dwordx4 v208, s[32:35], s66 offen lds\n"
"v_mfma_f32_16x16x32_bf16 v[56:59], v[188:191], a[248:251], v[56:59]\n"
"s_add_u32 s66, s61, 0x51000\n"
"s_add_u32 m0, s59, 0x3180\n"
"ds_read_b64_tr_b16 v[178:179], v201 offset:29824\n"
"v_cvt_pk_bf16_f32 v123, v123, v123\n"
"v_mfma_f32_16x16x32_bf16 v[48:51], v[188:191], a[240:243], v[48:51]\n"
"s_waitcnt lgkmcnt(4)\n"
"ds_read_b64_tr_b16 v[180:181], v201 offset:29600\n"
"v_lshlrev_b32 v123, 16, v123\n"
"v_mfma_f32_16x16x32_bf16 v[0:3], v[192:195], v[144:147], v[0:3]\n"
"ds_read_b64_tr_b16 v[182:183], v201 offset:29856\n"
"v_add_f32 v123, 0x3727c5ac, v123\n"
"v_mfma_f32_16x16x32_bf16 v[8:11], v[192:195], v[152:155], v[8:11]\n"
"ds_read_b64_tr_b16 v[184:185], v201 offset:33792\n"
"v_cvt_pk_bf16_f32 v123, v123, v123\n"
"v_mfma_f32_16x16x32_bf16 v[4:7], v[192:195], v[148:151], v[4:7]\n"
"buffer_load_dwordx4 v208, s[32:35], s66 offen lds\n"
"v_mfma_f32_16x16x32_bf16 v[12:15], v[192:195], v[156:159], v[12:15]\n"
"s_waitcnt lgkmcnt(4)\n"
"s_add_u32 s66, s61, 0x51480\n"
"ds_read_b64_tr_b16 v[186:187], v201 offset:34048\n"
"v_lshlrev_b32 v123, 16, v123\n"
"v_mfma_f32_16x16x32_bf16 v[28:31], v[196:199], v[156:159], v[28:31]\n"
"s_add_u32 m0, s59, 0x4200\n"
"v_sqrt_f32 v123, v123\n"
"v_mfma_f32_16x16x32_bf16 v[20:23], v[196:199], v[148:151], v[20:23]\n"
"ds_read_b64_tr_b16 v[188:189], v201 offset:33824\n"
"v_cvt_pk_bf16_f32 v123, v123, v123\n"
"v_mfma_f32_16x16x32_bf16 v[24:27], v[196:199], v[152:155], v[24:27]\n"
"ds_read_b64_tr_b16 v[190:191], v201 offset:34080\n"
"v_lshrrev_b32 v124, 4, v233\n"
"v_mfma_f32_16x16x32_bf16 v[16:19], v[196:199], v[144:147], v[16:19]\n"
"s_waitcnt lgkmcnt(3)\n"
"buffer_load_dwordx4 v208, s[32:35], s66 offen lds\n"
"v_mfma_f32_16x16x32_bf16 v[32:35], v[176:179], v[144:147], v[32:35]\n"
"s_add_u32 s66, s61, 0x51900\n"
"s_add_u32 m0, s59, 0x5280\n"
"ds_read_b64_tr_b16 v[192:193], v201 offset:38016\n"
"v_cvt_pk_bf16_f32 v107, v78, v79\n"
"v_mfma_f32_16x16x32_bf16 v[40:43], v[176:179], v[152:155], v[40:43]\n"
"ds_write_b128 v238, v[104:107] offset:4224\n"
"v_mfma_f32_16x16x32_bf16 v[36:39], v[176:179], v[148:151], v[36:39]\n"
"ds_read_b64_tr_b16 v[194:195], v201 offset:38272\n"
"v_cvt_pk_bf16_f32 v108, v80, v81\n"
"v_mfma_f32_16x16x32_bf16 v[44:47], v[176:179], v[156:159], v[44:47]\n"
"buffer_load_dwordx4 v208, s[32:35], s66 offen lds\n"
"v_mfma_f32_16x16x32_bf16 v[60:63], v[180:183], v[156:159], v[60:63]\n"
"s_add_u32 s66, s61, 0x6c000\n"
"s_add_u32 m0, s59, 0xc600\n"
"ds_read_b64_tr_b16 v[196:197], v201 offset:38048\n"
"v_cvt_pk_bf16_f32 v109, v82, v83\n"
"v_mfma_f32_16x16x32_bf16 v[52:55], v[180:183], v[148:151], v[52:55]\n"
"ds_read_b64_tr_b16 v[198:199], v201 offset:38304\n"
"v_cvt_pk_bf16_f32 v110, v84, v85\n"
"v_mfma_f32_16x16x32_bf16 v[56:59], v[180:183], v[152:155], v[56:59]\n"
"ds_read_b32 v224, v120 offset:0\n"
"v_cvt_pk_bf16_f32 v111, v86, v87\n"
"v_cvt_pk_bf16_f32 v112, v88, v89\n"
"v_mfma_f32_16x16x32_bf16 v[48:51], v[180:183], v[144:147], v[48:51]\n"
"s_waitcnt lgkmcnt(4)\n"
"ds_read_b32 v225, v120 offset:64\n"
"ds_read_b32 v226, v121 offset:0\n"
"v_cvt_pk_bf16_f32 v113, v90, v91\n"
"v_mfma_f32_16x16x32_bf16 v[0:3], v[184:187], v[160:163], v[0:3]\n"
"ds_read_b32 v227, v121 offset:64\n"
"v_cvt_pk_bf16_f32 v114, v92, v93\n"
"v_cvt_pk_bf16_f32 v115, v94, v95\n"
"v_mfma_f32_16x16x32_bf16 v[8:11], v[184:187], v[168:171], v[8:11]\n"
"buffer_load_dwordx4 v208, s[32:35], s66 offen lds\n"
"v_cvt_pk_bf16_f32 v116, v96, v97\n"
"v_cvt_pk_bf16_f32 v117, v98, v99\n"
"v_lshlrev_b32 v123, 16, v123\n"
"v_mfma_f32_16x16x32_bf16 v[4:7], v[184:187], v[164:167], v[4:7]\n"
"s_add_u32 s66, s61, 0x6c480\n"
"s_add_u32 m0, s59, 0xd680\n"
"ds_write_b128 v238, v[108:111] offset:8448\n"
"v_mfma_f32_16x16x32_bf16 v[12:15], v[184:187], v[172:175], v[12:15]\n"
"ds_write_b128 v238, v[112:115] offset:12672\n"
"v_mfma_f32_16x16x32_bf16 v[28:31], v[188:191], v[172:175], v[28:31]\n"
"ds_write_b64 v239, v[116:117] offset:16896\n"
"v_mfma_f32_16x16x32_bf16 v[20:23], v[188:191], v[164:167], v[20:23]\n"
"v_lshl_add_u32 v124, v124, 2, s69\n"
"ds_write_b32 v124, v123 offset:0\n"
"s_waitcnt vmcnt(7) lgkmcnt(0)\n"
"v_rcp_f32 v220, v224\n"
"v_mfma_f32_16x16x32_bf16 v[24:27], v[188:191], v[168:171], v[24:27]\n"
"s_barrier\n"
"s_lshl_b32 s69, s55, 12\n"
"buffer_load_dwordx4 v208, s[32:35], s66 offen lds\n"
"v_mfma_f32_16x16x32_bf16 v[16:19], v[188:191], v[160:163], v[16:19]\n"
"s_add_u32 s66, s61, 0x6c900\n"
"s_add_u32 m0, s59, 0xe700\n"
"v_rcp_f32 v221, v225\n"
"v_mfma_f32_16x16x32_bf16 v[32:35], v[192:195], v[160:163], v[32:35]\n"
"s_add_u32 s68, s68, s69\n"
"v_rcp_f32 v222, v226\n"
"v_mfma_f32_16x16x32_bf16 v[40:43], v[192:195], v[168:171], v[40:43]\n"
"s_lshl_b32 s69, s15, 10\n"
"v_rcp_f32 v223, v227\n"
"v_mfma_f32_16x16x32_bf16 v[36:39], v[192:195], v[164:167], v[36:39]\n"
"s_add_u32 s70, s68, s69\n"
"ds_read_b64_tr_b16 v[176:177], v214 offset:38016\n"
"v_mul_f32 v220, 0xbfb8aa3b, v220\n"
"v_mfma_f32_16x16x32_bf16 v[44:47], v[192:195], v[172:175], v[44:47]\n"
"s_add_u32 s52, s70, 0\n"
"ds_read_b64_tr_b16 v[178:179], v214 offset:38272\n"
"v_mul_f32 v224, v224, v224\n"
"v_mfma_f32_16x16x32_bf16 v[60:63], v[196:199], v[172:175], v[60:63]\n"
"s_xor_b32 s69, s69, 0x400\n"
"s_add_u32 s71, s68, s69\n"
"buffer_load_dwordx4 v208, s[32:35], s66 offen lds\n"
"v_mfma_f32_16x16x32_bf16 v[52:55], v[196:199], v[164:167], v[52:55]\n"
"s_add_u32 s66, s61, 0x87000\n"
"s_add_u32 m0, s59, 0xf780\n"
"ds_read_b64_tr_b16 v[180:181], v214 offset:42240\n"
"v_mul_f32 v221, 0xbfb8aa3b, v221\n"
"v_mfma_f32_16x16x32_bf16 v[56:59], v[196:199], v[168:171], v[56:59]\n"
"s_lshl_b32 s68, s55, 7\n"
"ds_read_b64_tr_b16 v[182:183], v214 offset:42496\n"
"v_mul_f32 v225, v225, v225\n"
"v_mfma_f32_16x16x32_bf16 v[48:51], v[196:199], v[160:163], v[48:51]\n"
"s_waitcnt lgkmcnt(2)\n"
"ds_read_b64_tr_b16 v[184:185], v214 offset:46464\n"
"v_mul_f32 v222, 0xbfb8aa3b, v222\n"
"v_mfma_f32_16x16x32_bf16 v[76:79], v[176:179], a[12:15], 0\n"
"s_add_u32 s17, s17, s68\n"
"s_mul_i32 s68, s58, 0x280\n"
"buffer_load_dwordx4 v208, s[32:35], s66 offen lds\n"
"v_mfma_f32_16x16x32_bf16 v[72:75], v[176:179], a[8:11], 0\n"
"s_add_u32 s66, s61, 0x87480\n"
"s_add_u32 m0, s59, 0x10800\n"
"ds_read_b64_tr_b16 v[186:187], v214 offset:46720\n"
"v_mul_f32 v226, v226, v226\n"
"v_mfma_f32_16x16x32_bf16 v[68:71], v[176:179], a[4:7], 0\n"
"s_add_u32 s17, s17, 0x1300800\n"
"ds_read_b64_tr_b16 v[188:189], v216 offset:38016\n"
"v_mul_f32 v223, 0xbfb8aa3b, v223\n"
"v_mfma_f32_16x16x32_bf16 v[64:67], v[176:179], a[0:3], 0\n"
"s_waitcnt lgkmcnt(3)\n"
"ds_read_b64_tr_b16 v[190:191], v216 offset:38272\n"
"v_mul_f32 v227, v227, v227\n"
"v_mfma_f32_16x16x32_bf16 v[80:83], v[180:183], a[0:3], 0\n"
"ds_read_b128 v[192:195], v218 offset:0\n"
"v_mul_f32 v240, v220, v0\n"
"v_mfma_f32_16x16x32_bf16 v[84:87], v[180:183], a[4:7], 0\n"
"buffer_load_dwordx4 v208, s[32:35], s66 offen lds\n"
"v_mfma_f32_16x16x32_bf16 v[88:91], v[180:183], a[8:11], 0\n"
"s_add_u32 s66, s61, 0x87900\n"
"s_add_u32 m0, s59, 0x11880\n"
"v_exp_f32 v240, v240\n"
"v_mfma_f32_16x16x32_bf16 v[92:95], v[180:183], a[12:15], 0\n"
"s_waitcnt lgkmcnt(3)\n"
"ds_read_b64_tr_b16 v[196:197], v217 offset:38016\n"
"v_fma_f32 v240, v240, v224, v224\n"
"v_mfma_f32_16x16x32_bf16 v[108:111], v[184:187], a[12:15], 0\n"
"v_rcp_f32 v240, v240\n"
"v_mfma_f32_16x16x32_bf16 v[104:107], v[184:187], a[8:11], 0\n"
"ds_read_b64_tr_b16 v[198:199], v217 offset:38272\n"
"v_mul_f32 v32, v0, v32\n"
"v_mfma_f32_16x16x32_bf16 v[100:103], v[184:187], a[4:7], 0\n"
"ds_read_b64_tr_b16 v[176:177], v214 offset:50688\n"
"v_mul_f32 v32, v32, v240\n"
"v_mfma_f32_16x16x32_bf16 v[96:99], v[184:187], a[0:3], 0\n"
"s_waitcnt lgkmcnt(3)\n"
"ds_read_b64_tr_b16 v[178:179], v214 offset:50944\n"
"buffer_load_dwordx4 v208, s[32:35], s66 offen lds\n"
"v_mfma_f32_16x16x32_bf16 v[112:115], v[188:191], a[0:3], 0\n"
"s_add_u32 s66, s61, 0xa2000\n"
"s_add_u32 m0, s59, 0x6300\n"
"ds_read_b32 v228, v122 offset:0\n"
"v_mul_f32 v241, v220, v1\n"
"v_mfma_f32_16x16x32_bf16 v[116:119], v[188:191], a[4:7], 0\n"
"v_exp_f32 v241, v241\n"
"v_mfma_f32_16x16x32_bf16 v[120:123], v[188:191], v[192:195], 0\n"
"s_waitcnt lgkmcnt(3)\n"
"ds_read_b64_tr_b16 v[180:181], v214 offset:54912\n"
"v_fma_f32 v241, v241, v224, v224\n"
"v_mfma_f32_16x16x32_bf16 v[124:127], v[196:199], v[192:195], 0\n"
"s_waitcnt lgkmcnt(2)\n"
"ds_read_b64_tr_b16 v[182:183], v214 offset:55168\n"
"v_mul_f32 v33, v1, v33\n"
"v_mfma_f32_16x16x32_bf16 v[76:79], v[176:179], a[28:31], v[76:79]\n"
"buffer_load_dwordx4 v208, s[32:35], s66 offen lds\n"
"v_mfma_f32_16x16x32_bf16 v[72:75], v[176:179], a[24:27], v[72:75]\n"
"s_waitcnt lgkmcnt(2)\n"
"s_add_u32 s66, s61, 0xa2480\n"
"v_rcp_f32 v228, v228\n"
"v_mfma_f32_16x16x32_bf16 v[68:71], v[176:179], a[20:23], v[68:71]\n"
"s_add_u32 m0, s59, 0x7380\n"
"ds_read_b64_tr_b16 v[184:185], v214 offset:59136\n"
"v_mul_f32 v228, 0xbfb8aa3b, v228\n"
"v_mfma_f32_16x16x32_bf16 v[64:67], v[176:179], a[16:19], v[64:67]\n"
"s_waitcnt lgkmcnt(1)\n"
"ds_read_b64_tr_b16 v[186:187], v214 offset:59392\n"
"v_add_u32 v214, 0xffff0880, v214\n"
"v_mfma_f32_16x16x32_bf16 v[80:83], v[180:183], a[16:19], v[80:83]\n"
"buffer_load_dwordx4 v208, s[32:35], s66 offen lds\n"
"v_mfma_f32_16x16x32_bf16 v[84:87], v[180:183], a[20:23], v[84:87]\n"
"s_waitcnt vmcnt(8)\n"
"s_add_u32 s66, s61, 0xa2900\n"
"ds_read_b64_tr_b16 v[188:189], v216 offset:50688\n"
"v_mul_f32 v242, v220, v2\n"
"v_mfma_f32_16x16x32_bf16 v[88:91], v[180:183], a[24:27], v[88:91]\n"
"s_barrier\n"
"s_add_u32 m0, s59, 0x8400\n"
"ds_read_b64_tr_b16 v[190:191], v216 offset:50944\n"
"v_add_u32 v216, 0xffff0880, v216\n"
"v_mfma_f32_16x16x32_bf16 v[92:95], v[180:183], a[28:31], v[92:95]\n"
"s_waitcnt lgkmcnt(2)\n"
"ds_read_b128 v[192:195], v218 offset:1088\n"
"v_mul_f32 v34, v2, v34\n"
"v_mfma_f32_16x16x32_bf16 v[108:111], v[184:187], a[28:31], v[108:111]\n"
"ds_read_b64_tr_b16 v[196:197], v217 offset:50688\n"
"v_mul_f32 v243, v220, v3\n"
"v_mfma_f32_16x16x32_bf16 v[104:107], v[184:187], a[24:27], v[104:107]\n"
"ds_read_b64_tr_b16 v[198:199], v217 offset:50944\n"
"v_add_u32 v217, 0xffff0880, v217\n"
"v_mfma_f32_16x16x32_bf16 v[100:103], v[184:187], a[20:23], v[100:103]\n"
"ds_read_b64_tr_b16 v[176:177], v214 offset:0\n"
"buffer_load_dwordx4 v208, s[32:35], s66 offen lds\n"
"v_mfma_f32_16x16x32_bf16 v[96:99], v[184:187], a[16:19], v[96:99]\n"
"s_waitcnt lgkmcnt(4)\n"
"s_add_u32 s66, s61, 0xbd000\n"
"ds_read_b64_tr_b16 v[178:179], v214 offset:256\n"
"v_mul_f32 v35, v3, v35\n"
"v_mfma_f32_16x16x32_bf16 v[112:115], v[188:191], a[16:19], v[112:115]\n"
"s_add_u32 m0, s59, 0x9480\n"
"v_rcp_f32 v241, v241\n"
"v_mfma_f32_16x16x32_bf16 v[116:119], v[188:191], a[20:23], v[116:119]\n"
"s_waitcnt lgkmcnt(4)\n"
"v_exp_f32 v242, v242\n"
"v_mfma_f32_16x16x32_bf16 v[120:123], v[188:191], v[192:195], v[120:123]\n"
"s_waitcnt lgkmcnt(2)\n"
"ds_read_b64_tr_b16 v[180:181], v214 offset:4224\n"
"v_mul_f32 v33, v33, v241\n"
"v_mfma_f32_16x16x32_bf16 v[124:127], v[196:199], v[192:195], v[124:127]\n"
"s_waitcnt lgkmcnt(1)\n"
"ds_read_b64_tr_b16 v[182:183], v214 offset:4480\n"
"buffer_load_dwordx4 v208, s[32:35], s66 offen lds\n"
"v_mfma_f32_16x16x32_bf16 v[76:79], v[176:179], a[44:47], v[76:79]\n"
"s_add_u32 s66, s61, 0xbd480\n"
"s_add_u32 m0, s59, 0xa500\n"
"v_exp_f32 v243, v243\n"
"v_mfma_f32_16x16x32_bf16 v[72:75], v[176:179], a[40:43], v[72:75]\n"
"ds_read_b64_tr_b16 v[184:185], v214 offset:8448\n"
"v_cvt_pk_bf16_f32 v248, v32, v33\n"
"v_mfma_f32_16x16x32_bf16 v[68:71], v[176:179], a[36:39], v[68:71]\n"
"ds_read_b64_tr_b16 v[186:187], v214 offset:8704\n"
"v_fma_f32 v242, v242, v224, v224\n"
"v_mfma_f32_16x16x32_bf16 v[64:67], v[176:179], a[32:35], v[64:67]\n"
"s_waitcnt lgkmcnt(2)\n"
"buffer_load_dwordx4 v208, s[32:35], s66 offen lds\n"
"v_mfma_f32_16x16x32_bf16 v[80:83], v[180:183], a[32:35], v[80:83]\n"
"s_add_u32 s66, s61, 0xbd900\n"
"s_add_u32 m0, s59, 0xb580\n"
"v_rcp_f32 v242, v242\n"
"v_mfma_f32_16x16x32_bf16 v[84:87], v[180:183], a[36:39], v[84:87]\n"
"ds_read_b64_tr_b16 v[188:189], v216 offset:0\n"
"v_mul_f32 v34, v34, v242\n"
"v_mfma_f32_16x16x32_bf16 v[88:91], v[180:183], a[40:43], v[88:91]\n"
"ds_read_b64_tr_b16 v[190:191], v216 offset:256\n"
"v_fma_f32 v243, v243, v224, v224\n"
"v_mfma_f32_16x16x32_bf16 v[92:95], v[180:183], a[44:47], v[92:95]\n"
"s_waitcnt lgkmcnt(2)\n"
"ds_read_b128 v[192:195], v218 offset:2112\n"
"v_mul_f32 v244, v220, v16\n"
"v_mfma_f32_16x16x32_bf16 v[108:111], v[184:187], a[44:47], v[108:111]\n"
"ds_read_b64_tr_b16 v[196:197], v217 offset:0\n"
"v_mul_f32 v48, v16, v48\n"
"v_mfma_f32_16x16x32_bf16 v[104:107], v[184:187], a[40:43], v[104:107]\n"
"ds_read_b64_tr_b16 v[198:199], v217 offset:256\n"
"buffer_load_dwordx4 v208, s[32:35], s66 offen lds\n"
"v_mfma_f32_16x16x32_bf16 v[100:103], v[184:187], a[36:39], v[100:103]\n"
"s_add_u32 s66, s61, 0xd8000\n"
"s_add_u32 m0, s59, 0x12900\n"
"ds_read_b64_tr_b16 v[176:177], v214 offset:12672\n"
"v_mul_f32 v245, v220, v17\n"
"v_mfma_f32_16x16x32_bf16 v[96:99], v[184:187], a[32:35], v[96:99]\n"
"s_waitcnt lgkmcnt(4)\n"
"ds_read_b64_tr_b16 v[178:179], v214 offset:12928\n"
"v_mul_f32 v49, v17, v49\n"
"v_mfma_f32_16x16x32_bf16 v[112:115], v[188:191], a[32:35], v[112:115]\n"
"v_rcp_f32 v243, v243\n"
"v_mfma_f32_16x16x32_bf16 v[116:119], v[188:191], a[36:39], v[116:119]\n"
"s_waitcnt lgkmcnt(4)\n"
"buffer_load_dwordx4 v208, s[32:35], s66 offen lds\n"
"v_mfma_f32_16x16x32_bf16 v[120:123], v[188:191], v[192:195], v[120:123]\n"
"s_waitcnt lgkmcnt(2)\n"
"s_add_u32 s66, s61, 0xd8480\n"
"ds_read_b64_tr_b16 v[180:181], v214 offset:16896\n"
"v_mul_f32 v35, v35, v243\n"
"v_mfma_f32_16x16x32_bf16 v[124:127], v[196:199], v[192:195], v[124:127]\n"
"s_waitcnt lgkmcnt(1)\n"
"s_add_u32 m0, s59, 0x13980\n"
"ds_read_b64_tr_b16 v[182:183], v214 offset:17152\n"
"v_cvt_pk_bf16_f32 v249, v34, v35\n"
"v_mfma_f32_16x16x32_bf16 v[76:79], v[176:179], a[60:63], v[76:79]\n"
"v_exp_f32 v244, v244\n"
"v_mfma_f32_16x16x32_bf16 v[72:75], v[176:179], a[56:59], v[72:75]\n"
"v_exp_f32 v245, v245\n"
"v_mfma_f32_16x16x32_bf16 v[68:71], v[176:179], a[52:55], v[68:71]\n"
"ds_read_b64_tr_b16 v[184:185], v214 offset:21120\n"
"v_fma_f32 v244, v244, v224, v224\n"
"v_mfma_f32_16x16x32_bf16 v[64:67], v[176:179], a[48:51], v[64:67]\n"
"s_waitcnt lgkmcnt(1)\n"
"ds_read_b64_tr_b16 v[186:187], v214 offset:21376\n"
"v_fma_f32 v245, v245, v224, v224\n"
"v_mfma_f32_16x16x32_bf16 v[80:83], v[180:183], a[48:51], v[80:83]\n"
"buffer_load_dwordx4 v208, s[32:35], s66 offen lds\n"
"v_mfma_f32_16x16x32_bf16 v[84:87], v[180:183], a[52:55], v[84:87]\n"
"s_waitcnt vmcnt(8)\n"
"s_add_u32 s66, s61, 0xd8900\n"
"ds_read_b64_tr_b16 v[188:189], v216 offset:12672\n"
"v_mul_f32 v246, v220, v18\n"
"v_mfma_f32_16x16x32_bf16 v[88:91], v[180:183], a[56:59], v[88:91]\n"
"s_barrier\n"
"s_add_u32 m0, s59, 0x14a00\n"
"ds_read_b64_tr_b16 v[190:191], v216 offset:12928\n"
"v_mul_f32 v50, v18, v50\n"
"v_mfma_f32_16x16x32_bf16 v[92:95], v[180:183], a[60:63], v[92:95]\n"
"s_waitcnt lgkmcnt(2)\n"
"ds_read_b128 v[192:195], v218 offset:3200\n"
"v_mul_f32 v247, v220, v19\n"
"v_mfma_f32_16x16x32_bf16 v[108:111], v[184:187], a[60:63], v[108:111]\n"
"ds_read_b64_tr_b16 v[196:197], v217 offset:12672\n"
"v_mul_f32 v51, v19, v51\n"
"v_mfma_f32_16x16x32_bf16 v[104:107], v[184:187], a[56:59], v[104:107]\n"
"ds_read_b64_tr_b16 v[198:199], v217 offset:12928\n"
"buffer_load_dwordx4 v208, s[32:35], s66 offen lds\n"
"v_mfma_f32_16x16x32_bf16 v[100:103], v[184:187], a[52:55], v[100:103]\n"
"s_add_u32 s66, s61, 0xf3000\n"
"s_add_u32 m0, s59, 0x15a80\n"
"ds_read_b64_tr_b16 v[176:177], v214 offset:50688\n"
"v_mul_f32 v240, v221, v4\n"
"v_mfma_f32_16x16x32_bf16 v[96:99], v[184:187], a[48:51], v[96:99]\n"
"s_waitcnt lgkmcnt(4)\n"
"ds_read_b64_tr_b16 v[178:179], v214 offset:50944\n"
"v_mul_f32 v36, v4, v36\n"
"v_mfma_f32_16x16x32_bf16 v[112:115], v[188:191], a[48:51], v[112:115]\n"
"v_rcp_f32 v244, v244\n"
"v_mfma_f32_16x16x32_bf16 v[116:119], v[188:191], a[52:55], v[116:119]\n"
"s_waitcnt lgkmcnt(4)\n"
"buffer_load_dwordx4 v208, s[32:35], s66 offen lds\n"
"v_mfma_f32_16x16x32_bf16 v[120:123], v[188:191], v[192:195], v[120:123]\n"
"s_waitcnt lgkmcnt(2)\n"
"s_add_u32 s66, s61, 0xf3480\n"
"ds_read_b64_tr_b16 v[180:181], v214 offset:54912\n"
"v_mul_f32 v48, v48, v244\n"
"v_mfma_f32_16x16x32_bf16 v[124:127], v[196:199], v[192:195], v[124:127]\n"
"L_st2_%=:\n s_cbranch_execz L_st6_%=\n"
"s_waitcnt lgkmcnt(1)\n"
"s_add_u32 m0, s59, 0x16b00\n"
"ds_read_b64_tr_b16 v[182:183], v214 offset:55168\n"
"v_mul_f32 v241, v221, v5\n"
"v_mfma_f32_16x16x32_bf16 v[76:79], v[176:179], a[76:79], v[76:79]\n"
"v_rcp_f32 v245, v245\n"
"v_mfma_f32_16x16x32_bf16 v[72:75], v[176:179], a[72:75], v[72:75]\n"
"v_exp_f32 v246, v246\n"
"v_mfma_f32_16x16x32_bf16 v[68:71], v[176:179], a[68:71], v[68:71]\n"
"ds_read_b64_tr_b16 v[184:185], v214 offset:59136\n"
"v_mul_f32 v49, v49, v245\n"
"v_mfma_f32_16x16x32_bf16 v[64:67], v[176:179], a[64:67], v[64:67]\n"
"s_waitcnt lgkmcnt(1)\n"
"ds_read_b64_tr_b16 v[186:187], v214 offset:59392\n"
"buffer_load_dwordx4 v208, s[32:35], s66 offen lds\n"
"v_mfma_f32_16x16x32_bf16 v[80:83], v[180:183], a[64:67], v[80:83]\n"
"s_add_u32 s66, s61, 0xf3900\n"
"s_add_u32 m0, s59, 0x17b80\n"
"v_exp_f32 v247, v247\n"
"v_mfma_f32_16x16x32_bf16 v[84:87], v[180:183], a[68:71], v[84:87]\n"
"ds_read_b64_tr_b16 v[188:189], v216 offset:50688\n"
"v_add_u32 v214, 0xf780, v214\n"
"v_mfma_f32_16x16x32_bf16 v[88:91], v[180:183], a[72:75], v[88:91]\n"
"ds_read_b64_tr_b16 v[190:191], v216 offset:50944\n"
"v_add_u32 v216, 0xf780, v216\n"
"v_mfma_f32_16x16x32_bf16 v[92:95], v[180:183], a[76:79], v[92:95]\n"
"s_waitcnt lgkmcnt(2)\n"
"ds_read_b128 v[192:195], v218 offset:4224\n"
"v_cvt_pk_bf16_f32 v250, v48, v49\n"
"v_mfma_f32_16x16x32_bf16 v[108:111], v[184:187], a[76:79], v[108:111]\n"
"ds_read_b64_tr_b16 v[196:197], v217 offset:50688\n"
"buffer_load_dwordx4 v208, s[32:35], s66 offen lds\n"
"v_mfma_f32_16x16x32_bf16 v[104:107], v[184:187], a[72:75], v[104:107]\n"
"s_add_u32 s66, s61, 0x10e000\n"
"s_add_u32 m0, s59, 0x18c00\n"
"ds_read_b64_tr_b16 v[198:199], v217 offset:50944\n"
"v_add_u32 v217, 0xf780, v217\n"
"v_mfma_f32_16x16x32_bf16 v[100:103], v[184:187], a[68:71], v[100:103]\n"
"ds_read_b64_tr_b16 v[176:177], v214 offset:0\n"
"v_fma_f32 v246, v246, v224, v224\n"
"v_mfma_f32_16x16x32_bf16 v[96:99], v[184:187], a[64:67], v[96:99]\n"
"s_waitcnt lgkmcnt(4)\n"
"ds_read_b64_tr_b16 v[178:179], v214 offset:256\n"
"v_fma_f32 v247, v247, v224, v224\n"
"v_mfma_f32_16x16x32_bf16 v[112:115], v[188:191], a[64:67], v[112:115]\n"
"buffer_load_dwordx4 v208, s[32:35], s66 offen lds\n"
"v_mfma_f32_16x16x32_bf16 v[116:119], v[188:191], a[68:71], v[116:119]\n"
"s_waitcnt lgkmcnt(4)\n"
"s_add_u32 s66, s61, 0x10e480\n"
"v_rcp_f32 v246, v246\n"
"v_mfma_f32_16x16x32_bf16 v[120:123], v[188:191], v[192:195], v[120:123]\n"
"s_waitcnt lgkmcnt(2)\n"
"s_add_u32 m0, s59, 0x19c80\n"
"ds_read_b64_tr_b16 v[180:181], v214 offset:4224\n"
"v_mul_f32 v50, v50, v246\n"
"v_mfma_f32_16x16x32_bf16 v[124:127], v[196:199], v[192:195], v[124:127]\n"
"s_waitcnt lgkmcnt(1)\n"
"ds_read_b64_tr_b16 v[182:183], v214 offset:4480\n"
"v_mul_f32 v37, v5, v37\n"
"v_mfma_f32_16x16x32_bf16 v[76:79], v[176:179], a[92:95], v[76:79]\n"
"v_rcp_f32 v247, v247\n"
"v_mfma_f32_16x16x32_bf16 v[72:75], v[176:179], a[88:91], v[72:75]\n"
"v_exp_f32 v240, v240\n"
"v_mfma_f32_16x16x32_bf16 v[68:71], v[176:179], a[84:87], v[68:71]\n"
"ds_read_b64_tr_b16 v[184:185], v214 offset:8448\n"
"v_mul_f32 v51, v51, v247\n"
"v_mfma_f32_16x16x32_bf16 v[64:67], v[176:179], a[80:83], v[64:67]\n"
"s_waitcnt lgkmcnt(1)\n"
"ds_read_b64_tr_b16 v[186:187], v214 offset:8704\n"
"buffer_load_dwordx4 v208, s[32:35], s66 offen lds\n"
"v_mfma_f32_16x16x32_bf16 v[80:83], v[180:183], a[80:83], v[80:83]\n"
"s_add_u32 s66, s61, 0x10e900\n"
"s_add_u32 m0, s59, 0x1ad00\n"
"v_exp_f32 v241, v241\n"
"v_mfma_f32_16x16x32_bf16 v[84:87], v[180:183], a[84:87], v[84:87]\n"
"s_waitcnt vmcnt(8)\n"
"ds_read_b64_tr_b16 v[188:189], v216 offset:0\n"
"v_add_u32 v214, 0xffff0880, v214\n"
"v_mfma_f32_16x16x32_bf16 v[88:91], v[180:183], a[88:91], v[88:91]\n"
"s_barrier\n"
"ds_read_b64_tr_b16 v[190:191], v216 offset:256\n"
"v_add_u32 v216, 0xffff0880, v216\n"
"v_mfma_f32_16x16x32_bf16 v[92:95], v[180:183], a[92:95], v[92:95]\n"
"s_waitcnt lgkmcnt(2)\n"
"ds_read_b128 v[192:195], v218 offset:5312\n"
"v_cvt_pk_bf16_f32 v251, v50, v51\n"
"v_mfma_f32_16x16x32_bf16 v[108:111], v[184:187], a[92:95], v[108:111]\n"
"ds_read_b64_tr_b16 v[196:197], v217 offset:0\n"
"v_fma_f32 v240, v240, v225, v225\n"
"v_mfma_f32_16x16x32_bf16 v[104:107], v[184:187], a[88:91], v[104:107]\n"
"ds_read_b64_tr_b16 v[198:199], v217 offset:256\n"
"buffer_store_dwordx4 v[248:251], v139, s[44:47], s52 offen offset:0 sc0 sc1\n"
"v_mfma_f32_16x16x32_bf16 v[100:103], v[184:187], a[84:87], v[100:103]\n"
"s_add_u32 s52, s70, 0x200000\n"
"ds_read_b64_tr_b16 v[176:177], v214 offset:25344\n"
"v_add_u32 v217, 0xffff0880, v217\n"
"v_mfma_f32_16x16x32_bf16 v[96:99], v[184:187], a[80:83], v[96:99]\n"
"s_waitcnt lgkmcnt(4)\n"
"ds_read_b64_tr_b16 v[178:179], v214 offset:25600\n"
"v_fma_f32 v241, v241, v225, v225\n"
"v_mfma_f32_16x16x32_bf16 v[112:115], v[188:191], a[80:83], v[112:115]\n"
"buffer_load_dwordx4 v208, s[32:35], s66 offen lds\n"
"v_mfma_f32_16x16x32_bf16 v[116:119], v[188:191], a[84:87], v[116:119]\n"
"s_waitcnt lgkmcnt(4)\n"
"s_add_u32 s66, s61, 0x129000\n"
"v_rcp_f32 v240, v240\n"
"v_mfma_f32_16x16x32_bf16 v[120:123], v[188:191], v[192:195], v[120:123]\n"
"s_waitcnt lgkmcnt(2)\n"
"s_add_u32 m0, s59, 0\n"
"ds_read_b64_tr_b16 v[180:181], v214 offset:29568\n"
"v_mul_f32 v36, v36, v240\n"
"v_mfma_f32_16x16x32_bf16 v[124:127], v[196:199], v[192:195], v[124:127]\n"
"s_waitcnt lgkmcnt(1)\n"
"ds_read_b64_tr_b16 v[182:183], v214 offset:29824\n"
"v_mul_f32 v242, v221, v6\n"
"v_mfma_f32_16x16x32_bf16 v[76:79], v[176:179], a[108:111], v[76:79]\n"
"v_rcp_f32 v241, v241\n"
"v_mfma_f32_16x16x32_bf16 v[72:75], v[176:179], a[104:107], v[72:75]\n"
"buffer_load_dwordx4 v208, s[32:35], s66 offen lds\n"
"v_mfma_f32_16x16x32_bf16 v[68:71], v[176:179], a[100:103], v[68:71]\n"
"s_add_u32 s66, s61, 0x129480\n"
"s_add_u32 m0, s59, 0x1080\n"
"ds_read_b64_tr_b16 v[184:185], v214 offset:33792\n"
"v_mul_f32 v37, v37, v241\n"
"v_mfma_f32_16x16x32_bf16 v[64:67], v[176:179], a[96:99], v[64:67]\n"
"s_waitcnt lgkmcnt(1)\n"
"ds_read_b64_tr_b16 v[186:187], v214 offset:34048\n"
"v_cvt_pk_bf16_f32 v252, v36, v37\n"
"v_mfma_f32_16x16x32_bf16 v[80:83], v[180:183], a[96:99], v[80:83]\n"
"v_exp_f32 v242, v242\n"
"v_mfma_f32_16x16x32_bf16 v[84:87], v[180:183], a[100:103], v[84:87]\n"
"ds_read_b64_tr_b16 v[188:189], v216 offset:25344\n"
"v_fma_f32 v242, v242, v225, v225\n"
"v_mfma_f32_16x16x32_bf16 v[88:91], v[180:183], a[104:107], v[88:91]\n"
"ds_read_b64_tr_b16 v[190:191], v216 offset:25600\n"
"v_mul_f32 v38, v6, v38\n"
"v_mfma_f32_16x16x32_bf16 v[92:95], v[180:183], a[108:111], v[92:95]\n"
"s_waitcnt lgkmcnt(2)\n"
"ds_read_b128 v[192:195], v218 offset:6336\n"
"buffer_load_dwordx4 v208, s[32:35], s66 offen lds\n"
"v_mfma_f32_16x16x32_bf16 v[108:111], v[184:187], a[108:111], v[108:111]\n"
"s_add_u32 s66, s61, 0x129900\n"
"s_add_u32 m0, s59, 0x2100\n"
"ds_read_b64_tr_b16 v[196:197], v217 offset:25344\n"
"v_mul_f32 v243, v221, v7\n"
"v_mfma_f32_16x16x32_bf16 v[104:107], v[184:187], a[104:107], v[104:107]\n"
"ds_read_b64_tr_b16 v[198:199], v217 offset:25600\n"
"v_mul_f32 v39, v7, v39\n"
"v_mfma_f32_16x16x32_bf16 v[100:103], v[184:187], a[100:103], v[100:103]\n"
"ds_read_b64_tr_b16 v[176:177], v214 offset:38016\n"
"v_mul_f32 v244, v221, v20\n"
"v_mfma_f32_16x16x32_bf16 v[96:99], v[184:187], a[96:99], v[96:99]\n"
"s_waitcnt lgkmcnt(4)\n"
"ds_read_b64_tr_b16 v[178:179], v214 offset:38272\n"
"v_mul_f32 v52, v20, v52\n"
"v_mfma_f32_16x16x32_bf16 v[112:115], v[188:191], a[96:99], v[112:115]\n"
"buffer_load_dwordx4 v208, s[32:35], s66 offen lds\n"
"v_mfma_f32_16x16x32_bf16 v[116:119], v[188:191], a[100:103], v[116:119]\n"
"s_waitcnt lgkmcnt(4)\n"
"s_add_u32 s66, s61, 0x144000\n"
"v_rcp_f32 v242, v242\n"
"v_mfma_f32_16x16x32_bf16 v[120:123], v[188:191], v[192:195], v[120:123]\n"
"s_waitcnt lgkmcnt(2)\n"
"s_add_u32 m0, s59, 0x1bd80\n"
"ds_read_b64_tr_b16 v[180:181], v214 offset:42240\n"
"v_mul_f32 v38, v38, v242\n"
"v_mfma_f32_16x16x32_bf16 v[124:127], v[196:199], v[192:195], v[124:127]\n"
"s_waitcnt lgkmcnt(1)\n"
"ds_read_b64_tr_b16 v[182:183], v214 offset:42496\n"
"v_mul_f32 v245, v221, v21\n"
"v_mfma_f32_16x16x32_bf16 v[76:79], v[176:179], a[124:127], v[76:79]\n"
"buffer_load_dwordx4 v208, s[32:35], s66 offen lds\n"
"v_mfma_f32_16x16x32_bf16 v[72:75], v[176:179], a[120:123], v[72:75]\n"
"s_add_u32 s66, s61, 0x144480\n"
"s_add_u32 m0, s59, 0x1ce00\n"
"v_exp_f32 v243, v243\n"
"v_mfma_f32_16x16x32_bf16 v[68:71], v[176:179], a[116:119], v[68:71]\n"
"ds_read_b64_tr_b16 v[184:185], v214 offset:46464\n"
"v_fma_f32 v243, v243, v225, v225\n"
"v_mfma_f32_16x16x32_bf16 v[64:67], v[176:179], a[112:115], v[64:67]\n"
"s_waitcnt lgkmcnt(1)\n"
"ds_read_b64_tr_b16 v[186:187], v214 offset:46720\n"
"v_add_u32 v214, 0xf780, v214\n"
"v_mfma_f32_16x16x32_bf16 v[80:83], v[180:183], a[112:115], v[80:83]\n"
"v_rcp_f32 v243, v243\n"
"v_mfma_f32_16x16x32_bf16 v[84:87], v[180:183], a[116:119], v[84:87]\n"
"s_waitcnt vmcnt(8)\n"
"ds_read_b64_tr_b16 v[188:189], v216 offset:38016\n"
"v_mul_f32 v39, v39, v243\n"
"v_mfma_f32_16x16x32_bf16 v[88:91], v[180:183], a[120:123], v[88:91]\n"
"s_barrier\n"
"ds_read_b64_tr_b16 v[190:191], v216 offset:38272\n"
"buffer_load_dwordx4 v208, s[32:35], s66 offen lds\n"
"v_mfma_f32_16x16x32_bf16 v[92:95], v[180:183], a[124:127], v[92:95]\n"
"s_waitcnt lgkmcnt(2)\n"
"s_add_u32 s66, s61, 0x144900\n"
"ds_read_b128 v[192:195], v218 offset:7424\n"
"v_add_u32 v216, 0xf780, v216\n"
"v_mfma_f32_16x16x32_bf16 v[108:111], v[184:187], a[124:127], v[108:111]\n"
"s_add_u32 m0, s59, 0x1de80\n"
"ds_read_b64_tr_b16 v[196:197], v217 offset:38016\n"
"v_cvt_pk_bf16_f32 v253, v38, v39\n"
"v_mfma_f32_16x16x32_bf16 v[104:107], v[184:187], a[120:123], v[104:107]\n"
"ds_read_b64_tr_b16 v[198:199], v217 offset:38272\n"
"v_add_u32 v217, 0xf780, v217\n"
"v_mfma_f32_16x16x32_bf16 v[100:103], v[184:187], a[116:119], v[100:103]\n"
"ds_read_b64_tr_b16 v[176:177], v214 offset:12672\n"
"v_mul_f32 v53, v21, v53\n"
"v_mfma_f32_16x16x32_bf16 v[96:99], v[184:187], a[112:115], v[96:99]\n"
"s_waitcnt lgkmcnt(4)\n"
"ds_read_b64_tr_b16 v[178:179], v214 offset:12928\n"
"buffer_load_dwordx4 v208, s[32:35], s66 offen lds\n"
"v_mfma_f32_16x16x32_bf16 v[112:115], v[188:191], a[112:115], v[112:115]\n"
"s_add_u32 s66, s61, 0x15f000\n"
"s_add_u32 m0, s59, 0x3180\n"
"v_exp_f32 v244, v244\n"
"v_mfma_f32_16x16x32_bf16 v[116:119], v[188:191], a[116:119], v[116:119]\n"
"s_waitcnt lgkmcnt(4)\n"
"v_exp_f32 v245, v245\n"
"v_mfma_f32_16x16x32_bf16 v[120:123], v[188:191], v[192:195], v[120:123]\n"
"s_waitcnt lgkmcnt(2)\n"
"ds_read_b64_tr_b16 v[180:181], v214 offset:16896\n"
"v_fma_f32 v244, v244, v225, v225\n"
"v_mfma_f32_16x16x32_bf16 v[124:127], v[196:199], v[192:195], v[124:127]\n"
"s_waitcnt lgkmcnt(1)\n"
"ds_read_b64_tr_b16 v[182:183], v214 offset:17152\n"
"v_fma_f32 v245, v245, v225, v225\n"
"v_mfma_f32_16x16x32_bf16 v[76:79], v[176:179], a[140:143], v[76:79]\n"
"buffer_load_dwordx4 v208, s[32:35], s66 offen lds\n"
"v_mfma_f32_16x16x32_bf16 v[72:75], v[176:179], a[136:139], v[72:75]\n"
"s_add_u32 s66, s61, 0x15f480\n"
"s_add_u32 m0, s59, 0x4200\n"
"v_rcp_f32 v244, v244\n"
"v_mfma_f32_16x16x32_bf16 v[68:71], v[176:179], a[132:135], v[68:71]\n"
"ds_read_b64_tr_b16 v[184:185], v214 offset:21120\n"
"v_mul_f32 v52, v52, v244\n"
"v_mfma_f32_16x16x32_bf16 v[64:67], v[176:179], a[128:131], v[64:67]\n"
"s_waitcnt lgkmcnt(1)\n"
"ds_read_b64_tr_b16 v[186:187], v214 offset:21376\n"
"v_mul_f32 v246, v221, v22\n"
"v_mfma_f32_16x16x32_bf16 v[80:83], v[180:183], a[128:131], v[80:83]\n"
"buffer_load_dwordx4 v208, s[32:35], s66 offen lds\n"
"v_mfma_f32_16x16x32_bf16 v[84:87], v[180:183], a[132:135], v[84:87]\n"
"s_add_u32 s66, s61, 0x15f900\n"
"s_add_u32 m0, s59, 0x5280\n"
"ds_read_b64_tr_b16 v[188:189], v216 offset:12672\n"
"v_mul_f32 v54, v22, v54\n"
"v_mfma_f32_16x16x32_bf16 v[88:91], v[180:183], a[136:139], v[88:91]\n"
"ds_read_b64_tr_b16 v[190:191], v216 offset:12928\n"
"v_mul_f32 v247, v221, v23\n"
"v_mfma_f32_16x16x32_bf16 v[92:95], v[180:183], a[140:143], v[92:95]\n"
"s_waitcnt lgkmcnt(2)\n"
"ds_read_b128 v[192:195], v218 offset:8448\n"
"v_mul_f32 v55, v23, v55\n"
"v_mfma_f32_16x16x32_bf16 v[108:111], v[184:187], a[140:143], v[108:111]\n"
"ds_read_b64_tr_b16 v[196:197], v217 offset:12672\n"
"v_mul_f32 v240, v222, v8\n"
"v_mfma_f32_16x16x32_bf16 v[104:107], v[184:187], a[136:139], v[104:107]\n"
"ds_read_b64_tr_b16 v[198:199], v217 offset:12928\n"
"v_mul_f32 v40, v8, v40\n"
"v_mfma_f32_16x16x32_bf16 v[100:103], v[184:187], a[132:135], v[100:103]\n"
"ds_read_b64_tr_b16 v[176:177], v214 offset:25344\n"
"buffer_load_dwordx4 v208, s[32:35], s66 offen lds\n"
"v_mfma_f32_16x16x32_bf16 v[96:99], v[184:187], a[128:131], v[96:99]\n"
"s_waitcnt lgkmcnt(4)\n"
"s_add_u32 s66, s61, 0x17a000\n"
"ds_read_b64_tr_b16 v[178:179], v214 offset:25600\n"
"v_mul_f32 v241, v222, v9\n"
"v_mfma_f32_16x16x32_bf16 v[112:115], v[188:191], a[128:131], v[112:115]\n"
"s_add_u32 m0, s59, 0xc600\n"
"v_rcp_f32 v245, v245\n"
"v_mfma_f32_16x16x32_bf16 v[116:119], v[188:191], a[132:135], v[116:119]\n"
"s_waitcnt lgkmcnt(4)\n"
"v_exp_f32 v246, v246\n"
"v_mfma_f32_16x16x32_bf16 v[120:123], v[188:191], v[192:195], v[120:123]\n"
"s_waitcnt lgkmcnt(2)\n"
"ds_read_b64_tr_b16 v[180:181], v214 offset:29568\n"
"v_mul_f32 v53, v53, v245\n"
"v_mfma_f32_16x16x32_bf16 v[124:127], v[196:199], v[192:195], v[124:127]\n"
"s_waitcnt lgkmcnt(1)\n"
"ds_read_b64_tr_b16 v[182:183], v214 offset:29824\n"
"buffer_load_dwordx4 v208, s[32:35], s66 offen lds\n"
"v_mfma_f32_16x16x32_bf16 v[76:79], v[176:179], a[156:159], v[76:79]\n"
"s_add_u32 s66, s61, 0x17a480\n"
"s_add_u32 m0, s59, 0xd680\n"
"v_exp_f32 v247, v247\n"
"v_mfma_f32_16x16x32_bf16 v[72:75], v[176:179], a[152:155], v[72:75]\n"
"v_exp_f32 v240, v240\n"
"v_mfma_f32_16x16x32_bf16 v[68:71], v[176:179], a[148:151], v[68:71]\n"
"ds_read_b64_tr_b16 v[184:185], v214 offset:33792\n"
"v_cvt_pk_bf16_f32 v254, v52, v53\n"
"v_mfma_f32_16x16x32_bf16 v[64:67], v[176:179], a[144:147], v[64:67]\n"
"s_waitcnt lgkmcnt(1)\n"
"ds_read_b64_tr_b16 v[186:187], v214 offset:34048\n"
"v_fma_f32 v246, v246, v225, v225\n"
"v_mfma_f32_16x16x32_bf16 v[80:83], v[180:183], a[144:147], v[80:83]\n"
"buffer_load_dwordx4 v208, s[32:35], s66 offen lds\n"
"v_mfma_f32_16x16x32_bf16 v[84:87], v[180:183], a[148:151], v[84:87]\n"
"s_waitcnt vmcnt(8)\n"
"s_add_u32 s66, s61, 0x17a900\n"
"ds_read_b64_tr_b16 v[188:189], v216 offset:25344\n"
"v_fma_f32 v247, v247, v225, v225\n"
"v_mfma_f32_16x16x32_bf16 v[88:91], v[180:183], a[152:155], v[88:91]\n"
"s_barrier\n"
"s_add_u32 m0, s59, 0xe700\n"
"ds_read_b64_tr_b16 v[190:191], v216 offset:25600\n"
"v_fma_f32 v240, v240, v226, v226\n"
"v_mfma_f32_16x16x32_bf16 v[92:95], v[180:183], a[156:159], v[92:95]\n"
"s_waitcnt lgkmcnt(2)\n"
"ds_read_b128 v[192:195], v218 offset:9536\n"
"v_mul_f32 v41, v9, v41\n"
"v_mfma_f32_16x16x32_bf16 v[108:111], v[184:187], a[156:159], v[108:111]\n"
"ds_read_b64_tr_b16 v[196:197], v217 offset:25344\n"
"v_mul_f32 v242, v222, v10\n"
"v_mfma_f32_16x16x32_bf16 v[104:107], v[184:187], a[152:155], v[104:107]\n"
"ds_read_b64_tr_b16 v[198:199], v217 offset:25600\n"
"buffer_load_dwordx4 v208, s[32:35], s66 offen lds\n"
"v_mfma_f32_16x16x32_bf16 v[100:103], v[184:187], a[148:151], v[100:103]\n"
"s_add_u32 s66, s61, 0x195000\n"
"s_add_u32 m0, s59, 0x6300\n"
"ds_read_b64_tr_b16 v[176:177], v214 offset:38016\n"
"v_lshlrev_b32 v140, 3, v233\n"
"v_mfma_f32_16x16x32_bf16 v[96:99], v[184:187], a[144:147], v[96:99]\n"
"s_waitcnt lgkmcnt(4)\n"
"ds_read_b64_tr_b16 v[178:179], v214 offset:38272\n"
"v_lshlrev_b32 v141, 2, v233\n"
"v_mfma_f32_16x16x32_bf16 v[112:115], v[188:191], a[144:147], v[112:115]\n"
"v_rcp_f32 v246, v246\n"
"v_mfma_f32_16x16x32_bf16 v[116:119], v[188:191], a[148:151], v[116:119]\n"
"s_waitcnt lgkmcnt(4)\n"
"buffer_load_dwordx4 v208, s[32:35], s66 offen lds\n"
"v_mfma_f32_16x16x32_bf16 v[120:123], v[188:191], v[192:195], v[120:123]\n"
"s_waitcnt lgkmcnt(2)\n"
"s_add_u32 s66, s61, 0x195480\n"
"ds_read_b64_tr_b16 v[180:181], v214 offset:42240\n"
"v_add_u32 v203, s67, v203\n"
"v_mfma_f32_16x16x32_bf16 v[124:127], v[196:199], v[192:195], v[124:127]\n"
"s_waitcnt lgkmcnt(1)\n"
"s_add_u32 m0, s59, 0x7380\n"
"ds_read_b64_tr_b16 v[182:183], v214 offset:42496\n"
"v_add_u32 v204, s67, v204\n"
"v_mfma_f32_16x16x32_bf16 v[76:79], v[176:179], a[172:175], v[76:79]\n"
"v_rcp_f32 v247, v247\n"
"v_mfma_f32_16x16x32_bf16 v[72:75], v[176:179], a[168:171], v[72:75]\n"
"v_rcp_f32 v240, v240\n"
"v_mfma_f32_16x16x32_bf16 v[68:71], v[176:179], a[164:167], v[68:71]\n"
"ds_read_b64_tr_b16 v[184:185], v214 offset:46464\n"
"v_mul_f32 v54, v54, v246\n"
"v_mfma_f32_16x16x32_bf16 v[64:67], v[176:179], a[160:163], v[64:67]\n"
"s_waitcnt lgkmcnt(1)\n"
"ds_read_b64_tr_b16 v[186:187], v214 offset:46720\n"
"buffer_load_dwordx4 v208, s[32:35], s66 offen lds\n"
"v_mfma_f32_16x16x32_bf16 v[80:83], v[180:183], a[160:163], v[80:83]\n"
"s_add_u32 s66, s61, 0x195900\n"
"s_add_u32 m0, s59, 0x8400\n"
"v_exp_f32 v241, v241\n"
"v_mfma_f32_16x16x32_bf16 v[84:87], v[180:183], a[164:167], v[84:87]\n"
"ds_read_b64_tr_b16 v[188:189], v216 offset:38016\n"
"v_add_u32 v214, 0xffff0880, v214\n"
"v_mfma_f32_16x16x32_bf16 v[88:91], v[180:183], a[168:171], v[88:91]\n"
"ds_read_b64_tr_b16 v[190:191], v216 offset:38272\n"
"v_add_u32 v216, 0xffff0880, v216\n"
"v_mfma_f32_16x16x32_bf16 v[92:95], v[180:183], a[172:175], v[92:95]\n"
"s_waitcnt lgkmcnt(2)\n"
"ds_read_b128 v[192:195], v218 offset:10560\n"
"v_mul_f32 v55, v55, v247\n"
"v_mfma_f32_16x16x32_bf16 v[108:111], v[184:187], a[172:175], v[108:111]\n"
"ds_read_b64_tr_b16 v[196:197], v217 offset:38016\n"
"buffer_load_dwordx4 v208, s[32:35], s66 offen lds\n"
"v_mfma_f32_16x16x32_bf16 v[104:107], v[184:187], a[168:171], v[104:107]\n"
"s_add_u32 s66, s61, 0x1b0000\n"
"s_add_u32 m0, s59, 0xf780\n"
"ds_read_b64_tr_b16 v[198:199], v217 offset:38272\n"
"v_add_u32 v217, 0xffff0880, v217\n"
"v_mfma_f32_16x16x32_bf16 v[100:103], v[184:187], a[164:167], v[100:103]\n"
"ds_read_b64_tr_b16 v[176:177], v214 offset:0\n"
"v_cvt_pk_bf16_f32 v255, v54, v55\n"
"v_mfma_f32_16x16x32_bf16 v[96:99], v[184:187], a[160:163], v[96:99]\n"
"s_waitcnt lgkmcnt(4)\n"
"ds_read_b64_tr_b16 v[178:179], v214 offset:256\n"
"v_mul_f32 v40, v40, v240\n"
"v_mfma_f32_16x16x32_bf16 v[112:115], v[188:191], a[160:163], v[112:115]\n"
"buffer_store_dwordx4 v[252:255], v139, s[44:47], s52 offen offset:0 sc0 sc1\n"
"v_fma_f32 v241, v241, v226, v226\n"
"v_mfma_f32_16x16x32_bf16 v[116:119], v[188:191], a[164:167], v[116:119]\n"
"s_waitcnt lgkmcnt(4)\n"
"v_rcp_f32 v241, v241\n"
"v_mfma_f32_16x16x32_bf16 v[120:123], v[188:191], v[192:195], v[120:123]\n"
"s_waitcnt lgkmcnt(2)\n"
"ds_read_b64_tr_b16 v[180:181], v214 offset:4224\n"
"v_mul_f32 v41, v41, v241\n"
"v_mfma_f32_16x16x32_bf16 v[124:127], v[196:199], v[192:195], v[124:127]\n"
"s_waitcnt lgkmcnt(1)\n"
"ds_read_b64_tr_b16 v[182:183], v214 offset:4480\n"
"v_cvt_pk_bf16_f32 v248, v40, v41\n"
"v_mfma_f32_16x16x32_bf16 v[76:79], v[176:179], a[188:191], v[76:79]\n"
"s_add_u32 s52, s71, 0\n"
"buffer_load_dwordx4 v208, s[32:35], s66 offen lds\n"
"v_mfma_f32_16x16x32_bf16 v[72:75], v[176:179], a[184:187], v[72:75]\n"
"s_add_u32 s66, s61, 0x1b0480\n"
"s_add_u32 m0, s59, 0x10800\n"
"v_exp_f32 v242, v242\n"
"v_mfma_f32_16x16x32_bf16 v[68:71], v[176:179], a[180:183], v[68:71]\n"
"ds_read_b64_tr_b16 v[184:185], v214 offset:8448\n"
"v_fma_f32 v242, v242, v226, v226\n"
"v_mfma_f32_16x16x32_bf16 v[64:67], v[176:179], a[176:179], v[64:67]\n"
"s_waitcnt lgkmcnt(1)\n"
"ds_read_b64_tr_b16 v[186:187], v214 offset:8704\n"
"v_add_u32 v214, 0xf780, v214\n"
"v_mfma_f32_16x16x32_bf16 v[80:83], v[180:183], a[176:179], v[80:83]\n"
"v_rcp_f32 v242, v242\n"
"v_mfma_f32_16x16x32_bf16 v[84:87], v[180:183], a[180:183], v[84:87]\n"
"s_waitcnt vmcnt(8)\n"
"ds_read_b64_tr_b16 v[188:189], v216 offset:0\n"
"v_mul_f32 v42, v10, v42\n"
"v_mfma_f32_16x16x32_bf16 v[88:91], v[180:183], a[184:187], v[88:91]\n"
"s_barrier\n"
"ds_read_b64_tr_b16 v[190:191], v216 offset:256\n"
"buffer_load_dwordx4 v208, s[32:35], s66 offen lds\n"
"v_mfma_f32_16x16x32_bf16 v[92:95], v[180:183], a[188:191], v[92:95]\n"
"s_waitcnt lgkmcnt(2)\n"
"s_add_u32 s66, s61, 0x1b0900\n"
"ds_read_b128 v[192:195], v218 offset:11648\n"
"v_add_u32 v216, 0xf780, v216\n"
"v_mfma_f32_16x16x32_bf16 v[108:111], v[184:187], a[188:191], v[108:111]\n"
"s_add_u32 m0, s59, 0x11880\n"
"ds_read_b64_tr_b16 v[196:197], v217 offset:0\n"
"v_mul_f32 v42, v42, v242\n"
"v_mfma_f32_16x16x32_bf16 v[104:107], v[184:187], a[184:187], v[104:107]\n"
"ds_read_b64_tr_b16 v[198:199], v217 offset:256\n"
"v_add_u32 v217, 0xf780, v217\n"
"v_mfma_f32_16x16x32_bf16 v[100:103], v[184:187], a[180:183], v[100:103]\n"
"ds_read_b64_tr_b16 v[176:177], v214 offset:50688\n"
"v_mul_f32 v243, v222, v11\n"
"v_mfma_f32_16x16x32_bf16 v[96:99], v[184:187], a[176:179], v[96:99]\n"
"s_waitcnt lgkmcnt(4)\n"
"ds_read_b64_tr_b16 v[178:179], v214 offset:50944\n"
"buffer_load_dwordx4 v208, s[32:35], s66 offen lds\n"
"v_mfma_f32_16x16x32_bf16 v[112:115], v[188:191], a[176:179], v[112:115]\n"
"s_add_u32 s66, s61, 0x1cb000\n"
"s_add_u32 m0, s59, 0x9480\n"
"v_exp_f32 v243, v243\n"
"v_mfma_f32_16x16x32_bf16 v[116:119], v[188:191], a[180:183], v[116:119]\n"
"s_waitcnt lgkmcnt(4)\n"
"ds_read_b64_tr_b16 v[180:181], v214 offset:54912\n"
"v_fma_f32 v243, v243, v226, v226\n"
"v_mfma_f32_16x16x32_bf16 v[120:123], v[188:191], v[192:195], v[120:123]\n"
"s_waitcnt lgkmcnt(3)\n"
"v_rcp_f32 v243, v243\n"
"v_mfma_f32_16x16x32_bf16 v[124:127], v[196:199], v[192:195], v[124:127]\n"
"s_waitcnt lgkmcnt(1)\n"
"ds_read_b64_tr_b16 v[182:183], v214 offset:55168\n"
"v_mul_f32 v43, v11, v43\n"
"v_mfma_f32_16x16x32_bf16 v[76:79], v[176:179], a[204:207], v[76:79]\n"
"buffer_load_dwordx4 v208, s[32:35], s66 offen lds\n"
"v_mfma_f32_16x16x32_bf16 v[72:75], v[176:179], a[200:203], v[72:75]\n"
"s_add_u32 s66, s61, 0x1cb480\n"
"s_add_u32 m0, s59, 0xa500\n"
"ds_read_b64_tr_b16 v[184:185], v214 offset:59136\n"
"v_mul_f32 v43, v43, v243\n"
"v_mfma_f32_16x16x32_bf16 v[68:71], v[176:179], a[196:199], v[68:71]\n"
"ds_read_b64_tr_b16 v[186:187], v214 offset:59392\n"
"v_add_u32 v214, 0xffff0880, v214\n"
"v_mfma_f32_16x16x32_bf16 v[64:67], v[176:179], a[192:195], v[64:67]\n"
"s_waitcnt lgkmcnt(2)\n"
"ds_read_b64_tr_b16 v[188:189], v216 offset:50688\n"
"v_cvt_pk_bf16_f32 v249, v42, v43\n"
"v_mfma_f32_16x16x32_bf16 v[80:83], v[180:183], a[192:195], v[80:83]\n"
"buffer_load_dwordx4 v208, s[32:35], s66 offen lds\n"
"v_mfma_f32_16x16x32_bf16 v[84:87], v[180:183], a[196:199], v[84:87]\n"
"s_add_u32 s66, s61, 0x1cb900\n"
"s_add_u32 m0, s59, 0xb580\n"
"ds_read_b64_tr_b16 v[190:191], v216 offset:50944\n"
"v_add_u32 v216, 0xffff0880, v216\n"
"v_mfma_f32_16x16x32_bf16 v[88:91], v[180:183], a[200:203], v[88:91]\n"
"ds_read_b128 v[192:195], v218 offset:12672\n"
"v_mul_f32 v244, v222, v24\n"
"v_mfma_f32_16x16x32_bf16 v[92:95], v[180:183], a[204:207], v[92:95]\n"
"s_waitcnt lgkmcnt(3)\n"
"v_exp_f32 v244, v244\n"
"v_mfma_f32_16x16x32_bf16 v[108:111], v[184:187], a[204:207], v[108:111]\n"
"ds_read_b64_tr_b16 v[196:197], v217 offset:50688\n"
"v_fma_f32 v244, v244, v226, v226\n"
"v_mfma_f32_16x16x32_bf16 v[104:107], v[184:187], a[200:203], v[104:107]\n"
"ds_read_b64_tr_b16 v[198:199], v217 offset:50944\n"
"v_add_u32 v217, 0xffff0880, v217\n"
"v_mfma_f32_16x16x32_bf16 v[100:103], v[184:187], a[196:199], v[100:103]\n"
"ds_read_b64_tr_b16 v[176:177], v214 offset:12672\n"
"buffer_load_dwordx4 v208, s[32:35], s66 offen lds\n"
"v_mfma_f32_16x16x32_bf16 v[96:99], v[184:187], a[192:195], v[96:99]\n"
"s_waitcnt lgkmcnt(4)\n"
"s_add_u32 s66, s60, 0x80\n"
"ds_read_b64_tr_b16 v[178:179], v214 offset:12928\n"
"v_mul_f32 v56, v24, v56\n"
"v_mfma_f32_16x16x32_bf16 v[112:115], v[188:191], a[192:195], v[112:115]\n"
"s_add_u32 m0, s59, 0x1ef00\n"
"v_rcp_f32 v244, v244\n"
"v_mfma_f32_16x16x32_bf16 v[116:119], v[188:191], a[196:199], v[116:119]\n"
"s_waitcnt lgkmcnt(4)\n"
"ds_read_b64_tr_b16 v[180:181], v214 offset:16896\n"
"v_mul_f32 v56, v56, v244\n"
"v_mfma_f32_16x16x32_bf16 v[120:123], v[188:191], v[192:195], v[120:123]\n"
"s_waitcnt lgkmcnt(3)\n"
"s_mov_b32 s32, 0xf0f0f0f\n"
"buffer_load_dwordx4 v207, s[24:27], s66 offen lds\n"
"v_mfma_f32_16x16x32_bf16 v[124:127], v[196:199], v[192:195], v[124:127]\n"
"s_waitcnt lgkmcnt(1)\n"
"s_add_u32 m0, s59, 0x1ff80\n"
"ds_read_b64_tr_b16 v[182:183], v214 offset:17152\n"
"v_mul_f32 v245, v222, v25\n"
"v_mfma_f32_16x16x32_bf16 v[76:79], v[176:179], a[220:223], v[76:79]\n"
"s_mov_b32 s33, 0xf0f0f0f\n"
"v_exp_f32 v245, v245\n"
"v_mfma_f32_16x16x32_bf16 v[72:75], v[176:179], a[216:219], v[72:75]\n"
"s_cmp_eq_u32 s58, 0\n s_cselect_b64 s[32:33], s[24:25], s[28:29]\n s_mov_b32 s34, s26\n"
"ds_read_b64_tr_b16 v[184:185], v214 offset:21120\n"
"v_fma_f32 v245, v245, v226, v226\n"
"v_mfma_f32_16x16x32_bf16 v[68:71], v[176:179], a[212:215], v[68:71]\n"
"v_rcp_f32 v245, v245\n"
"v_mfma_f32_16x16x32_bf16 v[64:67], v[176:179], a[208:211], v[64:67]\n"
"s_waitcnt lgkmcnt(1)\n"
"ds_read_b64_tr_b16 v[186:187], v214 offset:21376\n"
"v_mul_f32 v57, v25, v57\n"
"v_mfma_f32_16x16x32_bf16 v[80:83], v[180:183], a[208:211], v[80:83]\n"
"buffer_load_dwordx4 v207, s[28:31], s66 offen lds\n"
"v_mfma_f32_16x16x32_bf16 v[84:87], v[180:183], a[212:215], v[84:87]\n"
"s_waitcnt vmcnt(9)\n"
"s_add_u32 s66, s60, 0x48080\n"
"ds_read_b64_tr_b16 v[188:189], v216 offset:12672\n"
"v_mul_f32 v57, v57, v245\n"
"v_mfma_f32_16x16x32_bf16 v[88:91], v[180:183], a[216:219], v[88:91]\n"
"s_barrier\n"
"s_add_u32 m0, s59, 0x21000\n"
"ds_read_b64_tr_b16 v[190:191], v216 offset:12928\n"
"v_cvt_pk_bf16_f32 v250, v56, v57\n"
"v_mfma_f32_16x16x32_bf16 v[92:95], v[180:183], a[220:223], v[92:95]\n"
"s_waitcnt lgkmcnt(2)\n"
"ds_read_b128 v[192:195], v218 offset:13760\n"
"v_mul_f32 v246, v222, v26\n"
"v_mfma_f32_16x16x32_bf16 v[108:111], v[184:187], a[220:223], v[108:111]\n"
"ds_read_b64_tr_b16 v[196:197], v217 offset:12672\n"
"v_mul_f32 v58, v26, v58\n"
"v_mfma_f32_16x16x32_bf16 v[104:107], v[184:187], a[216:219], v[104:107]\n"
"ds_read_b64_tr_b16 v[198:199], v217 offset:12928\n"
"v_mul_f32 v247, v222, v27\n"
"v_mfma_f32_16x16x32_bf16 v[100:103], v[184:187], a[212:215], v[100:103]\n"
"ds_read_b64_tr_b16 v[176:177], v214 offset:50688\n"
"buffer_load_dwordx4 v207, s[24:27], s66 offen lds\n"
"v_mfma_f32_16x16x32_bf16 v[96:99], v[184:187], a[208:211], v[96:99]\n"
"s_waitcnt lgkmcnt(4)\n"
"s_add_u32 m0, s59, 0x22080\n"
"ds_read_b64_tr_b16 v[178:179], v214 offset:50944\n"
"v_mul_f32 v59, v27, v59\n"
"v_mfma_f32_16x16x32_bf16 v[112:115], v[188:191], a[208:211], v[112:115]\n"
"v_exp_f32 v246, v246\n"
"v_mfma_f32_16x16x32_bf16 v[116:119], v[188:191], a[212:215], v[116:119]\n"
"s_waitcnt lgkmcnt(4)\n"
"v_exp_f32 v247, v247\n"
"v_mfma_f32_16x16x32_bf16 v[120:123], v[188:191], v[192:195], v[120:123]\n"
"s_waitcnt lgkmcnt(2)\n"
"ds_read_b64_tr_b16 v[180:181], v214 offset:54912\n"
"v_fma_f32 v246, v246, v226, v226\n"
"v_mfma_f32_16x16x32_bf16 v[124:127], v[196:199], v[192:195], v[124:127]\n"
"s_waitcnt lgkmcnt(1)\n"
"ds_read_b64_tr_b16 v[182:183], v214 offset:55168\n"
"buffer_load_dwordx4 v207, s[28:31], s66 offen lds\n"
"v_mfma_f32_16x16x32_bf16 v[76:79], v[176:179], a[236:239], v[76:79]\n"
"s_add_u32 s66, s60, 0x90080\n"
"s_add_u32 m0, s59, 0x12900\n"
"v_rcp_f32 v246, v246\n"
"v_mfma_f32_16x16x32_bf16 v[72:75], v[176:179], a[232:235], v[72:75]\n"
"ds_read_b64_tr_b16 v[184:185], v214 offset:59136\n"
"v_mul_f32 v58, v58, v246\n"
"v_mfma_f32_16x16x32_bf16 v[68:71], v[176:179], a[228:231], v[68:71]\n"
"ds_read_b64_tr_b16 v[186:187], v214 offset:59392\n"
"v_fma_f32 v247, v247, v226, v226\n"
"v_mfma_f32_16x16x32_bf16 v[64:67], v[176:179], a[224:227], v[64:67]\n"
"s_waitcnt lgkmcnt(2)\n"
"buffer_load_dwordx4 v207, s[24:27], s66 offen lds\n"
"v_mfma_f32_16x16x32_bf16 v[80:83], v[180:183], a[224:227], v[80:83]\n"
"s_add_u32 m0, s59, 0x13980\n"
"v_rcp_f32 v247, v247\n"
"v_mfma_f32_16x16x32_bf16 v[84:87], v[180:183], a[228:231], v[84:87]\n"
"ds_read_b64_tr_b16 v[188:189], v216 offset:50688\n"
"v_mul_f32 v59, v59, v247\n"
"v_mfma_f32_16x16x32_bf16 v[88:91], v[180:183], a[232:235], v[88:91]\n"
"ds_read_b64_tr_b16 v[190:191], v216 offset:50944\n"
"v_cvt_pk_bf16_f32 v251, v58, v59\n"
"v_mfma_f32_16x16x32_bf16 v[92:95], v[180:183], a[236:239], v[92:95]\n"
"s_waitcnt lgkmcnt(2)\n"
"ds_read_b128 v[192:195], v218 offset:14784\n"
"v_mul_f32 v240, v223, v12\n"
"v_mfma_f32_16x16x32_bf16 v[108:111], v[184:187], a[236:239], v[108:111]\n"
"L_st3_%=:\n s_cbranch_execz L_st7_%=\n"
"ds_read_b64_tr_b16 v[196:197], v217 offset:50688\n"
"v_mul_f32 v44, v12, v44\n"
"v_mfma_f32_16x16x32_bf16 v[104:107], v[184:187], a[232:235], v[104:107]\n"
"ds_read_b64_tr_b16 v[198:199], v217 offset:50944\n"
"v_mul_f32 v241, v223, v13\n"
"v_mfma_f32_16x16x32_bf16 v[100:103], v[184:187], a[228:231], v[100:103]\n"
"ds_read_b64_tr_b16 v[176:177], v214 offset:25344\n"
"buffer_store_dwordx4 v[248:251], v139, s[44:47], s52 offen offset:0 sc0 sc1\n"
"v_mfma_f32_16x16x32_bf16 v[96:99], v[184:187], a[224:227], v[96:99]\n"
"s_waitcnt lgkmcnt(4)\n"
"ds_read_b64_tr_b16 v[178:179], v214 offset:25600\n"
"v_mul_f32 v45, v13, v45\n"
"v_mfma_f32_16x16x32_bf16 v[112:115], v[188:191], a[224:227], v[112:115]\n"
"s_add_u32 s52, s71, 0x200000\n"
"v_exp_f32 v240, v240\n"
"v_mfma_f32_16x16x32_bf16 v[116:119], v[188:191], a[228:231], v[116:119]\n"
"s_waitcnt lgkmcnt(4)\n"
"buffer_load_dwordx4 v207, s[28:31], s66 offen lds\n"
"v_mfma_f32_16x16x32_bf16 v[120:123], v[188:191], v[192:195], v[120:123]\n"
"s_waitcnt lgkmcnt(2)\n"
"s_add_u32 s66, s60, 0xd8080\n"
"ds_read_b64_tr_b16 v[180:181], v214 offset:29568\n"
"v_fma_f32 v240, v240, v227, v227\n"
"v_mfma_f32_16x16x32_bf16 v[124:127], v[196:199], v[192:195], v[124:127]\n"
"s_waitcnt lgkmcnt(1)\n"
"s_add_u32 m0, s59, 0x14a00\n"
"ds_read_b64_tr_b16 v[182:183], v214 offset:29824\n"
"v_mul_f32 v242, v223, v14\n"
"v_mfma_f32_16x16x32_bf16 v[76:79], v[176:179], a[252:255], v[76:79]\n"
"v_rcp_f32 v240, v240\n"
"v_mfma_f32_16x16x32_bf16 v[72:75], v[176:179], a[248:251], v[72:75]\n"
"v_exp_f32 v241, v241\n"
"v_mfma_f32_16x16x32_bf16 v[68:71], v[176:179], a[244:247], v[68:71]\n"
"ds_read_b64_tr_b16 v[184:185], v214 offset:33792\n"
"v_mul_f32 v46, v14, v46\n"
"v_mfma_f32_16x16x32_bf16 v[64:67], v[176:179], a[240:243], v[64:67]\n"
"s_waitcnt lgkmcnt(1)\n"
"ds_read_b64_tr_b16 v[186:187], v214 offset:34048\n"
"v_add_u32 v214, 0xf780, v214\n"
"v_mfma_f32_16x16x32_bf16 v[80:83], v[180:183], a[240:243], v[80:83]\n"
"buffer_load_dwordx4 v207, s[24:27], s66 offen lds\n"
"v_mfma_f32_16x16x32_bf16 v[84:87], v[180:183], a[244:247], v[84:87]\n"
"s_waitcnt vmcnt(8)\n"
"s_add_u32 m0, s59, 0x15a80\n"
"ds_read_b64_tr_b16 v[188:189], v216 offset:25344\n"
"v_mul_f32 v243, v223, v15\n"
"v_mfma_f32_16x16x32_bf16 v[88:91], v[180:183], a[248:251], v[88:91]\n"
"s_barrier\n"
"ds_read_b64_tr_b16 v[190:191], v216 offset:25600\n"
"v_add_u32 v216, 0xf780, v216\n"
"v_mfma_f32_16x16x32_bf16 v[92:95], v[180:183], a[252:255], v[92:95]\n"
"s_waitcnt lgkmcnt(2)\n"
"ds_read_b128 v[192:195], v218 offset:15872\n"
"v_mul_f32 v47, v15, v47\n"
"v_mfma_f32_16x16x32_bf16 v[108:111], v[184:187], a[252:255], v[108:111]\n"
"ds_read_b64_tr_b16 v[196:197], v217 offset:25344\n"
"v_mul_f32 v244, v223, v28\n"
"v_mfma_f32_16x16x32_bf16 v[104:107], v[184:187], a[248:251], v[104:107]\n"
"ds_read_b64_tr_b16 v[198:199], v217 offset:25600\n"
"buffer_load_dwordx4 v207, s[28:31], s66 offen lds\n"
"v_mfma_f32_16x16x32_bf16 v[100:103], v[184:187], a[244:247], v[100:103]\n"
"s_add_u32 s66, s60, 0x120080\n"
"s_add_u32 m0, s59, 0x16b00\n"
"ds_read_b64_tr_b16 v[176:177], v214 offset:0\n"
"v_add_u32 v217, 0xf780, v217\n"
"v_mfma_f32_16x16x32_bf16 v[96:99], v[184:187], a[240:243], v[96:99]\n"
"s_waitcnt lgkmcnt(4)\n"
"ds_read_b64_tr_b16 v[178:179], v214 offset:256\n"
"v_mul_f32 v60, v28, v60\n"
"v_mfma_f32_16x16x32_bf16 v[112:115], v[188:191], a[240:243], v[112:115]\n"
"v_exp_f32 v242, v242\n"
"v_mfma_f32_16x16x32_bf16 v[116:119], v[188:191], a[244:247], v[116:119]\n"
"s_waitcnt lgkmcnt(4)\n"
"buffer_load_dwordx4 v207, s[24:27], s66 offen lds\n"
"v_mfma_f32_16x16x32_bf16 v[120:123], v[188:191], v[192:195], v[120:123]\n"
"s_waitcnt lgkmcnt(2)\n"
"s_add_u32 m0, s59, 0x17b80\n"
"ds_read_b64_tr_b16 v[180:181], v214 offset:4224\n"
"v_mul_f32 v245, v223, v29\n"
"v_mfma_f32_16x16x32_bf16 v[124:127], v[196:199], v[192:195], v[124:127]\n"
"s_waitcnt lgkmcnt(1)\n"
"ds_read_b64_tr_b16 v[182:183], v214 offset:4480\n"
"v_mul_f32 v61, v29, v61\n"
"v_mfma_f32_16x16x32_bf16 v[76:79], v[176:179], v[156:159], v[76:79]\n"
"v_exp_f32 v243, v243\n"
"v_mfma_f32_16x16x32_bf16 v[72:75], v[176:179], v[152:155], v[72:75]\n"
"v_exp_f32 v244, v244\n"
"v_mfma_f32_16x16x32_bf16 v[68:71], v[176:179], v[148:151], v[68:71]\n"
"ds_read_b64_tr_b16 v[184:185], v214 offset:8448\n"
"v_mul_f32 v246, v223, v30\n"
"v_mfma_f32_16x16x32_bf16 v[64:67], v[176:179], v[144:147], v[64:67]\n"
"s_waitcnt lgkmcnt(1)\n"
"ds_read_b64_tr_b16 v[186:187], v214 offset:8704\n"
"buffer_load_dwordx4 v207, s[28:31], s66 offen lds\n"
"v_mfma_f32_16x16x32_bf16 v[80:83], v[180:183], v[144:147], v[80:83]\n"
"s_add_u32 s66, s60, 0x168080\n"
"s_add_u32 m0, s59, 0\n"
"v_exp_f32 v245, v245\n"
"v_mfma_f32_16x16x32_bf16 v[84:87], v[180:183], v[148:151], v[84:87]\n"
"ds_read_b64_tr_b16 v[188:189], v216 offset:0\n"
"v_add_u32 v214, 0xffff0880, v214\n"
"v_mfma_f32_16x16x32_bf16 v[88:91], v[180:183], v[152:155], v[88:91]\n"
"ds_read_b64_tr_b16 v[190:191], v216 offset:256\n"
"v_add_u32 v216, 0xffff0880, v216\n"
"v_mfma_f32_16x16x32_bf16 v[92:95], v[180:183], v[156:159], v[92:95]\n"
"s_waitcnt lgkmcnt(2)\n"
"ds_read_b128 v[192:195], v218 offset:16896\n"
"v_mul_f32 v62, v30, v62\n"
"v_mfma_f32_16x16x32_bf16 v[108:111], v[184:187], v[156:159], v[108:111]\n"
"ds_read_b64_tr_b16 v[196:197], v217 offset:0\n"
"buffer_load_dwordx4 v207, s[24:27], s66 offen lds\n"
"v_mfma_f32_16x16x32_bf16 v[104:107], v[184:187], v[152:155], v[104:107]\n"
"s_add_u32 m0, s59, 0x1080\n"
"ds_read_b64_tr_b16 v[198:199], v217 offset:256\n"
"v_add_u32 v217, 0xffff0880, v217\n"
"v_mfma_f32_16x16x32_bf16 v[100:103], v[184:187], v[148:151], v[100:103]\n"
"ds_read_b64_tr_b16 v[176:177], v214 offset:38016\n"
"v_mul_f32 v247, v223, v31\n"
"v_mfma_f32_16x16x32_bf16 v[96:99], v[184:187], v[144:147], v[96:99]\n"
"s_waitcnt lgkmcnt(4)\n"
"ds_read_b64_tr_b16 v[178:179], v214 offset:38272\n"
"v_mul_f32 v63, v31, v63\n"
"v_mfma_f32_16x16x32_bf16 v[112:115], v[188:191], v[144:147], v[112:115]\n"
"v_exp_f32 v246, v246\n"
"v_mfma_f32_16x16x32_bf16 v[116:119], v[188:191], v[148:151], v[116:119]\n"
"s_waitcnt lgkmcnt(4)\n"
"buffer_load_dwordx4 v207, s[28:31], s66 offen lds\n"
"v_mfma_f32_16x16x32_bf16 v[120:123], v[188:191], v[192:195], v[120:123]\n"
"s_waitcnt lgkmcnt(2)\n"
"s_add_u32 s66, s60, 0x1b0080\n"
"ds_read_b64_tr_b16 v[180:181], v214 offset:42240\n"
"v_fma_f32 v241, v241, v227, v227\n"
"v_mfma_f32_16x16x32_bf16 v[124:127], v[196:199], v[192:195], v[124:127]\n"
"s_waitcnt lgkmcnt(1)\n"
"s_add_u32 m0, s59, 0x2100\n"
"ds_read_b64_tr_b16 v[182:183], v214 offset:42496\n"
"v_fma_f32 v242, v242, v227, v227\n"
"v_mfma_f32_16x16x32_bf16 v[76:79], v[176:179], v[172:175], v[76:79]\n"
"v_exp_f32 v247, v247\n"
"v_mfma_f32_16x16x32_bf16 v[72:75], v[176:179], v[168:171], v[72:75]\n"
"v_rcp_f32 v241, v241\n"
"v_mfma_f32_16x16x32_bf16 v[68:71], v[176:179], v[164:167], v[68:71]\n"
"ds_read_b64_tr_b16 v[184:185], v214 offset:46464\n"
"v_fma_f32 v243, v243, v227, v227\n"
"v_mfma_f32_16x16x32_bf16 v[64:67], v[176:179], v[160:163], v[64:67]\n"
"s_waitcnt lgkmcnt(1)\n"
"ds_read_b64_tr_b16 v[186:187], v214 offset:46720\n"
"v_fma_f32 v244, v244, v227, v227\n"
"v_mfma_f32_16x16x32_bf16 v[80:83], v[180:183], v[160:163], v[80:83]\n"
"buffer_load_dwordx4 v207, s[24:27], s66 offen lds\n"
"v_fma_f32 v245, v245, v227, v227\n"
"v_mfma_f32_16x16x32_bf16 v[84:87], v[180:183], v[164:167], v[84:87]\n"
"s_waitcnt vmcnt(10)\n"
"s_add_u32 m0, s59, 0x3180\n"
"ds_read_b64_tr_b16 v[188:189], v216 offset:38016\n"
"v_fma_f32 v246, v246, v227, v227\n"
"v_mfma_f32_16x16x32_bf16 v[88:91], v[180:183], v[168:171], v[88:91]\n"
"s_barrier\n"
"ds_read_b64_tr_b16 v[190:191], v216 offset:38272\n"
"v_fma_f32 v247, v247, v227, v227\n"
"v_mfma_f32_16x16x32_bf16 v[92:95], v[180:183], v[172:175], v[92:95]\n"
"s_waitcnt lgkmcnt(2)\n"
"ds_read_b128 v[192:195], v218 offset:17984\n"
"v_mul_f32 v44, v44, v240\n"
"v_mfma_f32_16x16x32_bf16 v[108:111], v[184:187], v[172:175], v[108:111]\n"
"ds_read_b64_tr_b16 v[196:197], v217 offset:38016\n"
"v_mul_f32 v45, v45, v241\n"
"v_mfma_f32_16x16x32_bf16 v[104:107], v[184:187], v[168:171], v[104:107]\n"
"ds_read_b64_tr_b16 v[198:199], v217 offset:38272\n"
"v_cvt_pk_bf16_f32 v252, v44, v45\n"
"v_mfma_f32_16x16x32_bf16 v[100:103], v[184:187], v[164:167], v[100:103]\n"
"ds_read_b64_tr_b16 v[176:177], v202 offset:8448\n"
"buffer_load_dwordx4 v207, s[28:31], s66 offen lds\n"
"v_mfma_f32_16x16x32_bf16 v[96:99], v[184:187], v[160:163], v[96:99]\n"
"s_waitcnt lgkmcnt(4)\n"
"s_add_u32 s66, s60, 0x1f8080\n"
"ds_read_b64_tr_b16 v[178:179], v202 offset:8704\n"
"v_mul_f32 v108, v223, v108\n"
"v_mfma_f32_16x16x32_bf16 v[112:115], v[188:191], v[160:163], v[112:115]\n"
"s_add_u32 m0, s59, 0x4200\n"
"v_rcp_f32 v242, v242\n"
"v_mfma_f32_16x16x32_bf16 v[116:119], v[188:191], v[164:167], v[116:119]\n"
"s_waitcnt lgkmcnt(4)\n"
"v_rcp_f32 v243, v243\n"
"v_mfma_f32_16x16x32_bf16 v[120:123], v[188:191], v[192:195], v[120:123]\n"
"s_waitcnt lgkmcnt(2)\n"
"ds_read_b64_tr_b16 v[180:181], v202 offset:8480\n"
"v_mul_f32 v46, v46, v242\n"
"v_mfma_f32_16x16x32_bf16 v[124:127], v[196:199], v[192:195], v[124:127]\n"
"s_waitcnt lgkmcnt(1)\n"
"ds_read_b64_tr_b16 v[182:183], v202 offset:8736\n"
"v_mul_f32 v47, v47, v243\n"
"v_mfma_f32_16x16x32_bf16 v[0:3], v[176:179], a[0:3], 0\n"
"buffer_load_dwordx4 v207, s[24:27], s66 offen lds\n"
"v_cvt_pk_bf16_f32 v253, v46, v47\n"
"v_mfma_f32_16x16x32_bf16 v[4:7], v[176:179], a[4:7], 0\n"
"s_add_u32 m0, s59, 0x5280\n"
"v_rcp_f32 v244, v244\n"
"v_mfma_f32_16x16x32_bf16 v[8:11], v[176:179], a[8:11], 0\n"
"ds_read_b64_tr_b16 v[184:185], v202 offset:12672\n"
"v_mul_f32 v60, v60, v244\n"
"v_mfma_f32_16x16x32_bf16 v[12:15], v[176:179], a[12:15], 0\n"
"s_waitcnt lgkmcnt(1)\n"
"ds_read_b64_tr_b16 v[186:187], v202 offset:12928\n"
"v_mul_f32 v96, v220, v96\n"
"v_mfma_f32_16x16x32_bf16 v[28:31], v[180:183], a[12:15], 0\n"
"buffer_load_dwordx4 v207, s[28:31], s66 offen lds\n"
"v_mul_f32 v97, v220, v97\n"
"v_mfma_f32_16x16x32_bf16 v[24:27], v[180:183], a[8:11], 0\n"
"s_add_u32 s66, s60, 0x240080\n"
"v_rcp_f32 v245, v245\n"
"v_mfma_f32_16x16x32_bf16 v[20:23], v[180:183], a[4:7], 0\n"
"s_add_u32 m0, s59, 0xc600\n"
"ds_read_b64_tr_b16 v[188:189], v202 offset:12704\n"
"v_mul_f32 v61, v61, v245\n"
"v_mfma_f32_16x16x32_bf16 v[16:19], v[180:183], a[0:3], 0\n"
"s_waitcnt lgkmcnt(1)\n"
"ds_read_b64_tr_b16 v[190:191], v202 offset:12960\n"
"v_cvt_pk_bf16_f32 v254, v60, v61\n"
"v_mfma_f32_16x16x32_bf16 v[32:35], v[184:187], a[0:3], 0\n"
"v_rcp_f32 v246, v246\n"
"v_mfma_f32_16x16x32_bf16 v[36:39], v[184:187], a[4:7], 0\n"
"buffer_load_dwordx4 v207, s[24:27], s66 offen lds\n"
"v_mul_f32 v62, v62, v246\n"
"v_mfma_f32_16x16x32_bf16 v[40:43], v[184:187], a[8:11], 0\n"
"s_add_u32 m0, s59, 0xd680\n"
"ds_read_b64_tr_b16 v[192:193], v202 offset:16896\n"
"v_mul_f32 v98, v220, v98\n"
"v_mfma_f32_16x16x32_bf16 v[44:47], v[184:187], a[12:15], 0\n"
"ds_read_b64_tr_b16 v[194:195], v202 offset:17152\n"
"v_mul_f32 v99, v220, v99\n"
"v_rcp_f32 v247, v247\n"
"s_nop 0\n"
"v_mul_f32 v63, v63, v247\n"
"v_cvt_pk_bf16_f32 v255, v62, v63\n"
"s_waitcnt lgkmcnt(2)\n"
"v_mfma_f32_16x16x32_bf16 v[60:63], v[188:191], a[12:15], 0\n"
"ds_read_b64_tr_b16 v[196:197], v202 offset:16928\n"
"v_mul_f32 v112, v220, v112\n"
"v_mfma_f32_16x16x32_bf16 v[56:59], v[188:191], a[8:11], 0\n"
"ds_read_b64_tr_b16 v[198:199], v202 offset:17184\n"
"v_mul_f32 v113, v220, v113\n"
"v_mfma_f32_16x16x32_bf16 v[52:55], v[188:191], a[4:7], 0\n"
"ds_read_b64_tr_b16 v[176:177], v202 offset:21120\n"
"v_mul_f32 v114, v220, v114\n"
"v_mfma_f32_16x16x32_bf16 v[48:51], v[188:191], a[0:3], 0\n"
"s_waitcnt lgkmcnt(3)\n"
"buffer_store_dwordx4 v[252:255], v139, s[44:47], s52 offen offset:0 sc0 sc1\n"
"v_mul_f32 v115, v220, v115\n"
"v_mfma_f32_16x16x32_bf16 v[0:3], v[192:195], a[16:19], v[0:3]\n"
"s_add_u32 s52, s70, 0x1000000\n"
"ds_read_b64_tr_b16 v[178:179], v202 offset:21376\n"
"v_mul_f32 v116, v221, v116\n"
"v_mfma_f32_16x16x32_bf16 v[4:7], v[192:195], a[20:23], v[4:7]\n"
"ds_read_b64_tr_b16 v[180:181], v202 offset:21152\n"
"v_mul_f32 v117, v221, v117\n"
"v_mfma_f32_16x16x32_bf16 v[8:11], v[192:195], a[24:27], v[8:11]\n"
"ds_read_b64_tr_b16 v[182:183], v202 offset:21408\n"
"v_mul_f32 v118, v221, v118\n"
"v_mfma_f32_16x16x32_bf16 v[12:15], v[192:195], a[28:31], v[12:15]\n"
"s_waitcnt lgkmcnt(4)\n"
"buffer_load_dwordx4 v207, s[28:31], s66 offen lds\n"
"v_mfma_f32_16x16x32_bf16 v[28:31], v[196:199], a[28:31], v[28:31]\n"
"s_add_u32 s66, s60, 0x288080\n"
"s_add_u32 m0, s59, 0x6300\n"
"v_mul_f32 v119, v221, v119\n"
"v_mul_f32 v120, v228, v120\n"
"v_mfma_f32_16x16x32_bf16 v[24:27], v[196:199], a[24:27], v[24:27]\n"
"s_waitcnt vmcnt(9)\n"
"v_mul_f32 v121, v228, v121\n"
"v_mul_f32 v122, v228, v122\n"
"v_mfma_f32_16x16x32_bf16 v[20:23], v[196:199], a[20:23], v[20:23]\n"
"s_barrier\n"
"v_mul_f32 v123, v228, v123\n"
"v_mul_f32 v100, v221, v100\n"
"v_mfma_f32_16x16x32_bf16 v[16:19], v[196:199], a[16:19], v[16:19]\n"
"s_waitcnt lgkmcnt(1)\n"
"ds_read_b64_tr_b16 v[184:185], v201 offset:16896\n"
"v_mul_f32 v101, v221, v101\n"
"v_mfma_f32_16x16x32_bf16 v[32:35], v[176:179], a[16:19], v[32:35]\n"
"ds_read_b64_tr_b16 v[186:187], v201 offset:17152\n"
"v_mul_f32 v102, v221, v102\n"
"v_mfma_f32_16x16x32_bf16 v[36:39], v[176:179], a[20:23], v[36:39]\n"
"buffer_load_dwordx4 v207, s[24:27], s66 offen lds\n"
"v_mfma_f32_16x16x32_bf16 v[40:43], v[176:179], a[24:27], v[40:43]\n"
"s_add_u32 m0, s59, 0x7380\n"
"ds_read_b64_tr_b16 v[188:189], v201 offset:16928\n"
"v_mul_f32 v103, v221, v103\n"
"v_mfma_f32_16x16x32_bf16 v[44:47], v[176:179], a[28:31], v[44:47]\n"
"s_waitcnt lgkmcnt(3)\n"
"ds_read_b64_tr_b16 v[190:191], v201 offset:17184\n"
"v_mul_f32 v104, v222, v104\n"
"v_mfma_f32_16x16x32_bf16 v[60:63], v[180:183], a[28:31], v[60:63]\n"
"ds_read_b64_tr_b16 v[192:193], v201 offset:21120\n"
"v_mul_f32 v105, v222, v105\n"
"v_mfma_f32_16x16x32_bf16 v[56:59], v[180:183], a[24:27], v[56:59]\n"
"ds_read_b64_tr_b16 v[194:195], v201 offset:21376\n"
"v_mul_f32 v106, v222, v106\n"
"v_mfma_f32_16x16x32_bf16 v[52:55], v[180:183], a[20:23], v[52:55]\n"
"ds_read_b64_tr_b16 v[196:197], v201 offset:21152\n"
"v_mul_f32 v107, v222, v107\n"
"v_mfma_f32_16x16x32_bf16 v[48:51], v[180:183], a[16:19], v[48:51]\n"
"s_waitcnt lgkmcnt(5)\n"
"buffer_load_dwordx4 v207, s[28:31], s66 offen lds\n"
"v_mfma_f32_16x16x32_bf16 v[0:3], v[184:187], a[32:35], v[0:3]\n"
"s_add_u32 s66, s60, 0x2d0080\n"
"s_add_u32 m0, s59, 0xe700\n"
"ds_read_b64_tr_b16 v[198:199], v201 offset:21408\n"
"v_mul_f32 v109, v223, v109\n"
"v_mfma_f32_16x16x32_bf16 v[4:7], v[184:187], a[36:39], v[4:7]\n"
"ds_read_b64_tr_b16 v[176:177], v201 offset:25344\n"
"v_mul_f32 v110, v223, v110\n"
"v_mfma_f32_16x16x32_bf16 v[8:11], v[184:187], a[40:43], v[8:11]\n"
"ds_read_b64_tr_b16 v[178:179], v201 offset:25600\n"
"v_mul_f32 v111, v223, v111\n"
"v_mfma_f32_16x16x32_bf16 v[12:15], v[184:187], a[44:47], v[12:15]\n"
"s_waitcnt lgkmcnt(4)\n"
"ds_read_b64_tr_b16 v[180:181], v201 offset:25376\n"
"v_mul_f32 v84, v221, v84\n"
"v_mfma_f32_16x16x32_bf16 v[28:31], v[188:191], a[44:47], v[28:31]\n"
"ds_read_b64_tr_b16 v[182:183], v201 offset:25632\n"
"v_mul_f32 v85, v221, v85\n"
"v_mfma_f32_16x16x32_bf16 v[24:27], v[188:191], a[40:43], v[24:27]\n"
"buffer_load_dwordx4 v207, s[24:27], s66 offen lds\n"
"v_mfma_f32_16x16x32_bf16 v[20:23], v[188:191], a[36:39], v[20:23]\n"
"s_add_u32 m0, s59, 0xf780\n"
"ds_read_b64_tr_b16 v[184:185], v201 offset:29568\n"
"v_mul_f32 v86, v221, v86\n"
"v_mfma_f32_16x16x32_bf16 v[16:19], v[188:191], a[32:35], v[16:19]\n"
"ds_read_b64_tr_b16 v[186:187], v201 offset:29824\n"
"v_mul_f32 v87, v221, v87\n"
"v_mfma_f32_16x16x32_bf16 v[32:35], v[192:195], a[32:35], v[32:35]\n"
"ds_read_b64_tr_b16 v[188:189], v201 offset:29600\n"
"v_mul_f32 v92, v223, v92\n"
"v_mfma_f32_16x16x32_bf16 v[36:39], v[192:195], a[36:39], v[36:39]\n"
"ds_read_b64_tr_b16 v[190:191], v201 offset:29856\n"
"v_mul_f32 v93, v223, v93\n"
"v_mfma_f32_16x16x32_bf16 v[40:43], v[192:195], a[40:43], v[40:43]\n"
"v_mul_f32 v94, v223, v94\n"
"v_mul_f32 v95, v223, v95\n"
"v_mfma_f32_16x16x32_bf16 v[44:47], v[192:195], a[44:47], v[44:47]\n"
"s_waitcnt lgkmcnt(4)\n"
"buffer_load_dwordx4 v207, s[28:31], s66 offen lds\n"
"v_mfma_f32_16x16x32_bf16 v[60:63], v[196:199], a[44:47], v[60:63]\n"
"s_add_u32 s66, s60, 0x318080\n"
"ds_read_b64_tr_b16 v[192:193], v201 offset:33792\n"
"v_cvt_pk_bf16_f32 v112, v112, v113\n"
"v_mfma_f32_16x16x32_bf16 v[56:59], v[196:199], a[40:43], v[56:59]\n"
"s_add_u32 m0, s59, 0x10800\n"
"ds_read_b64_tr_b16 v[194:195], v201 offset:34048\n"
"v_cvt_pk_bf16_f32 v113, v114, v115\n"
"v_mfma_f32_16x16x32_bf16 v[52:55], v[196:199], a[36:39], v[52:55]\n"
"v_cvt_pk_bf16_f32 v116, v116, v117\n"
"v_cvt_pk_bf16_f32 v117, v118, v119\n"
"v_mfma_f32_16x16x32_bf16 v[48:51], v[196:199], a[32:35], v[48:51]\n"
"ds_read_b64_tr_b16 v[196:197], v201 offset:33824\n"
"v_cvt_pk_bf16_f32 v120, v120, v121\n"
"v_mfma_f32_16x16x32_bf16 v[0:3], v[176:179], a[48:51], v[0:3]\n"
"ds_read_b64_tr_b16 v[198:199], v201 offset:34080\n"
"v_cvt_pk_bf16_f32 v121, v122, v123\n"
"v_mfma_f32_16x16x32_bf16 v[4:7], v[176:179], a[52:55], v[4:7]\n"
"buffer_load_dwordx4 v207, s[24:27], s66 offen lds\n"
"v_mul_f32 v124, v228, v124\n"
"v_mfma_f32_16x16x32_bf16 v[8:11], v[176:179], a[56:59], v[8:11]\n"
"s_add_u32 m0, s59, 0x11880\n"
"v_mul_f32 v125, v228, v125\n"
"v_mul_f32 v126, v228, v126\n"
"v_mfma_f32_16x16x32_bf16 v[12:15], v[176:179], a[60:63], v[12:15]\n"
"ds_read_b64_tr_b16 v[176:177], v201 offset:38016\n"
"v_mul_f32 v127, v228, v127\n"
"v_mfma_f32_16x16x32_bf16 v[28:31], v[180:183], a[60:63], v[28:31]\n"
"ds_read_b64_tr_b16 v[178:179], v201 offset:38272\n"
"v_cvt_pk_bf16_f32 v96, v96, v97\n"
"v_mfma_f32_16x16x32_bf16 v[24:27], v[180:183], a[56:59], v[24:27]\n"
"v_cvt_pk_bf16_f32 v97, v98, v99\n"
"v_cvt_pk_bf16_f32 v100, v100, v101\n"
"v_mfma_f32_16x16x32_bf16 v[20:23], v[180:183], a[52:55], v[20:23]\n"
"ds_write_b64 v128, v[96:97] offset:2752\n"
"v_cvt_pk_bf16_f32 v101, v102, v103\n"
"v_mfma_f32_16x16x32_bf16 v[16:19], v[180:183], a[48:51], v[16:19]\n"
"s_waitcnt lgkmcnt(3)\n"
"buffer_load_dwordx4 v207, s[28:31], s66 offen lds\n"
"v_cvt_pk_bf16_f32 v104, v104, v105\n"
"v_cvt_pk_bf16_f32 v105, v106, v107\n"
"v_mfma_f32_16x16x32_bf16 v[32:35], v[184:187], a[48:51], v[32:35]\n"
"s_add_u32 s66, s60, 0x360080\n"
"ds_write_b64 v128, v[100:101] offset:5440\n"
"v_cvt_pk_bf16_f32 v108, v108, v109\n"
"v_mfma_f32_16x16x32_bf16 v[36:39], v[184:187], a[52:55], v[36:39]\n"
"s_add_u32 m0, s59, 0x8400\n"
"ds_write_b64 v129, v[104:105] offset:2752\n"
"v_cvt_pk_bf16_f32 v109, v110, v111\n"
"v_mfma_f32_16x16x32_bf16 v[40:43], v[184:187], a[56:59], v[40:43]\n"
"ds_write_b64 v129, v[108:109] offset:5440\n"
"v_cvt_pk_bf16_f32 v84, v84, v85\n"
"v_mfma_f32_16x16x32_bf16 v[44:47], v[184:187], a[60:63], v[44:47]\n"
"ds_read_b64_tr_b16 v[180:181], v201 offset:38048\n"
"v_cvt_pk_bf16_f32 v85, v86, v87\n"
"v_mfma_f32_16x16x32_bf16 v[60:63], v[188:191], a[60:63], v[60:63]\n"
"ds_read_b64_tr_b16 v[182:183], v201 offset:38304\n"
"v_cvt_pk_bf16_f32 v92, v92, v93\n"
"v_cvt_pk_bf16_f32 v93, v94, v95\n"
"v_mfma_f32_16x16x32_bf16 v[56:59], v[188:191], a[56:59], v[56:59]\n"
"buffer_load_dwordx4 v207, s[24:27], s66 offen lds\n"
"v_cndmask_b32 v112, v112, v113, s[92:93]\n"
"v_mfma_f32_16x16x32_bf16 v[52:55], v[188:191], a[52:55], v[52:55]\n"
"ds_write_b64 v128, v[84:85] offset:5376\n"
"v_cndmask_b32 v113, v113, v138, s[94:95]\n"
"v_cndmask_b32 v116, v116, v117, s[92:93]\n"
"v_mfma_f32_16x16x32_bf16 v[48:51], v[188:191], a[48:51], v[48:51]\n"
"s_add_u32 m0, s59, 0x9480\n"
"ds_write_b64 v129, v[92:93] offset:5376\n"
"v_cndmask_b32 v117, v117, v138, s[94:95]\n"
"v_mfma_f32_16x16x32_bf16 v[0:3], v[192:195], a[64:67], v[0:3]\n"
"ds_write_b64 v130, v[112:113] offset:2816\n"
"v_cndmask_b32 v120, v120, v121, s[92:93]\n"
"v_mfma_f32_16x16x32_bf16 v[4:7], v[192:195], a[68:71], v[4:7]\n"
"ds_write_b64 v130, v[116:117] offset:5504\n"
"v_cndmask_b32 v121, v121, v138, s[94:95]\n"
"v_cvt_pk_bf16_f32 v124, v124, v125\n"
"v_mfma_f32_16x16x32_bf16 v[8:11], v[192:195], a[72:75], v[8:11]\n"
"ds_write_b64 v135, v[120:121] offset:128\n"
"v_mfma_f32_16x16x32_bf16 v[12:15], v[192:195], a[76:79], v[12:15]\n"
"buffer_load_dwordx4 v207, s[28:31], s66 offen lds\n"
"v_mfma_f32_16x16x32_bf16 v[28:31], v[196:199], a[76:79], v[28:31]\n"
"s_add_u32 s66, s60, 0x3a8080\n"
"s_add_u32 m0, s59, 0xa500\n"
"v_cvt_pk_bf16_f32 v125, v126, v127\n"
"v_mul_f32 v80, v220, v80\n"
"v_mfma_f32_16x16x32_bf16 v[24:27], v[196:199], a[72:75], v[24:27]\n"
"ds_write_b64 v134, v[124:125] offset:0\n"
"s_waitcnt vmcnt(11) lgkmcnt(0)\n"
"v_mul_f32 v81, v220, v81\n"
"v_mul_f32 v82, v220, v82\n"
"v_mfma_f32_16x16x32_bf16 v[20:23], v[196:199], a[68:71], v[20:23]\n"
"s_barrier\n"
"v_mul_f32 v83, v220, v83\n"
"v_cvt_pk_bf16_f32 v80, v80, v81\n"
"v_mfma_f32_16x16x32_bf16 v[16:19], v[196:199], a[64:67], v[16:19]\n"
"ds_read_b64_tr_b16 v[184:185], v200 offset:0\n"
"v_cvt_pk_bf16_f32 v81, v82, v83\n"
"v_mfma_f32_16x16x32_bf16 v[32:35], v[176:179], a[64:67], v[32:35]\n"
"ds_read_b64_tr_b16 v[186:187], v200 offset:256\n"
"v_mul_f32 v88, v222, v88\n"
"v_mfma_f32_16x16x32_bf16 v[36:39], v[176:179], a[68:71], v[36:39]\n"
"buffer_load_dwordx4 v207, s[24:27], s66 offen lds\n"
"v_mfma_f32_16x16x32_bf16 v[40:43], v[176:179], a[72:75], v[40:43]\n"
"s_add_u32 m0, s59, 0xb580\n"
"ds_read_b64_tr_b16 v[188:189], v200 offset:32\n"
"v_mul_f32 v89, v222, v89\n"
"v_mfma_f32_16x16x32_bf16 v[44:47], v[176:179], a[76:79], v[44:47]\n"
"ds_read_b64_tr_b16 v[190:191], v200 offset:288\n"
"v_mul_f32 v90, v222, v90\n"
"v_mfma_f32_16x16x32_bf16 v[60:63], v[180:183], a[76:79], v[60:63]\n"
"ds_read_b64_tr_b16 v[192:193], v200 offset:4224\n"
"v_mul_f32 v91, v222, v91\n"
"v_mfma_f32_16x16x32_bf16 v[56:59], v[180:183], a[72:75], v[56:59]\n"
"ds_read_b64_tr_b16 v[194:195], v200 offset:4480\n"
"v_cvt_pk_bf16_f32 v88, v88, v89\n"
"v_mfma_f32_16x16x32_bf16 v[52:55], v[180:183], a[68:71], v[52:55]\n"
"ds_read_b64_tr_b16 v[196:197], v200 offset:4256\n"
"v_cvt_pk_bf16_f32 v89, v90, v91\n"
"v_mfma_f32_16x16x32_bf16 v[48:51], v[180:183], a[64:67], v[48:51]\n"
"s_waitcnt lgkmcnt(5)\n"
"buffer_load_dwordx4 v207, s[28:31], s66 offen lds\n"
"v_mfma_f32_16x16x32_bf16 v[0:3], v[184:187], a[80:83], v[0:3]\n"
"s_add_u32 s66, s60, 0x3f0080\n"
"ds_read_b64_tr_b16 v[198:199], v200 offset:4512\n"
"v_mul_f32 v64, v220, v64\n"
"v_mfma_f32_16x16x32_bf16 v[4:7], v[184:187], a[84:87], v[4:7]\n"
"s_add_u32 m0, s59, 0x1ef00\n"
"ds_read_b64_tr_b16 v[176:177], v200 offset:8448\n"
"v_mul_f32 v65, v220, v65\n"
"v_mfma_f32_16x16x32_bf16 v[8:11], v[184:187], a[88:91], v[8:11]\n"
"ds_read_b64_tr_b16 v[178:179], v200 offset:8704\n"
"v_mul_f32 v66, v220, v66\n"
"v_mfma_f32_16x16x32_bf16 v[12:15], v[184:187], a[92:95], v[12:15]\n"
"s_waitcnt lgkmcnt(4)\n"
"ds_read_b64_tr_b16 v[180:181], v200 offset:8480\n"
"v_mul_f32 v67, v220, v67\n"
"v_mfma_f32_16x16x32_bf16 v[28:31], v[188:191], a[92:95], v[28:31]\n"
"ds_read_b64_tr_b16 v[182:183], v200 offset:8736\n"
"v_cvt_pk_bf16_f32 v64, v64, v65\n"
"v_mfma_f32_16x16x32_bf16 v[24:27], v[188:191], a[88:91], v[24:27]\n"
"buffer_load_dwordx4 v207, s[24:27], s66 offen lds\n"
"v_mfma_f32_16x16x32_bf16 v[20:23], v[188:191], a[84:87], v[20:23]\n"
"s_add_u32 m0, s59, 0x1ff80\n"
"ds_read_b64_tr_b16 v[184:185], v200 offset:12672\n"
"v_cvt_pk_bf16_f32 v65, v66, v67\n"
"v_mfma_f32_16x16x32_bf16 v[16:19], v[188:191], a[80:83], v[16:19]\n"
"ds_read_b64_tr_b16 v[186:187], v200 offset:12928\n"
"v_mul_f32 v68, v221, v68\n"
"v_mfma_f32_16x16x32_bf16 v[32:35], v[192:195], a[80:83], v[32:35]\n"
"ds_read_b64_tr_b16 v[188:189], v200 offset:12704\n"
"v_mul_f32 v69, v221, v69\n"
"v_mfma_f32_16x16x32_bf16 v[36:39], v[192:195], a[84:87], v[36:39]\n"
"ds_read_b64_tr_b16 v[190:191], v200 offset:12960\n"
"v_mul_f32 v70, v221, v70\n"
"v_mfma_f32_16x16x32_bf16 v[40:43], v[192:195], a[88:91], v[40:43]\n"
"ds_read_b64 v[66:67], v131 offset:2816\n"
"v_mul_f32 v71, v221, v71\n"
"v_mfma_f32_16x16x32_bf16 v[44:47], v[192:195], a[92:95], v[44:47]\n"
"s_waitcnt lgkmcnt(5)\n"
"buffer_load_dwordx4 v207, s[28:31], s66 offen lds\n"
"v_mfma_f32_16x16x32_bf16 v[60:63], v[196:199], a[92:95], v[60:63]\n"
"s_add_u32 s66, s60, 0x438080\n"
"ds_read_b64_tr_b16 v[192:193], v200 offset:16896\n"
"v_cvt_pk_bf16_f32 v68, v68, v69\n"
"v_mfma_f32_16x16x32_bf16 v[56:59], v[196:199], a[88:91], v[56:59]\n"
"s_add_u32 m0, s59, 0x23100\n"
"ds_read_b64_tr_b16 v[194:195], v200 offset:17152\n"
"v_cvt_pk_bf16_f32 v69, v70, v71\n"
"v_mfma_f32_16x16x32_bf16 v[52:55], v[196:199], a[84:87], v[52:55]\n"
"ds_read_b64 v[96:97], v128 offset:0\n"
"v_mul_f32 v72, v222, v72\n"
"v_mfma_f32_16x16x32_bf16 v[48:51], v[196:199], a[80:83], v[48:51]\n"
"ds_read_b64_tr_b16 v[196:197], v200 offset:16928\n"
"v_mul_f32 v73, v222, v73\n"
"v_mfma_f32_16x16x32_bf16 v[0:3], v[176:179], a[96:99], v[0:3]\n"
"ds_read_b64_tr_b16 v[198:199], v200 offset:17184\n"
"v_mul_f32 v74, v222, v74\n"
"v_mfma_f32_16x16x32_bf16 v[4:7], v[176:179], a[100:103], v[4:7]\n"
"buffer_load_dwordx4 v207, s[24:27], s66 offen lds\n"
"v_mfma_f32_16x16x32_bf16 v[8:11], v[176:179], a[104:107], v[8:11]\n"
"s_add_u32 m0, s59, 0x24180\n"
"ds_read_b64 v[98:99], v131 offset:136\n"
"v_mul_f32 v75, v222, v75\n"
"v_mfma_f32_16x16x32_bf16 v[12:15], v[176:179], a[108:111], v[12:15]\n"
"ds_read_b64_tr_b16 v[176:177], v200 offset:21120\n"
"v_cvt_pk_bf16_f32 v72, v72, v73\n"
"v_mfma_f32_16x16x32_bf16 v[28:31], v[180:183], a[108:111], v[28:31]\n"
"ds_read_b64_tr_b16 v[178:179], v200 offset:21376\n"
"v_cvt_pk_bf16_f32 v73, v74, v75\n"
"v_mfma_f32_16x16x32_bf16 v[24:27], v[180:183], a[104:107], v[24:27]\n"
"ds_read_b64 v[82:83], v131 offset:2824\n"
"v_mul_f32 v76, v223, v76\n"
"v_mfma_f32_16x16x32_bf16 v[20:23], v[180:183], a[100:103], v[20:23]\n"
"ds_read_b64 v[70:71], v131 offset:5504\n"
"v_mul_f32 v77, v223, v77\n"
"v_mfma_f32_16x16x32_bf16 v[16:19], v[180:183], a[96:99], v[16:19]\n"
"s_waitcnt lgkmcnt(5)\n"
"buffer_load_dwordx4 v207, s[28:31], s66 offen lds\n"
"v_mfma_f32_16x16x32_bf16 v[32:35], v[184:187], a[96:99], v[32:35]\n"
"s_add_u32 s66, s60, 0x480080\n"
"ds_read_b64_tr_b16 v[180:181], v200 offset:21152\n"
"v_mul_f32 v78, v223, v78\n"
"v_mfma_f32_16x16x32_bf16 v[36:39], v[184:187], a[100:103], v[36:39]\n"
"s_add_u32 m0, s59, 0x25200\n"
"ds_read_b64_tr_b16 v[182:183], v200 offset:21408\n"
"v_mul_f32 v79, v223, v79\n"
"v_mfma_f32_16x16x32_bf16 v[40:43], v[184:187], a[104:107], v[40:43]\n"
"ds_read_b64 v[86:87], v131 offset:5512\n"
"v_cvt_pk_bf16_f32 v76, v76, v77\n"
"v_mfma_f32_16x16x32_bf16 v[44:47], v[184:187], a[108:111], v[44:47]\n"
"ds_read_b64_tr_b16 v[116:117], v133 offset:144\n"
"v_cvt_pk_bf16_f32 v77, v78, v79\n"
"v_mfma_f32_16x16x32_bf16 v[60:63], v[188:191], a[108:111], v[60:63]\n"
"ds_read_b64_tr_b16 v[118:119], v133 offset:2832\n"
"v_cndmask_b32 v66, 0, v66, s[88:89]\n"
"v_mfma_f32_16x16x32_bf16 v[56:59], v[188:191], a[104:107], v[56:59]\n"
"buffer_load_dwordx4 v207, s[24:27], s66 offen lds\n"
"v_mfma_f32_16x16x32_bf16 v[52:55], v[188:191], a[100:103], v[52:55]\n"
"s_add_u32 m0, s59, 0x26280\n"
"ds_read_b64_tr_b16 v[104:105], v132 offset:64\n"
"v_cndmask_b32 v67, 0, v67, s[90:91]\n"
"v_mfma_f32_16x16x32_bf16 v[48:51], v[188:191], a[96:99], v[48:51]\n"
"s_waitcnt lgkmcnt(4)\n"
"ds_read_b64_tr_b16 v[106:107], v132 offset:2752\n"
"v_cndmask_b32 v70, 0, v70, s[88:89]\n"
"v_mfma_f32_16x16x32_bf16 v[0:3], v[192:195], a[112:115], v[0:3]\n"
"ds_read_b64_tr_b16 v[120:121], v133 offset:5520\n"
"v_cndmask_b32 v71, 0, v71, s[90:91]\n"
"v_mfma_f32_16x16x32_bf16 v[240:243], v[96:99], v[64:67], 0\n"
"ds_read_b64_tr_b16 v[108:109], v132 offset:5440\n"
"v_add_u32 v131, s19, v131\n"
"v_mfma_f32_16x16x32_bf16 v[244:247], v[80:83], v[64:67], 0\n"
"ds_read_b64 v[100:101], v129 offset:0\n"
"v_add_u32 v132, s19, v132\n"
"v_mfma_f32_16x16x32_bf16 v[4:7], v[192:195], a[116:119], v[4:7]\n"
"ds_read_b64 v[102:103], v131 offset:136\n"
"v_add_u32 v133, s19, v133\n"
"v_mfma_f32_16x16x32_bf16 v[8:11], v[192:195], a[120:123], v[8:11]\n"
"ds_read_b64 v[74:75], v131 offset:2816\n"
"v_cndmask_b32 v240, v240, v241, s[74:75]\n"
"v_mfma_f32_16x16x32_bf16 v[248:251], v[80:83], v[68:71], 0\n"
"s_waitcnt lgkmcnt(7)\n"
"ds_read_b64 v[90:91], v131 offset:2824\n"
"v_cndmask_b32 v244, v244, v245, s[74:75]\n"
"v_mfma_f32_16x16x32_bf16 v[252:255], v[84:87], v[68:71], 0\n"
"L_st4_%=:\n s_cbranch_execz L_st8_%=\n"
"ds_read_b64 v[78:79], v131 offset:5504\n"
"v_cndmask_b32 v214, v240, v244, s[76:77]\n"
"v_mfma_f32_16x16x32_bf16 v[12:15], v[192:195], a[124:127], v[12:15]\n"
"buffer_load_dwordx4 v207, s[28:31], s66 offen lds\n"
"v_mfma_f32_16x16x32_bf16 v[28:31], v[196:199], a[124:127], v[28:31]\n"
"s_add_u32 s66, s60, 0x4c8080\n"
"ds_read_b64 v[94:95], v131 offset:5512\n"
"v_cndmask_b32 v214, v214, v137, s[80:81]\n"
"v_mfma_f32_16x16x32_bf16 v[24:27], v[196:199], a[120:123], v[24:27]\n"
"s_waitcnt vmcnt(12)\n"
"ds_read_b64_tr_b16 v[110:111], v132 offset:64\n"
"v_cndmask_b32 v242, v242, v243, s[74:75]\n"
"v_mfma_f32_16x16x32_bf16 v[20:23], v[196:199], a[116:119], v[20:23]\n"
"s_barrier\n"
"ds_read_b64_tr_b16 v[122:123], v133 offset:144\n"
"v_cndmask_b32 v246, v246, v247, s[74:75]\n"
"v_mfma_f32_16x16x32_bf16 v[16:19], v[196:199], a[112:115], v[16:19]\n"
"s_waitcnt lgkmcnt(5)\n"
"ds_read_b64_tr_b16 v[184:185], v200 offset:50688\n"
"v_cndmask_b32 v74, 0, v74, s[88:89]\n"
"v_mfma_f32_16x16x32_bf16 v[32:35], v[176:179], a[112:115], v[32:35]\n"
"s_add_u32 m0, s59, 0x12900\n"
"ds_read_b64_tr_b16 v[186:187], v200 offset:50944\n"
"v_cndmask_b32 v75, 0, v75, s[90:91]\n"
"v_mfma_f32_16x16x32_bf16 v[36:39], v[176:179], a[116:119], v[36:39]\n"
"buffer_load_dwordx4 v207, s[24:27], s66 offen lds\n"
"v_mfma_f32_16x16x32_bf16 v[40:43], v[176:179], a[120:123], v[40:43]\n"
"s_waitcnt lgkmcnt(4)\n"
"ds_read_b64_tr_b16 v[188:189], v200 offset:50720\n"
"v_cndmask_b32 v78, 0, v78, s[88:89]\n"
"v_mfma_f32_16x16x32_bf16 v[44:47], v[176:179], a[124:127], v[44:47]\n"
"s_add_u32 m0, s59, 0x13980\n"
"ds_read_b64_tr_b16 v[190:191], v200 offset:50976\n"
"v_cndmask_b32 v79, 0, v79, s[90:91]\n"
"v_mfma_f32_16x16x32_bf16 v[60:63], v[180:183], a[124:127], v[60:63]\n"
"s_lshr_b32 s89, s56, 1\n"
"ds_read_b64_tr_b16 v[192:193], v200 offset:54912\n"
"v_cndmask_b32 v208, v242, v246, s[78:79]\n"
"v_mfma_f32_16x16x32_bf16 v[56:59], v[180:183], a[120:123], v[56:59]\n"
"s_and_b32 s95, s89, 1\n"
"ds_read_b64_tr_b16 v[194:195], v200 offset:55168\n"
"v_cndmask_b32 v208, v208, v137, s[82:83]\n"
"v_mfma_f32_16x16x32_bf16 v[52:55], v[180:183], a[116:119], v[52:55]\n"
"s_lshr_b32 s67, s89, 1\n"
"ds_read_b64_tr_b16 v[196:197], v200 offset:54944\n"
"v_max_f32 v216, v214, v208\n"
"v_mfma_f32_16x16x32_bf16 v[48:51], v[180:183], a[112:115], v[48:51]\n"
"s_waitcnt lgkmcnt(5)\n"
"s_add_u32 s69, s67, 1\n"
"buffer_load_dwordx4 v207, s[28:31], s66 offen lds\n"
"v_mfma_f32_16x16x32_bf16 v[0:3], v[184:187], a[128:131], v[0:3]\n"
"s_add_u32 s66, s60, 0x100\n"
"ds_read_b64_tr_b16 v[198:199], v200 offset:55200\n"
"v_mov_b32 v217, v216\n"
"v_mfma_f32_16x16x32_bf16 v[4:7], v[184:187], a[132:135], v[4:7]\n"
"s_add_u32 m0, s59, 0x21000\n"
"ds_read_b64_tr_b16 v[176:177], v200 offset:25344\n"
"v_add_u32 v203, 0xffff1900, v203\n"
"v_mfma_f32_16x16x32_bf16 v[8:11], v[184:187], a[136:139], v[8:11]\n"
"s_and_b32 s69, s69, 3\n"
"v_permlane16_swap_b32 v216, v217\n"
"v_mfma_f32_16x16x32_bf16 v[12:15], v[184:187], a[140:143], v[12:15]\n"
"s_waitcnt lgkmcnt(3)\n"
"ds_read_b64_tr_b16 v[178:179], v200 offset:25600\n"
"v_max_f32 v216, v216, v217\n"
"v_mfma_f32_16x16x32_bf16 v[28:31], v[188:191], a[140:143], v[28:31]\n"
"s_lshl_b32 s69, s69, 7\n"
"ds_read_b64_tr_b16 v[180:181], v200 offset:25376\n"
"v_mov_b32 v217, v216\n"
"v_mfma_f32_16x16x32_bf16 v[24:27], v[188:191], a[136:139], v[24:27]\n"
"s_mul_i32 s9, s95, 0x2000\n"
"buffer_load_dwordx4 v207, s[24:27], s66 offen lds\n"
"v_mfma_f32_16x16x32_bf16 v[20:23], v[188:191], a[132:135], v[20:23]\n"
"s_add_u32 m0, s59, 0x22080\n"
"v_permlane32_swap_b32 v216, v217\n"
"v_mfma_f32_16x16x32_bf16 v[16:19], v[188:191], a[128:131], v[16:19]\n"
"ds_read_b64_tr_b16 v[182:183], v200 offset:25632\n"
"v_max_f32 v216, v216, v217\n"
"v_mfma_f32_16x16x32_bf16 v[32:35], v[192:195], a[128:131], v[32:35]\n"
"ds_read_b64_tr_b16 v[184:185], v200 offset:29568\n"
"v_mul_f32 v216, s16, v216\n"
"v_mfma_f32_16x16x32_bf16 v[36:39], v[192:195], a[132:135], v[36:39]\n"
"ds_read_b64_tr_b16 v[186:187], v200 offset:29824\n"
"v_fma_f32 v214, v214, s16, -v216\n"
"v_mfma_f32_16x16x32_bf16 v[40:43], v[192:195], a[136:139], v[40:43]\n"
"v_exp_f32 v214, v214\n"
"v_mfma_f32_16x16x32_bf16 v[44:47], v[192:195], a[140:143], v[44:47]\n"
"s_waitcnt lgkmcnt(3)\n"
"buffer_load_dwordx4 v207, s[28:31], s66 offen lds\n"
"v_mfma_f32_16x16x32_bf16 v[60:63], v[196:199], a[140:143], v[60:63]\n"
"s_add_u32 s66, s60, 0x48100\n"
"ds_read_b64_tr_b16 v[188:189], v200 offset:29600\n"
"v_cvt_pk_bf16_f32 v214, v214, v214\n"
"v_mfma_f32_16x16x32_bf16 v[56:59], v[196:199], a[136:139], v[56:59]\n"
"s_add_u32 m0, s59, 0\n"
"ds_read_b64_tr_b16 v[190:191], v200 offset:29856\n"
"v_and_b32 v64, v229, v214\n"
"v_mfma_f32_16x16x32_bf16 v[52:55], v[196:199], a[132:135], v[52:55]\n"
"ds_read_b64_tr_b16 v[192:193], v201 offset:0\n"
"v_and_b32 v66, v231, v214\n"
"v_mfma_f32_16x16x32_bf16 v[48:51], v[196:199], a[128:131], v[48:51]\n"
"ds_read_b64_tr_b16 v[194:195], v201 offset:256\n"
"v_fma_f32 v208, v208, s16, -v216\n"
"v_mfma_f32_16x16x32_bf16 v[0:3], v[176:179], a[144:147], v[0:3]\n"
"v_exp_f32 v208, v208\n"
"v_mfma_f32_16x16x32_bf16 v[4:7], v[176:179], a[148:151], v[4:7]\n"
"buffer_load_dwordx4 v207, s[24:27], s66 offen lds\n"
"v_mfma_f32_16x16x32_bf16 v[8:11], v[176:179], a[152:155], v[8:11]\n"
"s_add_u32 m0, s59, 0x1080\n"
"ds_read_b64_tr_b16 v[196:197], v201 offset:32\n"
"v_cvt_pk_bf16_f32 v208, v208, v208\n"
"v_mfma_f32_16x16x32_bf16 v[12:15], v[176:179], a[156:159], v[12:15]\n"
"s_waitcnt lgkmcnt(3)\n"
"ds_read_b64_tr_b16 v[198:199], v201 offset:288\n"
"v_and_b32 v65, v230, v208\n"
"v_mfma_f32_16x16x32_bf16 v[28:31], v[180:183], a[156:159], v[28:31]\n"
"ds_read_b64_tr_b16 v[176:177], v201 offset:4224\n"
"v_and_b32 v67, v232, v208\n"
"v_mfma_f32_16x16x32_bf16 v[24:27], v[180:183], a[152:155], v[24:27]\n"
"ds_read_b64_tr_b16 v[178:179], v201 offset:4480\n"
"v_cndmask_b32 v248, v248, v249, s[74:75]\n"
"v_mfma_f32_16x16x32_bf16 v[20:23], v[180:183], a[148:151], v[20:23]\n"
"ds_read_b64_tr_b16 v[112:113], v132 offset:2752\n"
"v_cndmask_b32 v252, v252, v253, s[74:75]\n"
"v_mfma_f32_16x16x32_bf16 v[244:247], v[116:119], v[64:67], 0\n"
"ds_read_b64_tr_b16 v[124:125], v133 offset:2832\n"
"v_cndmask_b32 v218, v248, v252, s[76:77]\n"
"v_mfma_f32_16x16x32_bf16 v[240:243], v[104:107], v[64:67], 0\n"
"ds_read_b64_tr_b16 v[114:115], v132 offset:5440\n"
"v_cndmask_b32 v250, v250, v251, s[74:75]\n"
"v_mfma_f32_16x16x32_bf16 v[16:19], v[180:183], a[144:147], v[16:19]\n"
"buffer_load_dwordx4 v207, s[28:31], s66 offen lds\n"
"v_mfma_f32_16x16x32_bf16 v[32:35], v[184:187], a[144:147], v[32:35]\n"
"s_add_u32 s66, s60, 0x90100\n"
"v_rcp_f32 v246, v246\n"
"v_mfma_f32_16x16x32_bf16 v[36:39], v[184:187], a[148:151], v[36:39]\n"
"s_add_u32 m0, s59, 0x2100\n"
"ds_read_b64_tr_b16 v[180:181], v201 offset:4256\n"
"v_mul_f32 v246, 0xbf317218, v246\n"
"v_mfma_f32_16x16x32_bf16 v[40:43], v[184:187], a[152:155], v[40:43]\n"
"ds_read_b64_tr_b16 v[182:183], v201 offset:4512\n"
"v_mul_f32 v240, v240, v246\n"
"v_mfma_f32_16x16x32_bf16 v[44:47], v[184:187], a[156:159], v[44:47]\n"
"ds_read_b64_tr_b16 v[126:127], v133 offset:5520\n"
"v_mul_f32 v241, v241, v246\n"
"v_mfma_f32_16x16x32_bf16 v[60:63], v[188:191], a[156:159], v[60:63]\n"
"v_mul_f32 v242, v242, v246\n"
"v_mfma_f32_16x16x32_bf16 v[56:59], v[188:191], a[152:155], v[56:59]\n"
"buffer_load_dwordx4 v207, s[24:27], s66 offen lds\n"
"v_mfma_f32_16x16x32_bf16 v[52:55], v[188:191], a[148:151], v[52:55]\n"
"s_add_u32 m0, s59, 0x3180\n"
"v_mul_f32 v243, v243, v246\n"
"v_mfma_f32_16x16x32_bf16 v[48:51], v[188:191], a[144:147], v[48:51]\n"
"s_waitcnt lgkmcnt(2)\n"
"v_mul_f32 v244, v244, v246\n"
"v_mfma_f32_16x16x32_bf16 v[0:3], v[192:195], a[160:163], v[0:3]\n"
"v_mul_f32 v245, v245, v246\n"
"v_mfma_f32_16x16x32_bf16 v[4:7], v[192:195], a[164:167], v[4:7]\n"
"v_cvt_pk_bf16_f32 v64, v240, v241\n"
"v_mfma_f32_16x16x32_bf16 v[8:11], v[192:195], a[168:171], v[8:11]\n"
"v_cvt_pk_bf16_f32 v65, v242, v243\n"
"v_mfma_f32_16x16x32_bf16 v[12:15], v[192:195], a[172:175], v[12:15]\n"
"buffer_store_dwordx2 v[64:65], v140, s[44:47], s52 offen offset:0 sc0 sc1\n"
"v_cvt_pk_bf16_f32 v66, v244, v245\n"
"v_mfma_f32_16x16x32_bf16 v[28:31], v[196:199], a[172:175], v[28:31]\n"
"ds_write_b64 v203, v[64:65] offset:25344\n"
"v_mfma_f32_16x16x32_bf16 v[240:243], v[100:103], v[72:75], 0\n"
"ds_write_b64 v203, v[66:67] offset:33792\n"
"v_mfma_f32_16x16x32_bf16 v[24:27], v[196:199], a[168:171], v[24:27]\n"
"buffer_load_dwordx4 v207, s[28:31], s66 offen lds\n"
"v_mfma_f32_16x16x32_bf16 v[244:247], v[88:91], v[72:75], 0\n"
"s_waitcnt vmcnt(15)\n"
"s_add_u32 s66, s60, 0xd8100\n"
"v_cndmask_b32 v254, v254, v255, s[74:75]\n"
"v_mfma_f32_16x16x32_bf16 v[20:23], v[196:199], a[164:167], v[20:23]\n"
"s_barrier\n"
"s_add_u32 m0, s59, 0x4200\n"
"v_cndmask_b32 v238, v250, v254, s[78:79]\n"
"v_mfma_f32_16x16x32_bf16 v[16:19], v[196:199], a[160:163], v[16:19]\n"
"ds_read_b64_tr_b16 v[184:185], v201 offset:8448\n"
"v_max_f32 v239, v218, v238\n"
"v_mfma_f32_16x16x32_bf16 v[32:35], v[176:179], a[160:163], v[32:35]\n"
"ds_read_b64_tr_b16 v[186:187], v201 offset:8704\n"
"v_mov_b32 v235, v239\n"
"v_mfma_f32_16x16x32_bf16 v[36:39], v[176:179], a[164:167], v[36:39]\n"
"ds_read_b64_tr_b16 v[188:189], v201 offset:8480\n"
"v_cndmask_b32 v240, v240, v241, s[74:75]\n"
"v_mfma_f32_16x16x32_bf16 v[40:43], v[176:179], a[168:171], v[40:43]\n"
"v_permlane16_swap_b32 v239, v235\n"
"v_mfma_f32_16x16x32_bf16 v[44:47], v[176:179], a[172:175], v[44:47]\n"
"s_waitcnt lgkmcnt(3)\n"
"buffer_store_dword v66, v141, s[44:47], s52 offen offset:512 sc0 sc1\n"
"v_max_f32 v239, v239, v235\n"
"v_mfma_f32_16x16x32_bf16 v[60:63], v[180:183], a[172:175], v[60:63]\n"
"s_add_u32 s52, s70, 0x1200000\n"
"ds_read_b64_tr_b16 v[190:191], v201 offset:8736\n"
"v_mov_b32 v235, v239\n"
"v_mfma_f32_16x16x32_bf16 v[56:59], v[180:183], a[168:171], v[56:59]\n"
"buffer_load_dwordx4 v207, s[24:27], s66 offen lds\n"
"v_mfma_f32_16x16x32_bf16 v[52:55], v[180:183], a[164:167], v[52:55]\n"
"s_add_u32 m0, s59, 0x5280\n"
"v_permlane32_swap_b32 v239, v235\n"
"v_mfma_f32_16x16x32_bf16 v[48:51], v[180:183], a[160:163], v[48:51]\n"
"s_waitcnt lgkmcnt(1)\n"
"ds_read_b64_tr_b16 v[192:193], v201 offset:12672\n"
"v_max_f32 v239, v239, v235\n"
"v_mfma_f32_16x16x32_bf16 v[0:3], v[184:187], a[176:179], v[0:3]\n"
"ds_read_b64_tr_b16 v[194:195], v201 offset:12928\n"
"v_mul_f32 v239, s16, v239\n"
"v_mfma_f32_16x16x32_bf16 v[4:7], v[184:187], a[180:183], v[4:7]\n"
"ds_read_b64_tr_b16 v[196:197], v201 offset:12704\n"
"v_fma_f32 v218, v218, s16, -v239\n"
"v_mfma_f32_16x16x32_bf16 v[8:11], v[184:187], a[184:187], v[8:11]\n"
"v_exp_f32 v218, v218\n"
"v_mfma_f32_16x16x32_bf16 v[12:15], v[184:187], a[188:191], v[12:15]\n"
"s_waitcnt lgkmcnt(3)\n"
"buffer_load_dwordx4 v207, s[28:31], s66 offen lds\n"
"v_mfma_f32_16x16x32_bf16 v[28:31], v[188:191], a[188:191], v[28:31]\n"
"s_add_u32 s66, s60, 0x120100\n"
"ds_read_b64_tr_b16 v[198:199], v201 offset:12960\n"
"v_cvt_pk_bf16_f32 v218, v218, v218\n"
"v_mfma_f32_16x16x32_bf16 v[24:27], v[188:191], a[184:187], v[24:27]\n"
"s_add_u32 m0, s59, 0x6300\n"
"ds_read_b64_tr_b16 v[176:177], v200 offset:33792\n"
"v_and_b32 v68, v229, v218\n"
"v_mfma_f32_16x16x32_bf16 v[20:23], v[188:191], a[180:183], v[20:23]\n"
"ds_read_b64_tr_b16 v[178:179], v200 offset:34048\n"
"v_and_b32 v70, v231, v218\n"
"v_mfma_f32_16x16x32_bf16 v[16:19], v[188:191], a[176:179], v[16:19]\n"
"s_waitcnt lgkmcnt(3)\n"
"ds_read_b64_tr_b16 v[180:181], v200 offset:33824\n"
"v_fma_f32 v238, v238, s16, -v239\n"
"v_mfma_f32_16x16x32_bf16 v[32:35], v[192:195], a[176:179], v[32:35]\n"
"v_exp_f32 v238, v238\n"
"v_mfma_f32_16x16x32_bf16 v[36:39], v[192:195], a[180:183], v[36:39]\n"
"buffer_load_dwordx4 v207, s[24:27], s66 offen lds\n"
"v_mfma_f32_16x16x32_bf16 v[40:43], v[192:195], a[184:187], v[40:43]\n"
"s_add_u32 m0, s59, 0x7380\n"
"ds_read_b64_tr_b16 v[182:183], v200 offset:34080\n"
"v_cvt_pk_bf16_f32 v238, v238, v238\n"
"v_mfma_f32_16x16x32_bf16 v[44:47], v[192:195], a[188:191], v[44:47]\n"
"s_waitcnt lgkmcnt(3)\n"
"ds_read_b64_tr_b16 v[184:185], v200 offset:38016\n"
"v_and_b32 v69, v230, v238\n"
"v_mfma_f32_16x16x32_bf16 v[60:63], v[196:199], a[188:191], v[60:63]\n"
"ds_read_b64_tr_b16 v[186:187], v200 offset:38272\n"
"v_and_b32 v71, v232, v238\n"
"v_mfma_f32_16x16x32_bf16 v[56:59], v[196:199], a[184:187], v[56:59]\n"
"ds_read_b64_tr_b16 v[188:189], v200 offset:38048\n"
"v_cndmask_b32 v244, v244, v245, s[74:75]\n"
"v_mfma_f32_16x16x32_bf16 v[52:55], v[196:199], a[180:183], v[52:55]\n"
"ds_read_b64_tr_b16 v[190:191], v200 offset:38304\n"
"v_cndmask_b32 v214, v240, v244, s[76:77]\n"
"v_mfma_f32_16x16x32_bf16 v[252:255], v[118:121], v[68:71], 0\n"
"ds_read_b64_tr_b16 v[192:193], v200 offset:42240\n"
"v_cndmask_b32 v214, v214, v137, s[84:85]\n"
"v_mfma_f32_16x16x32_bf16 v[248:251], v[106:109], v[68:71], 0\n"
"ds_read_b64_tr_b16 v[194:195], v200 offset:42496\n"
"v_cndmask_b32 v242, v242, v243, s[74:75]\n"
"v_mfma_f32_16x16x32_bf16 v[48:51], v[196:199], a[176:179], v[48:51]\n"
"s_waitcnt lgkmcnt(7)\n"
"buffer_load_dwordx4 v207, s[28:31], s66 offen lds\n"
"v_mfma_f32_16x16x32_bf16 v[0:3], v[176:179], a[192:195], v[0:3]\n"
"s_add_u32 s66, s60, 0x168100\n"
"v_rcp_f32 v254, v254\n"
"v_mfma_f32_16x16x32_bf16 v[4:7], v[176:179], a[196:199], v[4:7]\n"
"s_add_u32 m0, s59, 0xc600\n"
"ds_read_b64_tr_b16 v[196:197], v200 offset:42272\n"
"v_mul_f32 v254, 0xbf317218, v254\n"
"v_mfma_f32_16x16x32_bf16 v[8:11], v[176:179], a[200:203], v[8:11]\n"
"ds_read_b64_tr_b16 v[198:199], v200 offset:42528\n"
"v_mul_f32 v248, v248, v254\n"
"v_mfma_f32_16x16x32_bf16 v[12:15], v[176:179], a[204:207], v[12:15]\n"
"s_waitcnt lgkmcnt(5)\n"
"ds_read_b64_tr_b16 v[176:177], v200 offset:46464\n"
"v_mul_f32 v249, v249, v254\n"
"v_mfma_f32_16x16x32_bf16 v[28:31], v[180:183], a[204:207], v[28:31]\n"
"ds_read_b64_tr_b16 v[178:179], v200 offset:46720\n"
"v_mul_f32 v250, v250, v254\n"
"v_mfma_f32_16x16x32_bf16 v[24:27], v[180:183], a[200:203], v[24:27]\n"
"s_mov_b64 exec, 0x1ff\n buffer_load_dword v215, v209, s[24:27], 0 offen offset:384\n s_mov_b64 exec, -1\n"
"v_mfma_f32_16x16x32_bf16 v[20:23], v[180:183], a[196:199], v[20:23]\n"
"v_mul_f32 v251, v251, v254\n"
"v_mfma_f32_16x16x32_bf16 v[16:19], v[180:183], a[192:195], v[16:19]\n"
"ds_read_b64_tr_b16 v[180:181], v200 offset:46496\n"
"v_mul_f32 v252, v252, v254\n"
"v_mfma_f32_16x16x32_bf16 v[32:35], v[184:187], a[192:195], v[32:35]\n"
"ds_read_b64_tr_b16 v[182:183], v200 offset:46752\n"
"v_mul_f32 v253, v253, v254\n"
"v_mfma_f32_16x16x32_bf16 v[36:39], v[184:187], a[196:199], v[36:39]\n"
"v_cvt_pk_bf16_f32 v68, v248, v249\n"
"v_mfma_f32_16x16x32_bf16 v[40:43], v[184:187], a[200:203], v[40:43]\n"
"s_mov_b64 exec, 0x1ff\n buffer_load_dword v215, v209, s[28:31], 0 offen offset:384\n s_mov_b64 exec, -1\n"
"v_mfma_f32_16x16x32_bf16 v[44:47], v[184:187], a[204:207], v[44:47]\n"
"s_waitcnt lgkmcnt(2)\n"
"v_cvt_pk_bf16_f32 v69, v250, v251\n"
"v_mfma_f32_16x16x32_bf16 v[60:63], v[188:191], a[204:207], v[60:63]\n"
"ds_write_b64 v203, v[68:69] offset:26432\n"
"v_mfma_f32_16x16x32_bf16 v[56:59], v[188:191], a[200:203], v[56:59]\n"
"v_cvt_pk_bf16_f32 v70, v252, v253\n"
"v_mfma_f32_16x16x32_bf16 v[248:251], v[88:91], v[76:79], 0\n"
"ds_write_b64 v203, v[70:71] offset:34880\n"
"v_mfma_f32_16x16x32_bf16 v[52:55], v[188:191], a[196:199], v[52:55]\n"
"v_cndmask_b32 v246, v246, v247, s[74:75]\n"
"v_mfma_f32_16x16x32_bf16 v[48:51], v[188:191], a[192:195], v[48:51]\n"
"v_cndmask_b32 v208, v242, v246, s[78:79]\n"
"v_mfma_f32_16x16x32_bf16 v[252:255], v[92:95], v[76:79], 0\n"
"v_cndmask_b32 v208, v208, v137, s[86:87]\n"
"v_mfma_f32_16x16x32_bf16 v[0:3], v[192:195], a[208:211], v[0:3]\n"
"v_max_f32 v216, v214, v208\n"
"v_mfma_f32_16x16x32_bf16 v[4:7], v[192:195], a[212:215], v[4:7]\n"
"v_mov_b32 v217, v216\n"
"v_mfma_f32_16x16x32_bf16 v[8:11], v[192:195], a[216:219], v[8:11]\n"
"v_add_u32 v204, 0xffff1900, v204\n"
"v_mfma_f32_16x16x32_bf16 v[12:15], v[192:195], a[220:223], v[12:15]\n"
"v_permlane16_swap_b32 v216, v217\n"
"v_mfma_f32_16x16x32_bf16 v[28:31], v[196:199], a[220:223], v[28:31]\n"
"v_max_f32 v216, v216, v217\n"
"v_mfma_f32_16x16x32_bf16 v[24:27], v[196:199], a[216:219], v[24:27]\n"
"s_waitcnt vmcnt(16)\n"
"v_readfirstlane_b32 s18, v236\n"
"v_mfma_f32_16x16x32_bf16 v[20:23], v[196:199], a[212:215], v[20:23]\n"
"s_barrier\n"
"v_mov_b32 v217, v216\n"
"v_mfma_f32_16x16x32_bf16 v[16:19], v[196:199], a[208:211], v[16:19]\n"
"ds_read_b64_tr_b16 v[184:185], v202 offset:8448\n"
"v_mov_b32 v236, s18\n"
"v_mfma_f32_16x16x32_bf16 v[32:35], v[176:179], a[208:211], v[32:35]\n"
"s_sub_u32 s18, s18, 63\n"
"v_permlane32_swap_b32 v216, v217\n"
"v_mfma_f32_16x16x32_bf16 v[36:39], v[176:179], a[212:215], v[36:39]\n"
"ds_read_b64_tr_b16 v[186:187], v202 offset:8704\n"
"v_xor_b32 v237, 0x5bd1e995, v236\n"
"v_mfma_f32_16x16x32_bf16 v[40:43], v[176:179], a[216:219], v[40:43]\n"
"ds_read_b64_tr_b16 v[188:189], v202 offset:8480\n"
"v_max_f32 v216, v216, v217\n"
"v_mfma_f32_16x16x32_bf16 v[44:47], v[176:179], a[220:223], v[44:47]\n"
"s_waitcnt lgkmcnt(3)\n"
"ds_read_b64_tr_b16 v[190:191], v202 offset:8736\n"
"v_cndmask_b32 v248, v248, v249, s[74:75]\n"
"v_mfma_f32_16x16x32_bf16 v[60:63], v[180:183], a[220:223], v[60:63]\n"
"ds_read_b64_tr_b16 v[192:193], v202 offset:12672\n"
"v_cndmask_b32 v252, v252, v253, s[74:75]\n"
"v_mfma_f32_16x16x32_bf16 v[56:59], v[180:183], a[216:219], v[56:59]\n"
"ds_read_b64_tr_b16 v[194:195], v202 offset:12928\n"
"v_cndmask_b32 v250, v250, v251, s[74:75]\n"
"v_mfma_f32_16x16x32_bf16 v[52:55], v[180:183], a[212:215], v[52:55]\n"
"ds_read_b64_tr_b16 v[196:197], v202 offset:12704\n"
"v_cndmask_b32 v254, v254, v255, s[74:75]\n"
"v_mfma_f32_16x16x32_bf16 v[48:51], v[180:183], a[208:211], v[48:51]\n"
"s_waitcnt lgkmcnt(5)\n"
"ds_read_b64_tr_b16 v[198:199], v202 offset:12960\n"
"v_cndmask_b32 v218, v248, v252, s[76:77]\n"
"v_mfma_f32_16x16x32_bf16 v[0:3], v[184:187], a[224:227], v[0:3]\n"
"s_add_u32 s74, s69, s68\n"
"ds_read_b64_tr_b16 v[176:177], v202 offset:25344\n"
"v_cndmask_b32 v238, v250, v254, s[78:79]\n"
"v_mfma_f32_16x16x32_bf16 v[4:7], v[184:187], a[228:231], v[4:7]\n"
"s_add_u32 s69, s67, 2\n"
"ds_read_b64_tr_b16 v[178:179], v202 offset:25600\n"
"v_max_f32 v239, v218, v238\n"
"v_mfma_f32_16x16x32_bf16 v[8:11], v[184:187], a[232:235], v[8:11]\n"
"s_and_b32 s69, s69, 3\n"
"ds_read_b64_tr_b16 v[180:181], v202 offset:25376\n"
"v_mov_b32 v235, v239\n"
"v_mfma_f32_16x16x32_bf16 v[12:15], v[184:187], a[236:239], v[12:15]\n"
"s_waitcnt lgkmcnt(5)\n"
"ds_read_b64_tr_b16 v[182:183], v202 offset:25632\n"
"v_mul_f32 v216, s16, v216\n"
"v_mfma_f32_16x16x32_bf16 v[28:31], v[188:191], a[236:239], v[28:31]\n"
"s_lshl_b32 s69, s69, 7\n"
"v_permlane16_swap_b32 v239, v235\n"
"v_mfma_f32_16x16x32_bf16 v[24:27], v[188:191], a[232:235], v[24:27]\n"
"s_add_u32 s75, s69, s68\n"
"ds_read_b64_tr_b16 v[184:185], v202 offset:29568\n"
"v_max_f32 v239, v239, v235\n"
"v_mfma_f32_16x16x32_bf16 v[20:23], v[188:191], a[228:231], v[20:23]\n"
"s_add_u32 s69, s67, 3\n"
"ds_read_b64_tr_b16 v[186:187], v202 offset:29824\n"
"v_mov_b32 v235, v239\n"
"v_mfma_f32_16x16x32_bf16 v[16:19], v[188:191], a[224:227], v[16:19]\n"
"s_and_b32 s69, s69, 3\n"
"ds_read_b64_tr_b16 v[188:189], v202 offset:29600\n"
"v_fma_f32 v214, v214, s16, -v216\n"
"v_mfma_f32_16x16x32_bf16 v[32:35], v[192:195], a[224:227], v[32:35]\n"
"s_lshl_b32 s69, s69, 7\n"
"v_permlane32_swap_b32 v239, v235\n"
"v_mfma_f32_16x16x32_bf16 v[36:39], v[192:195], a[228:231], v[36:39]\n"
"s_add_u32 s76, s69, s68\n"
"v_exp_f32 v214, v214\n"
"v_mfma_f32_16x16x32_bf16 v[40:43], v[192:195], a[232:235], v[40:43]\n"
"s_add_u32 s69, s67, 4\n"
"ds_read_b64_tr_b16 v[190:191], v202 offset:29856\n"
"v_max_f32 v239, v239, v235\n"
"v_mfma_f32_16x16x32_bf16 v[44:47], v[192:195], a[236:239], v[44:47]\n"
"s_waitcnt lgkmcnt(3)\n"
"ds_read_b64_tr_b16 v[192:193], v202 offset:33792\n"
"v_fma_f32 v208, v208, s16, -v216\n"
"v_mfma_f32_16x16x32_bf16 v[60:63], v[196:199], a[236:239], v[60:63]\n"
"s_and_b32 s69, s69, 3\n"
"v_exp_f32 v208, v208\n"
"v_mfma_f32_16x16x32_bf16 v[56:59], v[196:199], a[232:235], v[56:59]\n"
"s_lshl_b32 s69, s69, 7\n"
"ds_read_b64_tr_b16 v[194:195], v202 offset:34048\n"
"v_mul_f32 v239, s16, v239\n"
"v_mfma_f32_16x16x32_bf16 v[52:55], v[196:199], a[228:231], v[52:55]\n"
"s_add_u32 s77, s69, s68\n"
"s_lshl_b32 s67, s58, 1\n"
"v_fma_f32 v218, v218, s16, -v239\n"
"v_mfma_f32_16x16x32_bf16 v[48:51], v[196:199], a[224:227], v[48:51]\n"
"s_sub_u32 s67, 1, s67\n"
"v_exp_f32 v218, v218\n"
"v_mfma_f32_16x16x32_bf16 v[0:3], v[176:179], a[240:243], v[0:3]\n"
"s_mul_i32 s68, s67, 0x5a000\n"
"ds_read_b64_tr_b16 v[196:197], v202 offset:33824\n"
"v_fma_f32 v238, v238, s16, -v239\n"
"v_mfma_f32_16x16x32_bf16 v[4:7], v[176:179], a[244:247], v[4:7]\n"
"s_add_u32 s78, s63, s68\n"
"v_exp_f32 v238, v238\n"
"v_mfma_f32_16x16x32_bf16 v[8:11], v[176:179], a[248:251], v[8:11]\n"
"s_mul_i32 s68, s67, 0x9000\n"
"ds_read_b64_tr_b16 v[198:199], v202 offset:34080\n"
"v_cvt_pk_bf16_f32 v214, v214, v214\n"
"v_mfma_f32_16x16x32_bf16 v[12:15], v[176:179], a[252:255], v[12:15]\n"
"s_add_u32 s79, s64, s68\n"
"ds_read_b64_tr_b16 v[176:177], v202 offset:38016\n"
"v_cvt_pk_bf16_f32 v208, v208, v208\n"
"v_mfma_f32_16x16x32_bf16 v[28:31], v[180:183], a[252:255], v[28:31]\n"
"s_mul_i32 s68, s67, 0xa200\n"
"ds_read_b64_tr_b16 v[178:179], v202 offset:38272\n"
"v_and_b32 v72, v229, v214\n"
"v_mfma_f32_16x16x32_bf16 v[24:27], v[180:183], a[248:251], v[24:27]\n"
"s_add_u32 s80, s65, s68\n"
"s_xor_b32 s67, s56, s55\n"
"v_and_b32 v74, v231, v214\n"
"v_mfma_f32_16x16x32_bf16 v[20:23], v[180:183], a[244:247], v[20:23]\n"
"s_mul_i32 s67, s67, 36\n"
"s_add_u32 s67, s67, s54\n"
"v_and_b32 v73, v230, v208\n"
"v_mfma_f32_16x16x32_bf16 v[16:19], v[180:183], a[240:243], v[16:19]\n"
"s_waitcnt lgkmcnt(3)\n"
"ds_read_b64_tr_b16 v[180:181], v202 offset:38048\n"
"v_and_b32 v75, v232, v208\n"
"v_mfma_f32_16x16x32_bf16 v[32:35], v[184:187], a[240:243], v[32:35]\n"
"s_add_u32 s67, s67, 16\n"
"ds_read_b64_tr_b16 v[182:183], v202 offset:38304\n"
"v_cvt_pk_bf16_f32 v218, v218, v218\n"
"v_mfma_f32_16x16x32_bf16 v[36:39], v[184:187], a[244:247], v[36:39]\n"
"s_mul_i32 s81, s67, 0x480\n"
"s_lshl_b32 s67, s95, 1\n"
"v_cvt_pk_bf16_f32 v238, v238, v238\n"
"v_mfma_f32_16x16x32_bf16 v[244:247], v[122:125], v[72:75], 0\n"
"s_lshl_b32 s68, s58, 2\n"
"s_add_u32 s68, s68, s67\n"
"v_and_b32 v76, v229, v218\n"
"v_mfma_f32_16x16x32_bf16 v[240:243], v[110:113], v[72:75], 0\n"
"s_mul_i32 s67, s95, 6\n"
"s_mul_i32 s69, s56, 0x48\n"
"v_and_b32 v78, v231, v218\n"
"v_mfma_f32_16x16x32_bf16 v[40:43], v[184:187], a[248:251], v[40:43]\n"
"s_and_b32 s69, s69, 0\n"
"v_rcp_f32 v246, v246\n"
"v_mfma_f32_16x16x32_bf16 v[44:47], v[184:187], a[252:255], v[44:47]\n"
"s_lshl_b32 s84, s69, 24\n"
"s_lshl_b32 s69, s15, 1\n"
"v_and_b32 v77, v230, v238\n"
"v_mfma_f32_16x16x32_bf16 v[60:63], v[188:191], a[252:255], v[60:63]\n"
"v_and_b32 v79, v232, v238\n"
"v_mfma_f32_16x16x32_bf16 v[56:59], v[188:191], a[248:251], v[56:59]\n"
"v_mul_f32 v246, 0xbf317218, v246\n"
"v_mfma_f32_16x16x32_bf16 v[52:55], v[188:191], a[244:247], v[52:55]\n"
"v_mul_f32 v240, v240, v246\n"
"v_mfma_f32_16x16x32_bf16 v[252:255], v[124:127], v[76:79], 0\n"
"v_mul_f32 v241, v241, v246\n"
"v_mfma_f32_16x16x32_bf16 v[248:251], v[112:115], v[76:79], 0\n"
"v_mul_f32 v242, v242, v246\n"
"v_mfma_f32_16x16x32_bf16 v[48:51], v[188:191], a[240:243], v[48:51]\n"
"v_mul_f32 v243, v243, v246\n"
"v_mfma_f32_16x16x32_bf16 v[0:3], v[192:195], v[144:147], v[0:3]\n"
"v_mul_f32 v244, v244, v246\n"
"v_mfma_f32_16x16x32_bf16 v[4:7], v[192:195], v[148:151], v[4:7]\n"
"v_rcp_f32 v254, v254\n"
"v_mfma_f32_16x16x32_bf16 v[8:11], v[192:195], v[152:155], v[8:11]\n"
"v_mul_f32 v245, v245, v246\n"
"v_mfma_f32_16x16x32_bf16 v[12:15], v[192:195], v[156:159], v[12:15]\n"
"s_waitcnt lgkmcnt(0)\n"
"v_mul_f32 v254, 0xbf317218, v254\n"
"v_mfma_f32_16x16x32_bf16 v[28:31], v[196:199], v[156:159], v[28:31]\n"
"v_cvt_pk_bf16_f32 v72, v240, v241\n"
"v_mfma_f32_16x16x32_bf16 v[24:27], v[196:199], v[152:155], v[24:27]\n"
"s_waitcnt vmcnt(10)\n"
"v_cvt_pk_bf16_f32 v73, v242, v243\n"
"v_mfma_f32_16x16x32_bf16 v[20:23], v[196:199], v[148:151], v[20:23]\n"
"s_barrier\n"
"buffer_store_dwordx2 v[68:69], v140, s[44:47], s52 offen offset:0 sc0 sc1\n"
"v_cvt_pk_bf16_f32 v74, v244, v245\n"
"v_mfma_f32_16x16x32_bf16 v[16:19], v[196:199], v[144:147], v[16:19]\n"
"ds_write_b64 v204, v[72:73] offset:25344\n"
"v_mfma_f32_16x16x32_bf16 v[32:35], v[176:179], v[144:147], v[32:35]\n"
"buffer_load_dwordx4 v207, s[24:27], s66 offen lds\n"
"v_mfma_f32_16x16x32_bf16 v[36:39], v[176:179], v[148:151], v[36:39]\n"
"s_add_u32 m0, s59, 0xd680\n"
"ds_write_b64 v204, v[74:75] offset:33792\n"
"v_mfma_f32_16x16x32_bf16 v[40:43], v[176:179], v[152:155], v[40:43]\n"
"ds_read_b64_tr_b16 v[184:185], v201 offset:16896\n"
"v_mul_f32 v248, v248, v254\n"
"v_mfma_f32_16x16x32_bf16 v[44:47], v[176:179], v[156:159], v[44:47]\n"
"ds_read_b64_tr_b16 v[186:187], v201 offset:17152\n"
"v_mul_f32 v249, v249, v254\n"
"v_mfma_f32_16x16x32_bf16 v[60:63], v[180:183], v[156:159], v[60:63]\n"
"ds_read_b64_tr_b16 v[188:189], v201 offset:16928\n"
"v_mul_f32 v250, v250, v254\n"
"v_mfma_f32_16x16x32_bf16 v[56:59], v[180:183], v[152:155], v[56:59]\n"
"ds_read_b64_tr_b16 v[190:191], v201 offset:17184\n"
"v_mul_f32 v251, v251, v254\n"
"v_mfma_f32_16x16x32_bf16 v[52:55], v[180:183], v[148:151], v[52:55]\n"
"buffer_store_dword v70, v141, s[44:47], s52 offen offset:512 sc0 sc1\n"
"v_mul_f32 v252, v252, v254\n"
"v_mfma_f32_16x16x32_bf16 v[48:51], v[180:183], v[144:147], v[48:51]\n"
"s_waitcnt lgkmcnt(2)\n"
"ds_read_b64_tr_b16 v[192:193], v201 offset:21120\n"
"v_mul_f32 v253, v253, v254\n"
"v_mfma_f32_16x16x32_bf16 v[0:3], v[184:187], v[160:163], v[0:3]\n"
"s_add_u32 s52, s71, 0x1000000\n"
"buffer_load_dwordx4 v207, s[28:31], s66 offen lds\n"
"v_mfma_f32_16x16x32_bf16 v[4:7], v[184:187], v[164:167], v[4:7]\n"
"s_add_u32 s66, s60, 0x1b0100\n"
"s_add_u32 m0, s59, 0x8400\n"
"ds_read_b64_tr_b16 v[194:195], v201 offset:21376\n"
"v_mfma_f32_16x16x32_bf16 v[8:11], v[184:187], v[168:171], v[8:11]\n"
"ds_read_b64_tr_b16 v[196:197], v201 offset:21152\n"
"v_cvt_pk_bf16_f32 v76, v248, v249\n"
"v_mfma_f32_16x16x32_bf16 v[12:15], v[184:187], v[172:175], v[12:15]\n"
"s_waitcnt lgkmcnt(3)\n"
"ds_read_b64_tr_b16 v[198:199], v201 offset:21408\n"
"v_cvt_pk_bf16_f32 v77, v250, v251\n"
"v_mfma_f32_16x16x32_bf16 v[28:31], v[188:191], v[172:175], v[28:31]\n"
"ds_write_b64 v204, v[76:77] offset:26432\n"
"v_mfma_f32_16x16x32_bf16 v[24:27], v[188:191], v[168:171], v[24:27]\n"
"ds_read_b64_tr_b16 v[176:177], v202 offset:16896\n"
"v_cvt_pk_bf16_f32 v78, v252, v253\n"
"v_mfma_f32_16x16x32_bf16 v[20:23], v[188:191], v[164:167], v[20:23]\n"
"buffer_store_dwordx2 v[72:73], v140, s[44:47], s52 offen offset:0 sc0 sc1\n"
"v_and_b32 v229, 7, v233\n"
"v_mfma_f32_16x16x32_bf16 v[16:19], v[188:191], v[160:163], v[16:19]\n"
"s_waitcnt lgkmcnt(4)\n"
"ds_write_b64 v204, v[78:79] offset:34880\n"
"v_mfma_f32_16x16x32_bf16 v[32:35], v[192:195], v[160:163], v[32:35]\n"
"buffer_load_dwordx4 v207, s[24:27], s66 offen lds\n"
"v_mfma_f32_16x16x32_bf16 v[36:39], v[192:195], v[164:167], v[36:39]\n"
"s_add_u32 m0, s59, 0x9480\n"
"ds_read_b64_tr_b16 v[178:179], v202 offset:17152\n"
"v_xor_b32 v232, s68, v229\n"
"v_mfma_f32_16x16x32_bf16 v[40:43], v[192:195], v[168:171], v[40:43]\n"
"s_mul_i32 s68, s56, 0xa200\n"
"ds_read_b64_tr_b16 v[180:181], v202 offset:16928\n"
"v_lshlrev_b32 v232, 4, v232\n"
"v_mfma_f32_16x16x32_bf16 v[44:47], v[192:195], v[172:175], v[44:47]\n"
"s_waitcnt lgkmcnt(3)\n"
"ds_read_b64_tr_b16 v[182:183], v202 offset:17184\n"
"v_cmp_gt_u32 s[10:11], 4, v229\n"
"v_mfma_f32_16x16x32_bf16 v[60:63], v[196:199], v[172:175], v[60:63]\n"
"ds_read_b64_tr_b16 v[184:185], v202 offset:21120\n"
"v_mov_b32 v138, 0x40000000\n"
"v_mfma_f32_16x16x32_bf16 v[56:59], v[196:199], v[168:171], v[56:59]\n"
"ds_read_b64_tr_b16 v[186:187], v202 offset:21376\n"
"v_cndmask_b32 v232, v138, v232, s[10:11]\n"
"v_mfma_f32_16x16x32_bf16 v[52:55], v[196:199], v[164:167], v[52:55]\n"
"buffer_store_dword v74, v141, s[44:47], s52 offen offset:512 sc0 sc1\n"
"v_xor_b32 v229, s67, v229\n"
"v_mfma_f32_16x16x32_bf16 v[48:51], v[196:199], v[160:163], v[48:51]\n"
"s_waitcnt lgkmcnt(4)\n"
"ds_read_b64_tr_b16 v[188:189], v202 offset:21152\n"
"v_lshlrev_b32 v229, 4, v229\n"
"v_mfma_f32_16x16x32_bf16 v[64:67], v[176:179], a[0:3], 0\n"
"s_add_u32 s52, s71, 0x1200000\n"
"s_mul_i32 s67, s58, 0xfffffdc0\n"
"buffer_load_dwordx4 v207, s[28:31], s66 offen lds\n"
"v_mfma_f32_16x16x32_bf16 v[68:71], v[176:179], a[4:7], 0\n"
"s_add_u32 s66, s60, 0x1f8100\n"
"s_add_u32 m0, s59, 0xe700\n"
"ds_read_b64_tr_b16 v[190:191], v202 offset:21408\n"
"v_add_u32 v216, v211, v232\n"
"v_mfma_f32_16x16x32_bf16 v[72:75], v[176:179], a[8:11], 0\n"
"s_add_u32 s67, s67, 0x200\n"
"ds_read_b64_tr_b16 v[192:193], v200 offset:0\n"
"v_add_u32 v211, v211, v229\n"
"buffer_store_dwordx2 v[76:77], v140, s[44:47], s52 offen offset:0 sc0 sc1\n"
"buffer_store_dword v78, v141, s[44:47], s52 offen offset:512 sc0 sc1\n"
"s_nop 1\n"
"v_mfma_f32_16x16x32_bf16 v[76:79], v[176:179], a[12:15], 0\n"
"L_st5_%=:\n s_cbranch_execz L_st9_%=\n"
"s_waitcnt lgkmcnt(3)\n"
"ds_read_b64_tr_b16 v[194:195], v200 offset:256\n"
"v_add_u32 v217, v212, v232\n"
"v_mfma_f32_16x16x32_bf16 v[92:95], v[180:183], a[12:15], 0\n"
"s_add_u32 s52, s70, 0x400000\n"
"ds_read_b64_tr_b16 v[196:197], v200 offset:32\n"
"v_add_u32 v212, v212, v229\n"
"v_mfma_f32_16x16x32_bf16 v[88:91], v[180:183], a[8:11], 0\n"
"buffer_load_dwordx4 v207, s[24:27], s66 offen lds\n"
"v_mfma_f32_16x16x32_bf16 v[84:87], v[180:183], a[4:7], 0\n"
"s_add_u32 m0, s59, 0xf780\n"
"ds_read_b64_tr_b16 v[198:199], v200 offset:288\n"
"v_add_u32 v218, v213, v232\n"
"v_mfma_f32_16x16x32_bf16 v[80:83], v[180:183], a[0:3], 0\n"
"ds_read_b64_tr_b16 v[176:177], v200 offset:4224\n"
"v_add_u32 v213, v213, v229\n"
"v_mfma_f32_16x16x32_bf16 v[96:99], v[184:187], a[0:3], 0\n"
"ds_read_b64_tr_b16 v[178:179], v200 offset:4480\n"
"v_add_u32 v209, s67, v209\n"
"v_mfma_f32_16x16x32_bf16 v[100:103], v[184:187], a[4:7], 0\n"
"s_mul_i32 s67, s73, 0x80\n"
"ds_read_b64_tr_b16 v[180:181], v200 offset:4256\n"
"v_mul_f32 v240, v220, v0\n"
"v_mfma_f32_16x16x32_bf16 v[104:107], v[184:187], a[8:11], 0\n"
"s_mul_i32 s73, s56, 0x51000\n"
"buffer_load_dwordx4 v207, s[28:31], s66 offen lds\n"
"v_mfma_f32_16x16x32_bf16 v[108:111], v[184:187], a[12:15], 0\n"
"s_waitcnt lgkmcnt(4)\n"
"v_exp_f32 v240, v240\n"
"v_mfma_f32_16x16x32_bf16 v[124:127], v[188:191], a[12:15], 0\n"
"s_add_u32 s66, s60, 0x240100\n"
"ds_read_b64_tr_b16 v[182:183], v200 offset:4512\n"
"v_fma_f32 v240, v240, v224, v224\n"
"v_mfma_f32_16x16x32_bf16 v[120:123], v[188:191], a[8:11], 0\n"
"s_add_u32 m0, s59, 0x10800\n"
"v_rcp_f32 v240, v240\n"
"v_mfma_f32_16x16x32_bf16 v[116:119], v[188:191], a[4:7], 0\n"
"v_mul_f32 v32, v0, v32\n"
"v_mfma_f32_16x16x32_bf16 v[112:115], v[188:191], a[0:3], 0\n"
"buffer_load_dwordx4 v207, s[24:27], s66 offen lds\n"
"v_mfma_f32_16x16x32_bf16 v[64:67], v[192:195], a[16:19], v[64:67]\n"
"s_add_u32 m0, s59, 0x11880\n"
"v_mul_f32 v32, v32, v240\n"
"v_mfma_f32_16x16x32_bf16 v[68:71], v[192:195], a[20:23], v[68:71]\n"
"v_mul_f32 v241, v220, v1\n"
"v_mfma_f32_16x16x32_bf16 v[72:75], v[192:195], a[24:27], v[72:75]\n"
"v_exp_f32 v241, v241\n"
"v_mfma_f32_16x16x32_bf16 v[76:79], v[192:195], a[28:31], v[76:79]\n"
"s_waitcnt lgkmcnt(0)\n"
"v_fma_f32 v241, v241, v224, v224\n"
"v_mfma_f32_16x16x32_bf16 v[92:95], v[196:199], a[28:31], v[92:95]\n"
"v_rcp_f32 v241, v241\n"
"v_mfma_f32_16x16x32_bf16 v[88:91], v[196:199], a[24:27], v[88:91]\n"
"s_waitcnt vmcnt(15)\n"
"buffer_load_dwordx4 v207, s[28:31], s66 offen lds\n"
"v_mfma_f32_16x16x32_bf16 v[84:87], v[196:199], a[20:23], v[84:87]\n"
"s_barrier\n"
"s_add_u32 s66, s60, 0x288100\n"
"v_mul_f32 v33, v1, v33\n"
"v_mfma_f32_16x16x32_bf16 v[80:83], v[196:199], a[16:19], v[80:83]\n"
"s_add_u32 m0, s59, 0xa500\n"
"ds_read_b64_tr_b16 v[184:185], v200 offset:8448\n"
"v_mul_f32 v33, v33, v241\n"
"v_mfma_f32_16x16x32_bf16 v[96:99], v[176:179], a[16:19], v[96:99]\n"
"ds_read_b64_tr_b16 v[186:187], v200 offset:8704\n"
"v_cvt_pk_bf16_f32 v248, v32, v33\n"
"v_mfma_f32_16x16x32_bf16 v[100:103], v[176:179], a[20:23], v[100:103]\n"
"ds_read_b64_tr_b16 v[188:189], v200 offset:8480\n"
"v_mul_f32 v242, v220, v2\n"
"v_mfma_f32_16x16x32_bf16 v[104:107], v[176:179], a[24:27], v[104:107]\n"
"v_exp_f32 v242, v242\n"
"v_mfma_f32_16x16x32_bf16 v[108:111], v[176:179], a[28:31], v[108:111]\n"
"buffer_load_dwordx4 v207, s[24:27], s66 offen lds\n"
"v_mfma_f32_16x16x32_bf16 v[124:127], v[180:183], a[28:31], v[124:127]\n"
"s_add_u32 m0, s59, 0xb580\n"
"ds_read_b64_tr_b16 v[190:191], v200 offset:8736\n"
"v_fma_f32 v242, v242, v224, v224\n"
"v_mfma_f32_16x16x32_bf16 v[120:123], v[180:183], a[24:27], v[120:123]\n"
"v_rcp_f32 v242, v242\n"
"v_mfma_f32_16x16x32_bf16 v[116:119], v[180:183], a[20:23], v[116:119]\n"
"ds_read_b64_tr_b16 v[192:193], v200 offset:12672\n"
"v_mul_f32 v34, v2, v34\n"
"v_mfma_f32_16x16x32_bf16 v[112:115], v[180:183], a[16:19], v[112:115]\n"
"s_waitcnt lgkmcnt(2)\n"
"ds_read_b64_tr_b16 v[194:195], v200 offset:12928\n"
"v_mul_f32 v34, v34, v242\n"
"v_mfma_f32_16x16x32_bf16 v[64:67], v[184:187], a[32:35], v[64:67]\n"
"ds_read_b64_tr_b16 v[196:197], v200 offset:12704\n"
"v_mul_f32 v243, v220, v3\n"
"v_mfma_f32_16x16x32_bf16 v[68:71], v[184:187], a[36:39], v[68:71]\n"
"buffer_load_dwordx4 v207, s[28:31], s66 offen lds\n"
"v_mfma_f32_16x16x32_bf16 v[72:75], v[184:187], a[40:43], v[72:75]\n"
"s_add_u32 s66, s60, 0x2d0100\n"
"s_add_u32 m0, s59, 0x18c00\n"
"v_exp_f32 v243, v243\n"
"v_mfma_f32_16x16x32_bf16 v[76:79], v[184:187], a[44:47], v[76:79]\n"
"s_waitcnt lgkmcnt(3)\n"
"ds_read_b64_tr_b16 v[198:199], v200 offset:12960\n"
"v_fma_f32 v243, v243, v224, v224\n"
"v_mfma_f32_16x16x32_bf16 v[92:95], v[188:191], a[44:47], v[92:95]\n"
"v_rcp_f32 v243, v243\n"
"v_mfma_f32_16x16x32_bf16 v[88:91], v[188:191], a[40:43], v[88:91]\n"
"ds_read_b64_tr_b16 v[176:177], v200 offset:16896\n"
"v_mul_f32 v35, v3, v35\n"
"v_mfma_f32_16x16x32_bf16 v[84:87], v[188:191], a[36:39], v[84:87]\n"
"ds_read_b64_tr_b16 v[178:179], v200 offset:17152\n"
"v_mul_f32 v35, v35, v243\n"
"v_mfma_f32_16x16x32_bf16 v[80:83], v[188:191], a[32:35], v[80:83]\n"
"s_waitcnt lgkmcnt(3)\n"
"buffer_load_dwordx4 v207, s[24:27], s66 offen lds\n"
"v_mfma_f32_16x16x32_bf16 v[96:99], v[192:195], a[32:35], v[96:99]\n"
"s_add_u32 m0, s59, 0x19c80\n"
"ds_read_b64_tr_b16 v[180:181], v200 offset:16928\n"
"v_cvt_pk_bf16_f32 v249, v34, v35\n"
"v_mfma_f32_16x16x32_bf16 v[100:103], v[192:195], a[36:39], v[100:103]\n"
"ds_read_b64_tr_b16 v[182:183], v200 offset:17184\n"
"v_mul_f32 v244, v220, v16\n"
"v_mfma_f32_16x16x32_bf16 v[104:107], v[192:195], a[40:43], v[104:107]\n"
"v_exp_f32 v244, v244\n"
"v_mfma_f32_16x16x32_bf16 v[108:111], v[192:195], a[44:47], v[108:111]\n"
"s_waitcnt lgkmcnt(3)\n"
"ds_read_b64_tr_b16 v[184:185], v200 offset:21120\n"
"v_fma_f32 v244, v244, v224, v224\n"
"v_mfma_f32_16x16x32_bf16 v[124:127], v[196:199], a[44:47], v[124:127]\n"
"v_rcp_f32 v244, v244\n"
"v_mfma_f32_16x16x32_bf16 v[120:123], v[196:199], a[40:43], v[120:123]\n"
"buffer_load_dwordx4 v207, s[28:31], s66 offen lds\n"
"v_mfma_f32_16x16x32_bf16 v[116:119], v[196:199], a[36:39], v[116:119]\n"
"s_add_u32 s66, s60, 0x318100\n"
"ds_read_b64_tr_b16 v[186:187], v200 offset:21376\n"
"v_mul_f32 v48, v16, v48\n"
"v_mfma_f32_16x16x32_bf16 v[112:115], v[196:199], a[32:35], v[112:115]\n"
"s_waitcnt lgkmcnt(2)\n"
"ds_read_b64_tr_b16 v[188:189], v200 offset:21152\n"
"v_mul_f32 v48, v48, v244\n"
"v_mfma_f32_16x16x32_bf16 v[64:67], v[176:179], a[48:51], v[64:67]\n"
"s_add_u32 m0, s59, 0x1ad00\n"
"ds_read_b64_tr_b16 v[190:191], v200 offset:21408\n"
"v_mul_f32 v245, v220, v17\n"
"v_mfma_f32_16x16x32_bf16 v[68:71], v[176:179], a[52:55], v[68:71]\n"
"v_exp_f32 v245, v245\n"
"v_mfma_f32_16x16x32_bf16 v[72:75], v[176:179], a[56:59], v[72:75]\n"
"ds_read_b64_tr_b16 v[192:193], v200 offset:25344\n"
"v_fma_f32 v245, v245, v224, v224\n"
"v_mfma_f32_16x16x32_bf16 v[76:79], v[176:179], a[60:63], v[76:79]\n"
"buffer_load_dwordx4 v207, s[24:27], s66 offen lds\n"
"v_mfma_f32_16x16x32_bf16 v[92:95], v[180:183], a[60:63], v[92:95]\n"
"s_add_u32 m0, s59, 0x1bd80\n"
"v_rcp_f32 v245, v245\n"
"v_mfma_f32_16x16x32_bf16 v[88:91], v[180:183], a[56:59], v[88:91]\n"
"ds_read_b64_tr_b16 v[194:195], v200 offset:25600\n"
"v_lshrrev_b32 v235, 4, v233\n"
"v_mfma_f32_16x16x32_bf16 v[84:87], v[180:183], a[52:55], v[84:87]\n"
"ds_read_b64_tr_b16 v[196:197], v200 offset:25376\n"
"v_mul_f32 v49, v17, v49\n"
"v_mfma_f32_16x16x32_bf16 v[80:83], v[180:183], a[48:51], v[80:83]\n"
"s_waitcnt lgkmcnt(3)\n"
"ds_read_b64_tr_b16 v[198:199], v200 offset:25632\n"
"v_mov_b32 v232, 0x3f80\n"
"v_mfma_f32_16x16x32_bf16 v[96:99], v[184:187], a[48:51], v[96:99]\n"
"ds_read_b64_tr_b16 v[176:177], v200 offset:29568\n"
"v_mul_f32 v49, v49, v245\n"
"v_mfma_f32_16x16x32_bf16 v[100:103], v[184:187], a[52:55], v[100:103]\n"
"buffer_load_dwordx4 v207, s[28:31], s66 offen lds\n"
"v_mfma_f32_16x16x32_bf16 v[104:107], v[184:187], a[56:59], v[104:107]\n"
"s_add_u32 s66, s60, 0x360100\n"
"ds_read_b64_tr_b16 v[178:179], v200 offset:29824\n"
"v_xor_b32 v138, v233, v235\n"
"v_mfma_f32_16x16x32_bf16 v[108:111], v[184:187], a[60:63], v[108:111]\n"
"s_add_u32 m0, s59, 0x1ce00\n"
"ds_read_b64_tr_b16 v[180:181], v200 offset:29600\n"
"v_lshrrev_b32 v132, 2, v233\n"
"v_mfma_f32_16x16x32_bf16 v[124:127], v[188:191], a[60:63], v[124:127]\n"
"ds_read_b64_tr_b16 v[182:183], v200 offset:29856\n"
"v_mul_u32_u24 v132, 0x480, v132\n"
"v_mfma_f32_16x16x32_bf16 v[120:123], v[188:191], a[56:59], v[120:123]\n"
"v_cvt_pk_bf16_f32 v250, v48, v49\n"
"v_mfma_f32_16x16x32_bf16 v[116:119], v[188:191], a[52:55], v[116:119]\n"
"v_mul_f32 v246, v220, v18\n"
"v_mfma_f32_16x16x32_bf16 v[112:115], v[188:191], a[48:51], v[112:115]\n"
"s_waitcnt lgkmcnt(3)\n"
"buffer_load_dwordx4 v207, s[24:27], s66 offen lds\n"
"v_mfma_f32_16x16x32_bf16 v[64:67], v[192:195], a[64:67], v[64:67]\n"
"s_add_u32 m0, s59, 0x1de80\n"
"v_exp_f32 v246, v246\n"
"v_mfma_f32_16x16x32_bf16 v[68:71], v[192:195], a[68:71], v[68:71]\n"
"v_fma_f32 v246, v246, v224, v224\n"
"v_mul_f32 v50, v18, v50\n"
"v_mfma_f32_16x16x32_bf16 v[72:75], v[192:195], a[72:75], v[72:75]\n"
"v_rcp_f32 v246, v246\n"
"v_mfma_f32_16x16x32_bf16 v[76:79], v[192:195], a[76:79], v[76:79]\n"
"v_mul_f32 v50, v50, v246\n"
"v_mfma_f32_16x16x32_bf16 v[92:95], v[196:199], a[76:79], v[92:95]\n"
"v_mul_f32 v247, v220, v19\n"
"v_mul_f32 v51, v19, v51\n"
"v_mfma_f32_16x16x32_bf16 v[88:91], v[196:199], a[72:75], v[88:91]\n"
"s_waitcnt vmcnt(9)\n"
"buffer_load_dwordx4 v207, s[28:31], s66 offen lds\n"
"v_mfma_f32_16x16x32_bf16 v[84:87], v[196:199], a[68:71], v[84:87]\n"
"s_barrier\n"
"s_add_u32 s66, s60, 0x3a8100\n"
"v_exp_f32 v247, v247\n"
"v_mfma_f32_16x16x32_bf16 v[80:83], v[196:199], a[64:67], v[80:83]\n"
"s_waitcnt lgkmcnt(0)\n"
"s_add_u32 m0, s59, 0x1ef00\n"
"ds_read_b64_tr_b16 v[184:185], v200 offset:50688\n"
"v_fma_f32 v247, v247, v224, v224\n"
"v_mfma_f32_16x16x32_bf16 v[96:99], v[176:179], a[64:67], v[96:99]\n"
"v_rcp_f32 v247, v247\n"
"v_mfma_f32_16x16x32_bf16 v[100:103], v[176:179], a[68:71], v[100:103]\n"
"ds_read_b64_tr_b16 v[186:187], v200 offset:50944\n"
"v_mul_f32 v51, v51, v247\n"
"v_mfma_f32_16x16x32_bf16 v[104:107], v[176:179], a[72:75], v[104:107]\n"
"ds_read_b64_tr_b16 v[188:189], v200 offset:50720\n"
"v_cvt_pk_bf16_f32 v251, v50, v51\n"
"v_mfma_f32_16x16x32_bf16 v[108:111], v[176:179], a[76:79], v[108:111]\n"
"buffer_store_dwordx4 v[248:251], v139, s[44:47], s52 offen offset:0 sc0 sc1\n"
"v_add_u32 v205, 0xe700, v205\n"
"v_mfma_f32_16x16x32_bf16 v[124:127], v[180:183], a[76:79], v[124:127]\n"
"s_add_u32 s52, s70, 0x600000\n"
"ds_write_b128 v205, v[248:251] offset:25344\n"
"v_mfma_f32_16x16x32_bf16 v[120:123], v[180:183], a[72:75], v[120:123]\n"
"ds_read_b64_tr_b16 v[190:191], v200 offset:50976\n"
"v_mul_f32 v240, v221, v4\n"
"v_mfma_f32_16x16x32_bf16 v[116:119], v[180:183], a[68:71], v[116:119]\n"
"v_exp_f32 v240, v240\n"
"v_mfma_f32_16x16x32_bf16 v[112:115], v[180:183], a[64:67], v[112:115]\n"
"s_waitcnt lgkmcnt(3)\n"
"s_mov_b64 exec, 0x1ff\n buffer_load_dword v215, v209, s[32:35], 0 offen offset:0\n s_mov_b64 exec, -1\n"
"v_mfma_f32_16x16x32_bf16 v[64:67], v[184:187], a[80:83], v[64:67]\n"
"ds_read_b64_tr_b16 v[192:193], v200 offset:54912\n"
"v_mul_u32_u24 v209, 0x2000, v233\n"
"v_mfma_f32_16x16x32_bf16 v[68:71], v[184:187], a[84:87], v[68:71]\n"
"ds_read_b64_tr_b16 v[194:195], v200 offset:55168\n"
"v_add_u32 v209, s67, v209\n"
"v_mfma_f32_16x16x32_bf16 v[72:75], v[184:187], a[88:91], v[72:75]\n"
"s_xor_b32 s67, s95, 0\n"
"ds_read_b64_tr_b16 v[196:197], v200 offset:54944\n"
"v_subrev_u32 v229, 0xa200, v209\n"
"v_mfma_f32_16x16x32_bf16 v[76:79], v[184:187], a[92:95], v[76:79]\n"
"s_waitcnt lgkmcnt(3)\n"
"ds_read_b64_tr_b16 v[198:199], v200 offset:55200\n"
"v_ashrrev_i32 v229, 31, v229\n"
"v_mfma_f32_16x16x32_bf16 v[92:95], v[188:191], a[92:95], v[92:95]\n"
"s_add_u32 s69, s69, s67\n"
"s_mul_i32 s67, s67, 0x440\n"
"buffer_load_dwordx4 v207, s[24:27], s66 offen lds\n"
"v_mfma_f32_16x16x32_bf16 v[88:91], v[188:191], a[88:91], v[88:91]\n"
"s_add_u32 m0, s59, 0x1ff80\n"
"ds_read_b64_tr_b16 v[176:177], v200 offset:33792\n"
"v_bfi_b32 v229, v229, v209, 2.0\n"
"v_mfma_f32_16x16x32_bf16 v[84:87], v[188:191], a[84:87], v[84:87]\n"
"ds_read_b64_tr_b16 v[178:179], v200 offset:34048\n"
"v_add_u32 v229, s84, v229\n"
"v_mfma_f32_16x16x32_bf16 v[80:83], v[188:191], a[80:83], v[80:83]\n"
"s_waitcnt lgkmcnt(4)\n"
"ds_read_b64_tr_b16 v[180:181], v200 offset:33824\n"
"v_subrev_u32 v230, 0x51000, v209\n"
"v_mfma_f32_16x16x32_bf16 v[96:99], v[192:195], a[80:83], v[96:99]\n"
"ds_read_b64_tr_b16 v[182:183], v200 offset:34080\n"
"v_ashrrev_i32 v230, 31, v230\n"
"v_mfma_f32_16x16x32_bf16 v[100:103], v[192:195], a[84:87], v[100:103]\n"
"ds_read_b64_tr_b16 v[184:185], v200 offset:38016\n"
"v_bfi_b32 v230, v230, v209, 2.0\n"
"v_mfma_f32_16x16x32_bf16 v[104:107], v[192:195], a[88:91], v[104:107]\n"
"s_mov_b64 exec, 0x3f\n buffer_load_dword v215, v229, s[40:43], s68 offen offset:0\n s_mov_b64 exec, -1\n"
"v_mfma_f32_16x16x32_bf16 v[108:111], v[192:195], a[92:95], v[108:111]\n"
"s_waitcnt lgkmcnt(4)\n"
"ds_read_b64_tr_b16 v[186:187], v200 offset:38272\n"
"v_add_u32 v230, s84, v230\n"
"v_mfma_f32_16x16x32_bf16 v[124:127], v[196:199], a[92:95], v[124:127]\n"
"s_mul_i32 s68, s15, 0x840\n"
"ds_read_b64_tr_b16 v[188:189], v200 offset:38048\n"
"v_add_u32 v209, 0x2a000, v209\n"
"v_mfma_f32_16x16x32_bf16 v[120:123], v[196:199], a[88:91], v[120:123]\n"
"s_add_u32 s67, s67, s68\n"
"ds_read_b64_tr_b16 v[190:191], v200 offset:38304\n"
"v_bfe_u32 v229, v233, 2, 2\n"
"v_mfma_f32_16x16x32_bf16 v[116:119], v[196:199], a[84:87], v[116:119]\n"
"s_add_u32 s67, s67, 0xe700\n"
"ds_read_b64_tr_b16 v[192:193], v201 offset:0\n"
"v_cmp_eq_u32 s[10:11], v229, v235\n"
"v_mfma_f32_16x16x32_bf16 v[112:115], v[196:199], a[80:83], v[112:115]\n"
"s_waitcnt lgkmcnt(4)\n"
"s_sub_u32 s68, s89, s69\n"
"s_mov_b32 exec_lo, 0x1fffff\n s_mov_b32 exec_hi, 0x0\n buffer_load_dword v215, v230, s[36:39], s73 offen offset:0\n s_mov_b64 exec, -1\n"
"v_mfma_f32_16x16x32_bf16 v[64:67], v[176:179], a[96:99], v[64:67]\n"
"s_sub_u32 s68, s68, 1\n"
"ds_read_b64_tr_b16 v[194:195], v201 offset:256\n"
"v_subrev_u32 v230, 0x51000, v209\n"
"v_mfma_f32_16x16x32_bf16 v[68:71], v[176:179], a[100:103], v[68:71]\n"
"s_and_b32 s68, s68, 7\n"
"ds_read_b64_tr_b16 v[196:197], v201 offset:32\n"
"v_ashrrev_i32 v230, 31, v230\n"
"v_mfma_f32_16x16x32_bf16 v[72:75], v[176:179], a[104:107], v[72:75]\n"
"s_lshl_b32 s68, s68, 8\n"
"ds_read_b64_tr_b16 v[198:199], v201 offset:288\n"
"v_bfi_b32 v230, v230, v209, 2.0\n"
"v_mfma_f32_16x16x32_bf16 v[76:79], v[176:179], a[108:111], v[76:79]\n"
"ds_read_b64_tr_b16 v[176:177], v201 offset:4224\n"
"v_add_u32 v230, s84, v230\n"
"v_mfma_f32_16x16x32_bf16 v[92:95], v[180:183], a[108:111], v[92:95]\n"
"ds_read_b64_tr_b16 v[178:179], v201 offset:4480\n"
"v_lshl_add_u32 v214, v138, 4, s67\n"
"v_mfma_f32_16x16x32_bf16 v[88:91], v[180:183], a[104:107], v[88:91]\n"
"s_lshl_b32 s67, s57, 3\n"
"v_and_b32 v229, 1, v233\n"
"v_lshlrev_b32 v229, 4, v229\n"
"v_mfma_f32_16x16x32_bf16 v[84:87], v[180:183], a[100:103], v[84:87]\n"
"s_add_u32 s67, s67, s69\n"
"v_lshlrev_b32 v229, v229, v232\n"
"v_cndmask_b32 v229, 0, v229, s[10:11]\n"
"v_mfma_f32_16x16x32_bf16 v[80:83], v[180:183], a[96:99], v[80:83]\n"
"s_waitcnt lgkmcnt(3)\n"
"ds_read_b64_tr_b16 v[180:181], v201 offset:4256\n"
"v_and_b32 v232, 2, v233\n"
"v_mfma_f32_16x16x32_bf16 v[96:99], v[184:187], a[96:99], v[96:99]\n"
"s_mul_i32 s90, s67, 0x4800\n"
"ds_read_b64_tr_b16 v[182:183], v201 offset:4512\n"
"v_cmp_eq_u32 s[10:11], 0, v232\n"
"v_mfma_f32_16x16x32_bf16 v[100:103], v[184:187], a[100:103], v[100:103]\n"
"s_lshl_b32 s67, s67, 1\n"
"v_xor_b32 v232, 1, v235\n"
"v_cndmask_b32 v128, 0, v229, s[10:11]\n"
"v_mfma_f32_16x16x32_bf16 v[104:107], v[184:187], a[104:107], v[104:107]\n"
"s_add_u32 s67, s67, s58\n"
"v_cndmask_b32 v129, v229, 0, s[10:11]\n"
"v_and_b32 v229, 3, v233\n"
"v_mfma_f32_16x16x32_bf16 v[108:111], v[184:187], a[108:111], v[108:111]\n"
"s_add_u32 s67, s67, s68\n"
"v_cmp_gt_u32 s[10:11], 2, v235\n"
"v_fma_f32 v240, v240, v225, v225\n"
"v_mfma_f32_16x16x32_bf16 v[124:127], v[188:191], a[108:111], v[124:127]\n"
"s_lshl_b32 s67, s67, 10\n"
"v_rcp_f32 v240, v240\n"
"v_mfma_f32_16x16x32_bf16 v[120:123], v[188:191], a[104:107], v[120:123]\n"
"s_add_u32 s82, s67, 0x1100000\n"
"v_mul_f32 v36, v4, v36\n"
"v_mul_f32 v36, v36, v240\n"
"v_mfma_f32_16x16x32_bf16 v[116:119], v[188:191], a[100:103], v[116:119]\n"
"s_xor_b32 s67, s95, 1\n"
"v_mul_f32 v241, v221, v5\n"
"v_mul_f32 v37, v5, v37\n"
"v_mfma_f32_16x16x32_bf16 v[112:115], v[188:191], a[96:99], v[112:115]\n"
"s_lshl_b32 s69, s15, 1\n"
"v_exp_f32 v241, v241\n"
"v_mfma_f32_16x16x32_bf16 v[64:67], v[192:195], a[112:115], v[64:67]\n"
"s_add_u32 s69, s69, s67\n"
"v_fma_f32 v241, v241, v225, v225\n"
"v_mul_f32 v242, v221, v6\n"
"v_mfma_f32_16x16x32_bf16 v[68:71], v[192:195], a[116:119], v[68:71]\n"
"s_mul_i32 s67, s67, 0x440\n"
"v_rcp_f32 v241, v241\n"
"v_mfma_f32_16x16x32_bf16 v[72:75], v[192:195], a[120:123], v[72:75]\n"
"s_mul_i32 s68, s15, 0x840\n"
"v_exp_f32 v242, v242\n"
"v_mfma_f32_16x16x32_bf16 v[76:79], v[192:195], a[124:127], v[76:79]\n"
"s_waitcnt lgkmcnt(0)\n"
"v_mul_f32 v37, v37, v241\n"
"v_cvt_pk_bf16_f32 v252, v36, v37\n"
"v_mfma_f32_16x16x32_bf16 v[92:95], v[196:199], a[124:127], v[92:95]\n"
"s_add_u32 s67, s67, s68\n"
"v_fma_f32 v242, v242, v225, v225\n"
"v_mul_f32 v38, v6, v38\n"
"v_mfma_f32_16x16x32_bf16 v[88:91], v[196:199], a[120:123], v[88:91]\n"
"s_waitcnt vmcnt(9)\n"
"v_rcp_f32 v242, v242\n"
"v_mfma_f32_16x16x32_bf16 v[84:87], v[196:199], a[116:119], v[84:87]\n"
"s_barrier\n"
"v_mul_f32 v38, v38, v242\n"
"v_mul_f32 v243, v221, v7\n"
"v_mfma_f32_16x16x32_bf16 v[80:83], v[196:199], a[112:115], v[80:83]\n"
"s_add_u32 s67, s67, 0xe700\n"
"v_exp_f32 v243, v243\n"
"v_mfma_f32_16x16x32_bf16 v[96:99], v[176:179], a[112:115], v[96:99]\n"
"s_sub_u32 s68, s89, s69\n"
"s_sub_u32 s68, s68, 1\n"
"buffer_load_dwordx4 v207, s[28:31], s66 offen lds\n"
"v_mfma_f32_16x16x32_bf16 v[100:103], v[176:179], a[116:119], v[100:103]\n"
"s_add_u32 s66, s60, 0x3f0100\n"
"s_add_u32 m0, s59, 0x12900\n"
"ds_read_b64_tr_b16 v[184:185], v201 offset:8448\n"
"v_lshl_add_u32 v208, v138, 4, s67\n"
"v_mfma_f32_16x16x32_bf16 v[104:107], v[176:179], a[120:123], v[104:107]\n"
"s_lshl_b32 s67, s57, 3\n"
"ds_read_b64_tr_b16 v[186:187], v201 offset:8704\n"
"v_fma_f32 v243, v243, v225, v225\n"
"v_mfma_f32_16x16x32_bf16 v[108:111], v[176:179], a[124:127], v[108:111]\n"
"s_add_u32 s67, s67, s69\n"
"v_rcp_f32 v243, v243\n"
"v_mfma_f32_16x16x32_bf16 v[124:127], v[180:183], a[124:127], v[124:127]\n"
"s_mul_i32 s91, s67, 0x4800\n"
"ds_read_b64_tr_b16 v[188:189], v201 offset:8480\n"
"v_mul_f32 v39, v7, v39\n"
"v_mfma_f32_16x16x32_bf16 v[120:123], v[180:183], a[120:123], v[120:123]\n"
"s_lshl_b32 s67, s67, 1\n"
"ds_read_b64_tr_b16 v[190:191], v201 offset:8736\n"
"v_mul_f32 v39, v39, v243\n"
"v_mfma_f32_16x16x32_bf16 v[116:119], v[180:183], a[116:119], v[116:119]\n"
"s_add_u32 s67, s67, s58\n"
"ds_read_b64_tr_b16 v[192:193], v201 offset:12672\n"
"v_cvt_pk_bf16_f32 v253, v38, v39\n"
"v_mfma_f32_16x16x32_bf16 v[112:115], v[180:183], a[112:115], v[112:115]\n"
"s_waitcnt lgkmcnt(3)\n"
"ds_read_b64_tr_b16 v[194:195], v201 offset:12928\n"
"v_mul_f32 v244, v221, v20\n"
"v_mfma_f32_16x16x32_bf16 v[64:67], v[184:187], a[128:131], v[64:67]\n"
"s_and_b32 s68, s68, 7\n"
"v_exp_f32 v244, v244\n"
"v_mfma_f32_16x16x32_bf16 v[68:71], v[184:187], a[132:135], v[68:71]\n"
"s_lshl_b32 s68, s68, 8\n"
"ds_read_b64_tr_b16 v[196:197], v201 offset:12704\n"
"v_fma_f32 v244, v244, v225, v225\n"
"v_mfma_f32_16x16x32_bf16 v[72:75], v[184:187], a[136:139], v[72:75]\n"
"s_add_u32 s67, s67, s68\n"
"s_lshl_b32 s67, s67, 10\n"
"buffer_load_dwordx4 v207, s[24:27], s66 offen lds\n"
"v_mfma_f32_16x16x32_bf16 v[76:79], v[184:187], a[140:143], v[76:79]\n"
"s_waitcnt lgkmcnt(3)\n"
"s_add_u32 m0, s59, 0x13980\n"
"v_rcp_f32 v244, v244\n"
"v_mfma_f32_16x16x32_bf16 v[92:95], v[188:191], a[140:143], v[92:95]\n"
"s_add_u32 s83, s67, 0x1100000\n"
"ds_read_b64_tr_b16 v[198:199], v201 offset:12960\n"
"v_mul_f32 v52, v20, v52\n"
"v_mfma_f32_16x16x32_bf16 v[88:91], v[188:191], a[136:139], v[88:91]\n"
"s_lshl_b32 s67, s95, 1\n"
"ds_read_b64_tr_b16 v[176:177], v200 offset:42240\n"
"v_mul_f32 v52, v52, v244\n"
"v_mfma_f32_16x16x32_bf16 v[84:87], v[188:191], a[132:135], v[84:87]\n"
"ds_read_b64_tr_b16 v[178:179], v200 offset:42496\n"
"v_xor_b32 v229, s67, v229\n"
"v_mfma_f32_16x16x32_bf16 v[80:83], v[188:191], a[128:131], v[80:83]\n"
"s_waitcnt lgkmcnt(3)\n"
"ds_read_b64_tr_b16 v[180:181], v200 offset:42272\n"
"v_lshl_add_u32 v132, v229, 4, v132\n"
"v_mfma_f32_16x16x32_bf16 v[96:99], v[192:195], a[128:131], v[96:99]\n"
"s_lshr_b32 s67, s89, 1\n"
"s_cmp_eq_u32 s67, s15\n s_cselect_b32 s93, 0x7f00, 0\n s_cselect_b32 s68, 0, 2.0\n s_or_b32 s93, s93, 15\n"
"buffer_load_dwordx4 v207, s[28:31], s66 offen lds\n"
"v_mfma_f32_16x16x32_bf16 v[100:103], v[192:195], a[132:135], v[100:103]\n"
"s_add_u32 s66, s60, 0x438100\n"
"s_add_u32 m0, s59, 0x21000\n"
"ds_read_b64_tr_b16 v[182:183], v200 offset:42528\n"
"v_add_u32 v133, s68, v132\n"
"v_mfma_f32_16x16x32_bf16 v[104:107], v[192:195], a[136:139], v[104:107]\n"
"s_mul_i32 s67, s58, 0x280\n"
"ds_read_b64_tr_b16 v[184:185], v200 offset:46464\n"
"v_add_u32 v134, s68, v139\n"
"v_mfma_f32_16x16x32_bf16 v[108:111], v[192:195], a[140:143], v[108:111]\n"
"s_waitcnt lgkmcnt(4)\n"
"ds_read_b64_tr_b16 v[186:187], v200 offset:46720\n"
"v_xor_b32 v229, s95, v235\n"
"v_mfma_f32_16x16x32_bf16 v[124:127], v[196:199], a[140:143], v[124:127]\n"
"s_lshl_b32 s68, s58, 6\n"
"ds_read_b64_tr_b16 v[188:189], v200 offset:46496\n"
"v_mul_u32_u24 v229, 0x8400, v229\n"
"v_mfma_f32_16x16x32_bf16 v[120:123], v[196:199], a[136:139], v[120:123]\n"
"s_add_u32 s68, s68, 0x200\n"
"ds_read_b64_tr_b16 v[190:191], v200 offset:46752\n"
"v_lshl_add_u32 v229, v232, 7, v229\n"
"v_mfma_f32_16x16x32_bf16 v[116:119], v[196:199], a[132:135], v[116:119]\n"
"s_add_u32 s92, s90, s68\n"
"s_lshl_b32 s68, s89, 6\n"
"buffer_load_dwordx4 v207, s[24:27], s66 offen lds\n"
"v_mfma_f32_16x16x32_bf16 v[112:115], v[196:199], a[128:131], v[112:115]\n"
"s_waitcnt lgkmcnt(4)\n"
"s_add_u32 m0, s59, 0x22080\n"
"ds_read_b64_tr_b16 v[192:193], v201 offset:42240\n"
"v_lshl_add_u32 v229, v234, 1, v229\n"
"v_mfma_f32_16x16x32_bf16 v[64:67], v[176:179], a[144:147], v[64:67]\n"
"s_add_u32 s67, s67, s68\n"
"ds_read_b64_tr_b16 v[194:195], v201 offset:42496\n"
"v_mov_b32 v232, 0x40000000\n"
"v_mfma_f32_16x16x32_bf16 v[68:71], v[176:179], a[148:151], v[68:71]\n"
"s_add_u32 s90, s90, s67\n"
"ds_read_b64_tr_b16 v[196:197], v201 offset:42272\n"
"v_cndmask_b32 v135, v232, v229, s[10:11]\n"
"v_mfma_f32_16x16x32_bf16 v[72:75], v[176:179], a[152:155], v[72:75]\n"
"s_add_u32 s91, s91, s67\n"
"ds_read_b64_tr_b16 v[198:199], v201 offset:42528\n"
"v_mul_f32 v245, v221, v21\n"
"v_mfma_f32_16x16x32_bf16 v[76:79], v[176:179], a[156:159], v[76:79]\n"
"s_lshl_b32 s67, s57, 4\n"
"v_exp_f32 v245, v245\n"
"v_mfma_f32_16x16x32_bf16 v[92:95], v[180:183], a[156:159], v[92:95]\n"
"s_add_u32 s68, s67, s56\n"
"s_add_u32 s67, s67, s58\n"
"buffer_load_dwordx4 v207, s[28:31], s66 offen lds\n"
"v_mfma_f32_16x16x32_bf16 v[88:91], v[180:183], a[152:155], v[88:91]\n"
"s_add_u32 s66, s60, 0x480100\n"
"s_add_u32 m0, s59, 0\n"
"ds_read_b64_tr_b16 v[176:177], v201 offset:46464\n"
"v_fma_f32 v245, v245, v225, v225\n"
"v_mfma_f32_16x16x32_bf16 v[84:87], v[180:183], a[148:151], v[84:87]\n"
"s_lshl_b32 s67, s67, 2\n"
"v_rcp_f32 v245, v245\n"
"v_mfma_f32_16x16x32_bf16 v[80:83], v[180:183], a[144:147], v[80:83]\n"
"s_waitcnt lgkmcnt(2)\n"
"ds_read_b64_tr_b16 v[178:179], v201 offset:46720\n"
"v_mul_f32 v53, v21, v53\n"
"v_mfma_f32_16x16x32_bf16 v[96:99], v[184:187], a[144:147], v[96:99]\n"
"s_add_u32 s67, s67, s15\n"
"ds_read_b64_tr_b16 v[180:181], v201 offset:46496\n"
"v_mul_f32 v53, v53, v245\n"
"v_mfma_f32_16x16x32_bf16 v[100:103], v[184:187], a[148:151], v[100:103]\n"
"s_mul_i32 s88, s67, 0x1080\n"
"s_mul_i32 s19, s67, 0x400\n"
"buffer_load_dwordx4 v207, s[24:27], s66 offen lds\n"
"v_mfma_f32_16x16x32_bf16 v[104:107], v[184:187], a[152:155], v[104:107]\n"
"s_add_u32 m0, s59, 0x1080\n"
"ds_read_b64_tr_b16 v[182:183], v201 offset:46752\n"
"v_cvt_pk_bf16_f32 v254, v52, v53\n"
"v_mfma_f32_16x16x32_bf16 v[108:111], v[184:187], a[156:159], v[108:111]\n"
"s_lshl_b32 s67, s15, 1\n"
"v_mul_f32 v246, v221, v22\n"
"v_mul_f32 v54, v22, v54\n"
"v_mfma_f32_16x16x32_bf16 v[124:127], v[188:191], a[156:159], v[124:127]\n"
"s_lshl_b32 s87, s68, 10\n"
"v_exp_f32 v246, v246\n"
"v_mfma_f32_16x16x32_bf16 v[120:123], v[188:191], a[152:155], v[120:123]\n"
"s_xor_b32 s69, s68, 1\n"
"v_add_u32 v229, s67, v235\n"
"v_sub_u32 v138, s89, v229\n"
"v_mfma_f32_16x16x32_bf16 v[116:119], v[188:191], a[148:151], v[116:119]\n"
"s_lshl_b32 s68, s68, 2\n"
"s_add_u32 s68, s68, s15\n"
"buffer_load_dwordx4 v207, s[28:31], s66 offen lds\n"
"v_mfma_f32_16x16x32_bf16 v[112:115], v[188:191], a[144:147], v[112:115]\n"
"s_add_u32 s66, s60, 0x4c8100\n"
"v_add_u32 v138, 7, v138\n"
"v_and_b32 v138, 7, v138\n"
"v_mfma_f32_16x16x32_bf16 v[64:67], v[192:195], a[160:163], v[64:67]\n"
"s_add_u32 m0, s59, 0x2100\n"
"v_mul_u32_u24 v229, 0x8400, v229\n"
"v_lshl_add_u32 v229, v138, 7, v229\n"
"v_mfma_f32_16x16x32_bf16 v[68:71], v[192:195], a[164:167], v[68:71]\n"
"s_mul_i32 s16, s68, 0x1080\n"
"v_and_b32 v138, 31, v233\n"
"v_lshlrev_b32 v138, 7, v138\n"
"v_mfma_f32_16x16x32_bf16 v[72:75], v[192:195], a[168:171], v[72:75]\n"
"s_add_u32 s16, s16, 0x1300000\n"
"v_fma_f32 v246, v246, v225, v225\n"
"v_mul_f32 v247, v221, v23\n"
"v_mfma_f32_16x16x32_bf16 v[76:79], v[192:195], a[172:175], v[76:79]\n"
"s_waitcnt lgkmcnt(0)\n"
"s_mul_i32 s85, s69, 0x2000\n"
"buffer_load_dwordx4 v207, s[24:27], s66 offen lds\n"
"v_mfma_f32_16x16x32_bf16 v[92:95], v[196:199], a[172:175], v[92:95]\n"
"s_add_u32 m0, s59, 0x3180\n"
"v_rcp_f32 v246, v246\n"
"v_mfma_f32_16x16x32_bf16 v[88:91], v[196:199], a[168:171], v[88:91]\n"
"s_waitcnt vmcnt(7)\n"
"v_exp_f32 v247, v247\n"
"v_mfma_f32_16x16x32_bf16 v[84:87], v[196:199], a[164:167], v[84:87]\n"
"s_barrier\n"
"v_mul_f32 v54, v54, v246\n"
"v_fma_f32 v247, v247, v225, v225\n"
"v_mfma_f32_16x16x32_bf16 v[80:83], v[196:199], a[160:163], v[80:83]\n"
"s_lshl_b32 s69, s15, 10\n"
"v_rcp_f32 v247, v247\n"
"v_mfma_f32_16x16x32_bf16 v[96:99], v[176:179], a[160:163], v[96:99]\n"
"s_add_u32 s85, s85, s69\n"
"ds_read_b64_tr_b16 v[184:185], v201 offset:50688\n"
"v_mul_f32 v55, v23, v55\n"
"v_mfma_f32_16x16x32_bf16 v[100:103], v[176:179], a[164:167], v[100:103]\n"
"s_mul_i32 s69, s95, 0x200000\n"
"s_add_u32 s85, s85, 0x1900000\n"
"buffer_load_dwordx4 v207, s[28:31], s66 offen lds\n"
"v_mfma_f32_16x16x32_bf16 v[104:107], v[176:179], a[168:171], v[104:107]\n"
"s_add_u32 s66, s60, 0x180\n"
"s_add_u32 m0, s59, 0x4200\n"
"ds_read_b64_tr_b16 v[186:187], v201 offset:50944\n"
"v_mul_f32 v55, v55, v247\n"
"v_mfma_f32_16x16x32_bf16 v[108:111], v[176:179], a[172:175], v[108:111]\n"
"s_xor_b32 s61, s69, 0x200000\n"
"ds_read_b64_tr_b16 v[188:189], v201 offset:50720\n"
"v_cvt_pk_bf16_f32 v255, v54, v55\n"
"v_mfma_f32_16x16x32_bf16 v[124:127], v[180:183], a[172:175], v[124:127]\n"
"s_add_u32 s61, s61, s85\n"
"s_add_u32 s85, s85, s69\n"
"ds_write_b128 v205, v[252:255] offset:26432\n"
"v_mfma_f32_16x16x32_bf16 v[120:123], v[180:183], a[168:171], v[120:123]\n"
"s_mul_i32 s67, s53, 0xc00\n"
"ds_read_b64_tr_b16 v[190:191], v201 offset:50976\n"
"v_mul_f32 v240, v222, v8\n"
"v_mfma_f32_16x16x32_bf16 v[116:119], v[180:183], a[164:167], v[116:119]\n"
"s_add_u32 s67, s67, 0x24180\n"
"v_exp_f32 v240, v240\n"
"v_mfma_f32_16x16x32_bf16 v[112:115], v[180:183], a[160:163], v[112:115]\n"
"s_waitcnt lgkmcnt(3)\n"
"buffer_store_dwordx4 v[252:255], v139, s[44:47], s52 offen offset:0 sc0 sc1\n"
"v_fma_f32 v240, v240, v226, v226\n"
"v_mfma_f32_16x16x32_bf16 v[64:67], v[184:187], a[176:179], v[64:67]\n"
"s_add_u32 s52, s71, 0x400000\n"
"v_rcp_f32 v240, v240\n"
"v_mfma_f32_16x16x32_bf16 v[68:71], v[184:187], a[180:183], v[68:71]\n"
"s_mul_i32 s86, s68, 0x400\n"
"ds_read_b64_tr_b16 v[192:193], v201 offset:54912\n"
"v_mul_f32 v40, v8, v40\n"
"v_mfma_f32_16x16x32_bf16 v[72:75], v[184:187], a[184:187], v[72:75]\n"
"s_add_u32 s88, s88, 0x1300000\n"
"ds_read_b64_tr_b16 v[194:195], v201 offset:55168\n"
"v_mul_f32 v40, v40, v240\n"
"v_mfma_f32_16x16x32_bf16 v[76:79], v[184:187], a[188:191], v[76:79]\n"
"L_st6_%=:\n s_cbranch_execz L_st10_%=\n"
"s_waitcnt lgkmcnt(2)\n"
"s_mul_i32 s68, s58, 0x280\n"
"buffer_load_dwordx4 v207, s[24:27], s66 offen lds\n"
"v_mfma_f32_16x16x32_bf16 v[92:95], v[188:191], a[188:191], v[92:95]\n"
"s_add_u32 m0, s59, 0x5280\n"
"ds_read_b64_tr_b16 v[196:197], v201 offset:54944\n"
"v_mul_f32 v241, v222, v9\n"
"v_mfma_f32_16x16x32_bf16 v[88:91], v[188:191], a[184:187], v[88:91]\n"
"s_sub_u32 s68, s74, s68\n"
"v_exp_f32 v241, v241\n"
"v_mfma_f32_16x16x32_bf16 v[84:87], v[188:191], a[180:183], v[84:87]\n"
"s_mul_i32 s84, s68, 0x210\n"
"ds_read_b64_tr_b16 v[198:199], v201 offset:55200\n"
"v_fma_f32 v241, v241, v226, v226\n"
"v_mfma_f32_16x16x32_bf16 v[80:83], v[188:191], a[176:179], v[80:83]\n"
"s_waitcnt lgkmcnt(2)\n"
"v_rcp_f32 v241, v241\n"
"v_mfma_f32_16x16x32_bf16 v[96:99], v[192:195], a[176:179], v[96:99]\n"
"s_add_u32 s84, s84, s88\n"
"ds_read_b64_tr_b16 v[176:177], v202 offset:0\n"
"v_mul_f32 v41, v9, v41\n"
"v_mfma_f32_16x16x32_bf16 v[100:103], v[192:195], a[180:183], v[100:103]\n"
"s_mul_i32 s68, s58, 0x280\n"
"s_sub_u32 s68, s75, s68\n"
"buffer_load_dwordx4 v207, s[28:31], s66 offen lds\n"
"v_mfma_f32_16x16x32_bf16 v[104:107], v[192:195], a[184:187], v[104:107]\n"
"s_add_u32 s66, s60, 0x48180\n"
"s_add_u32 m0, s59, 0x6300\n"
"ds_read_b64_tr_b16 v[178:179], v202 offset:256\n"
"v_mul_f32 v41, v41, v241\n"
"v_mfma_f32_16x16x32_bf16 v[108:111], v[192:195], a[188:191], v[108:111]\n"
"s_waitcnt lgkmcnt(2)\n"
"ds_read_b64_tr_b16 v[180:181], v202 offset:32\n"
"v_cvt_pk_bf16_f32 v248, v40, v41\n"
"v_mfma_f32_16x16x32_bf16 v[124:127], v[196:199], a[188:191], v[124:127]\n"
"s_add_u32 s87, s87, 0x1100000\n"
"ds_read_b64_tr_b16 v[182:183], v202 offset:288\n"
"v_mul_f32 v242, v222, v10\n"
"v_mfma_f32_16x16x32_bf16 v[120:123], v[196:199], a[184:187], v[120:123]\n"
"v_exp_f32 v242, v242\n"
"v_mfma_f32_16x16x32_bf16 v[116:119], v[196:199], a[180:183], v[116:119]\n"
"ds_read_b64_tr_b16 v[184:185], v202 offset:4224\n"
"v_fma_f32 v242, v242, v226, v226\n"
"v_mfma_f32_16x16x32_bf16 v[112:115], v[196:199], a[176:179], v[112:115]\n"
"s_waitcnt lgkmcnt(3)\n"
"buffer_load_dwordx4 v207, s[24:27], s66 offen lds\n"
"v_mfma_f32_16x16x32_bf16 v[64:67], v[176:179], a[192:195], v[64:67]\n"
"s_add_u32 m0, s59, 0x7380\n"
"v_rcp_f32 v242, v242\n"
"v_mfma_f32_16x16x32_bf16 v[68:71], v[176:179], a[196:199], v[68:71]\n"
"ds_read_b64_tr_b16 v[186:187], v202 offset:4480\n"
"v_mul_f32 v42, v10, v42\n"
"v_mfma_f32_16x16x32_bf16 v[72:75], v[176:179], a[200:203], v[72:75]\n"
"ds_read_b64_tr_b16 v[188:189], v202 offset:4256\n"
"v_mul_f32 v42, v42, v242\n"
"v_mfma_f32_16x16x32_bf16 v[76:79], v[176:179], a[204:207], v[76:79]\n"
"s_waitcnt lgkmcnt(3)\n"
"ds_read_b64_tr_b16 v[190:191], v202 offset:4512\n"
"v_mul_f32 v243, v222, v11\n"
"v_mfma_f32_16x16x32_bf16 v[92:95], v[180:183], a[204:207], v[92:95]\n"
"buffer_load_dwordx4 v207, s[28:31], s66 offen lds\n"
"v_mfma_f32_16x16x32_bf16 v[88:91], v[180:183], a[200:203], v[88:91]\n"
"s_add_u32 s66, s60, 0x90180\n"
"s_add_u32 m0, s59, 0x8400\n"
"v_exp_f32 v243, v243\n"
"v_mfma_f32_16x16x32_bf16 v[84:87], v[180:183], a[196:199], v[84:87]\n"
"ds_read_b64_tr_b16 v[192:193], v202 offset:8448\n"
"v_fma_f32 v243, v243, v226, v226\n"
"v_mfma_f32_16x16x32_bf16 v[80:83], v[180:183], a[192:195], v[80:83]\n"
"s_waitcnt lgkmcnt(3)\n"
"v_rcp_f32 v243, v243\n"
"v_mfma_f32_16x16x32_bf16 v[96:99], v[184:187], a[192:195], v[96:99]\n"
"ds_read_b64_tr_b16 v[194:195], v202 offset:8704\n"
"v_mul_f32 v43, v11, v43\n"
"v_mfma_f32_16x16x32_bf16 v[100:103], v[184:187], a[196:199], v[100:103]\n"
"buffer_load_dwordx4 v207, s[24:27], s66 offen lds\n"
"v_mfma_f32_16x16x32_bf16 v[104:107], v[184:187], a[200:203], v[104:107]\n"
"s_add_u32 m0, s59, 0x9480\n"
"ds_read_b64_tr_b16 v[196:197], v202 offset:8480\n"
"v_mul_f32 v43, v43, v243\n"
"v_mfma_f32_16x16x32_bf16 v[108:111], v[184:187], a[204:207], v[108:111]\n"
"s_waitcnt lgkmcnt(3)\n"
"ds_read_b64_tr_b16 v[198:199], v202 offset:8736\n"
"v_cvt_pk_bf16_f32 v249, v42, v43\n"
"v_mfma_f32_16x16x32_bf16 v[124:127], v[188:191], a[204:207], v[124:127]\n"
"ds_read_b64_tr_b16 v[176:177], v202 offset:12672\n"
"v_mul_f32 v244, v222, v24\n"
"v_mfma_f32_16x16x32_bf16 v[120:123], v[188:191], a[200:203], v[120:123]\n"
"v_exp_f32 v244, v244\n"
"v_mfma_f32_16x16x32_bf16 v[116:119], v[188:191], a[196:199], v[116:119]\n"
"ds_read_b64_tr_b16 v[178:179], v202 offset:12928\n"
"v_fma_f32 v244, v244, v226, v226\n"
"v_mfma_f32_16x16x32_bf16 v[112:115], v[188:191], a[192:195], v[112:115]\n"
"s_waitcnt lgkmcnt(4)\n"
"buffer_load_dwordx4 v207, s[28:31], s66 offen lds\n"
"v_mfma_f32_16x16x32_bf16 v[64:67], v[192:195], a[208:211], v[64:67]\n"
"s_add_u32 s66, s60, 0xd8180\n"
"s_add_u32 m0, s59, 0xc600\n"
"v_rcp_f32 v244, v244\n"
"v_mfma_f32_16x16x32_bf16 v[68:71], v[192:195], a[212:215], v[68:71]\n"
"ds_read_b64_tr_b16 v[180:181], v202 offset:12704\n"
"v_mul_f32 v56, v24, v56\n"
"v_mfma_f32_16x16x32_bf16 v[72:75], v[192:195], a[216:219], v[72:75]\n"
"ds_read_b64_tr_b16 v[182:183], v202 offset:12960\n"
"v_mul_f32 v56, v56, v244\n"
"v_mfma_f32_16x16x32_bf16 v[76:79], v[192:195], a[220:223], v[76:79]\n"
"s_waitcnt lgkmcnt(3)\n"
"v_mul_f32 v245, v222, v25\n"
"v_mul_f32 v57, v25, v57\n"
"v_mfma_f32_16x16x32_bf16 v[92:95], v[196:199], a[220:223], v[92:95]\n"
"v_exp_f32 v245, v245\n"
"v_mfma_f32_16x16x32_bf16 v[88:91], v[196:199], a[216:219], v[88:91]\n"
"s_waitcnt vmcnt(9)\n"
"buffer_load_dwordx4 v207, s[24:27], s66 offen lds\n"
"v_mfma_f32_16x16x32_bf16 v[84:87], v[196:199], a[212:215], v[84:87]\n"
"s_barrier\n"
"s_add_u32 m0, s59, 0xd680\n"
"v_fma_f32 v245, v245, v226, v226\n"
"v_mul_f32 v246, v222, v26\n"
"v_mfma_f32_16x16x32_bf16 v[80:83], v[196:199], a[208:211], v[80:83]\n"
"s_waitcnt lgkmcnt(1)\n"
"v_rcp_f32 v245, v245\n"
"v_mfma_f32_16x16x32_bf16 v[96:99], v[176:179], a[208:211], v[96:99]\n"
"v_exp_f32 v246, v246\n"
"v_mfma_f32_16x16x32_bf16 v[100:103], v[176:179], a[212:215], v[100:103]\n"
"ds_read_b64_tr_b16 v[184:185], v201 offset:16896\n"
"v_mul_f32 v57, v57, v245\n"
"v_mfma_f32_16x16x32_bf16 v[104:107], v[176:179], a[216:219], v[104:107]\n"
"ds_read_b64_tr_b16 v[186:187], v201 offset:17152\n"
"v_cvt_pk_bf16_f32 v250, v56, v57\n"
"v_mfma_f32_16x16x32_bf16 v[108:111], v[176:179], a[220:223], v[108:111]\n"
"s_waitcnt lgkmcnt(2)\n"
"buffer_load_dwordx4 v207, s[28:31], s66 offen lds\n"
"v_mfma_f32_16x16x32_bf16 v[124:127], v[180:183], a[220:223], v[124:127]\n"
"s_add_u32 s66, s60, 0x120180\n"
"s_add_u32 m0, s59, 0xa500\n"
"ds_read_b64_tr_b16 v[188:189], v201 offset:16928\n"
"v_fma_f32 v246, v246, v226, v226\n"
"v_mfma_f32_16x16x32_bf16 v[120:123], v[180:183], a[216:219], v[120:123]\n"
"v_rcp_f32 v246, v246\n"
"v_mfma_f32_16x16x32_bf16 v[116:119], v[180:183], a[212:215], v[116:119]\n"
"ds_read_b64_tr_b16 v[190:191], v201 offset:17184\n"
"v_mul_f32 v58, v26, v58\n"
"v_mfma_f32_16x16x32_bf16 v[112:115], v[180:183], a[208:211], v[112:115]\n"
"s_waitcnt lgkmcnt(2)\n"
"ds_read_b64_tr_b16 v[192:193], v201 offset:21120\n"
"v_mul_f32 v58, v58, v246\n"
"v_mfma_f32_16x16x32_bf16 v[64:67], v[184:187], a[224:227], v[64:67]\n"
"buffer_load_dwordx4 v207, s[24:27], s66 offen lds\n"
"v_mfma_f32_16x16x32_bf16 v[68:71], v[184:187], a[228:231], v[68:71]\n"
"s_add_u32 m0, s59, 0xb580\n"
"ds_read_b64_tr_b16 v[194:195], v201 offset:21376\n"
"v_mul_f32 v247, v222, v27\n"
"v_mfma_f32_16x16x32_bf16 v[72:75], v[184:187], a[232:235], v[72:75]\n"
"v_exp_f32 v247, v247\n"
"v_mfma_f32_16x16x32_bf16 v[76:79], v[184:187], a[236:239], v[76:79]\n"
"s_waitcnt lgkmcnt(2)\n"
"ds_read_b64_tr_b16 v[196:197], v201 offset:21152\n"
"v_fma_f32 v247, v247, v226, v226\n"
"v_mfma_f32_16x16x32_bf16 v[92:95], v[188:191], a[236:239], v[92:95]\n"
"v_rcp_f32 v247, v247\n"
"v_mfma_f32_16x16x32_bf16 v[88:91], v[188:191], a[232:235], v[88:91]\n"
"buffer_load_dwordx4 v207, s[28:31], s66 offen lds\n"
"v_mfma_f32_16x16x32_bf16 v[84:87], v[188:191], a[228:231], v[84:87]\n"
"s_add_u32 s66, s60, 0x168180\n"
"s_add_u32 m0, s59, 0xe700\n"
"ds_read_b64_tr_b16 v[198:199], v201 offset:21408\n"
"v_mul_f32 v59, v27, v59\n"
"v_mfma_f32_16x16x32_bf16 v[80:83], v[188:191], a[224:227], v[80:83]\n"
"s_waitcnt lgkmcnt(2)\n"
"ds_read_b64_tr_b16 v[176:177], v202 offset:16896\n"
"v_mul_f32 v59, v59, v247\n"
"v_mfma_f32_16x16x32_bf16 v[96:99], v[192:195], a[224:227], v[96:99]\n"
"ds_read_b64_tr_b16 v[178:179], v202 offset:17152\n"
"v_mul_f32 v240, v223, v12\n"
"v_mfma_f32_16x16x32_bf16 v[100:103], v[192:195], a[228:231], v[100:103]\n"
"v_exp_f32 v240, v240\n"
"v_mfma_f32_16x16x32_bf16 v[104:107], v[192:195], a[232:235], v[104:107]\n"
"buffer_load_dwordx4 v207, s[24:27], s66 offen lds\n"
"v_mfma_f32_16x16x32_bf16 v[108:111], v[192:195], a[236:239], v[108:111]\n"
"s_waitcnt lgkmcnt(2)\n"
"s_add_u32 m0, s59, 0xf780\n"
"ds_read_b64_tr_b16 v[180:181], v202 offset:16928\n"
"v_mul_f32 v241, v223, v13\n"
"v_mfma_f32_16x16x32_bf16 v[124:127], v[196:199], a[236:239], v[124:127]\n"
"v_exp_f32 v241, v241\n"
"v_mfma_f32_16x16x32_bf16 v[120:123], v[196:199], a[232:235], v[120:123]\n"
"ds_read_b64_tr_b16 v[182:183], v202 offset:17184\n"
"v_mul_f32 v242, v223, v14\n"
"v_mfma_f32_16x16x32_bf16 v[116:119], v[196:199], a[228:231], v[116:119]\n"
"v_exp_f32 v242, v242\n"
"v_mfma_f32_16x16x32_bf16 v[112:115], v[196:199], a[224:227], v[112:115]\n"
"s_waitcnt lgkmcnt(2)\n"
"buffer_load_dwordx4 v207, s[28:31], s66 offen lds\n"
"v_mfma_f32_16x16x32_bf16 v[64:67], v[176:179], a[240:243], v[64:67]\n"
"s_add_u32 s66, s60, 0x1b0180\n"
"s_add_u32 m0, s59, 0x18c00\n"
"ds_read_b64_tr_b16 v[184:185], v202 offset:21120\n"
"v_mul_f32 v243, v223, v15\n"
"v_mfma_f32_16x16x32_bf16 v[68:71], v[176:179], a[244:247], v[68:71]\n"
"v_exp_f32 v243, v243\n"
"v_mfma_f32_16x16x32_bf16 v[72:75], v[176:179], a[248:251], v[72:75]\n"
"ds_read_b64_tr_b16 v[186:187], v202 offset:21376\n"
"v_mul_f32 v244, v223, v28\n"
"v_mfma_f32_16x16x32_bf16 v[76:79], v[176:179], a[252:255], v[76:79]\n"
"s_waitcnt lgkmcnt(2)\n"
"buffer_load_dwordx4 v207, s[24:27], s66 offen lds\n"
"v_mfma_f32_16x16x32_bf16 v[92:95], v[180:183], a[252:255], v[92:95]\n"
"s_add_u32 m0, s59, 0x19c80\n"
"v_exp_f32 v244, v244\n"
"v_mfma_f32_16x16x32_bf16 v[88:91], v[180:183], a[248:251], v[88:91]\n"
"ds_read_b64_tr_b16 v[188:189], v202 offset:21152\n"
"v_mul_f32 v245, v223, v29\n"
"v_mfma_f32_16x16x32_bf16 v[84:87], v[180:183], a[244:247], v[84:87]\n"
"v_exp_f32 v245, v245\n"
"v_mfma_f32_16x16x32_bf16 v[80:83], v[180:183], a[240:243], v[80:83]\n"
"s_waitcnt lgkmcnt(1)\n"
"ds_read_b64_tr_b16 v[190:191], v202 offset:21408\n"
"v_mul_f32 v246, v223, v30\n"
"v_mfma_f32_16x16x32_bf16 v[96:99], v[184:187], a[240:243], v[96:99]\n"
"buffer_load_dwordx4 v207, s[28:31], s66 offen lds\n"
"v_mfma_f32_16x16x32_bf16 v[100:103], v[184:187], a[244:247], v[100:103]\n"
"s_add_u32 s66, s60, 0x1f8180\n"
"s_add_u32 m0, s59, 0x1ad00\n"
"v_exp_f32 v246, v246\n"
"v_mfma_f32_16x16x32_bf16 v[104:107], v[184:187], a[248:251], v[104:107]\n"
"ds_read_b64_tr_b16 v[192:193], v200 offset:0\n"
"v_mul_f32 v247, v223, v31\n"
"v_mfma_f32_16x16x32_bf16 v[108:111], v[184:187], a[252:255], v[108:111]\n"
"s_waitcnt lgkmcnt(1)\n"
"ds_read_b64_tr_b16 v[194:195], v200 offset:256\n"
"v_fma_f32 v240, v240, v227, v227\n"
"v_mfma_f32_16x16x32_bf16 v[124:127], v[188:191], a[252:255], v[124:127]\n"
"buffer_load_dwordx4 v207, s[24:27], s66 offen lds\n"
"v_mfma_f32_16x16x32_bf16 v[120:123], v[188:191], a[248:251], v[120:123]\n"
"s_add_u32 m0, s59, 0x1bd80\n"
"v_exp_f32 v247, v247\n"
"v_mfma_f32_16x16x32_bf16 v[116:119], v[188:191], a[244:247], v[116:119]\n"
"ds_read_b64_tr_b16 v[196:197], v200 offset:32\n"
"v_fma_f32 v241, v241, v227, v227\n"
"v_mfma_f32_16x16x32_bf16 v[112:115], v[188:191], a[240:243], v[112:115]\n"
"s_waitcnt lgkmcnt(1)\n"
"ds_read_b64_tr_b16 v[198:199], v200 offset:288\n"
"v_fma_f32 v242, v242, v227, v227\n"
"v_mfma_f32_16x16x32_bf16 v[64:67], v[192:195], v[144:147], v[64:67]\n"
"buffer_load_dwordx4 v207, s[28:31], s66 offen lds\n"
"v_mfma_f32_16x16x32_bf16 v[68:71], v[192:195], v[148:151], v[68:71]\n"
"s_add_u32 s66, s60, 0x240180\n"
"s_add_u32 m0, s59, 0x1ce00\n"
"v_rcp_f32 v240, v240\n"
"v_mfma_f32_16x16x32_bf16 v[72:75], v[192:195], v[152:155], v[72:75]\n"
"ds_read_b64_tr_b16 v[176:177], v200 offset:4224\n"
"v_fma_f32 v243, v243, v227, v227\n"
"v_mfma_f32_16x16x32_bf16 v[76:79], v[192:195], v[156:159], v[76:79]\n"
"s_waitcnt lgkmcnt(1)\n"
"ds_read_b64_tr_b16 v[178:179], v200 offset:4480\n"
"v_fma_f32 v244, v244, v227, v227\n"
"v_mfma_f32_16x16x32_bf16 v[92:95], v[196:199], v[156:159], v[92:95]\n"
"buffer_load_dwordx4 v207, s[24:27], s66 offen lds\n"
"v_mfma_f32_16x16x32_bf16 v[88:91], v[196:199], v[152:155], v[88:91]\n"
"s_waitcnt vmcnt(13)\n"
"s_add_u32 m0, s59, 0x1de80\n"
"v_rcp_f32 v241, v241\n"
"v_mfma_f32_16x16x32_bf16 v[84:87], v[196:199], v[148:151], v[84:87]\n"
"s_barrier\n"
"ds_read_b64_tr_b16 v[180:181], v200 offset:4256\n"
"v_fma_f32 v245, v245, v227, v227\n"
"v_mfma_f32_16x16x32_bf16 v[80:83], v[196:199], v[144:147], v[80:83]\n"
"s_waitcnt lgkmcnt(1)\n"
"ds_read_b64_tr_b16 v[182:183], v200 offset:4512\n"
"v_fma_f32 v246, v246, v227, v227\n"
"v_mfma_f32_16x16x32_bf16 v[96:99], v[176:179], v[144:147], v[96:99]\n"
"buffer_load_dwordx4 v207, s[28:31], s66 offen lds\n"
"v_mfma_f32_16x16x32_bf16 v[100:103], v[176:179], v[148:151], v[100:103]\n"
"s_add_u32 s66, s60, 0x288180\n"
"s_add_u32 m0, s59, 0x1ef00\n"
"v_rcp_f32 v242, v242\n"
"v_mfma_f32_16x16x32_bf16 v[104:107], v[176:179], v[152:155], v[104:107]\n"
"ds_read_b64_tr_b16 v[184:185], v200 offset:8448\n"
"v_fma_f32 v247, v247, v227, v227\n"
"v_mfma_f32_16x16x32_bf16 v[108:111], v[176:179], v[156:159], v[108:111]\n"
"s_waitcnt lgkmcnt(1)\n"
"ds_read_b64_tr_b16 v[186:187], v200 offset:8704\n"
"v_mul_f32 v44, v12, v44\n"
"v_mfma_f32_16x16x32_bf16 v[124:127], v[180:183], v[156:159], v[124:127]\n"
"buffer_load_dwordx4 v207, s[24:27], s66 offen lds\n"
"v_mul_f32 v45, v13, v45\n"
"v_mfma_f32_16x16x32_bf16 v[120:123], v[180:183], v[152:155], v[120:123]\n"
"s_add_u32 m0, s59, 0x1ff80\n"
"v_rcp_f32 v243, v243\n"
"v_mfma_f32_16x16x32_bf16 v[116:119], v[180:183], v[148:151], v[116:119]\n"
"ds_read_b64_tr_b16 v[188:189], v200 offset:8480\n"
"v_mul_f32 v46, v14, v46\n"
"v_mfma_f32_16x16x32_bf16 v[112:115], v[180:183], v[144:147], v[112:115]\n"
"s_waitcnt lgkmcnt(1)\n"
"ds_read_b64_tr_b16 v[190:191], v200 offset:8736\n"
"v_mul_f32 v47, v15, v47\n"
"v_mfma_f32_16x16x32_bf16 v[64:67], v[184:187], v[160:163], v[64:67]\n"
"buffer_load_dwordx4 v207, s[28:31], s66 offen lds\n"
"v_rcp_f32 v244, v244\n"
"v_mfma_f32_16x16x32_bf16 v[68:71], v[184:187], v[164:167], v[68:71]\n"
"s_add_u32 s66, s60, 0x2d0180\n"
"s_add_u32 m0, s59, 0x21000\n"
"v_rcp_f32 v245, v245\n"
"v_mfma_f32_16x16x32_bf16 v[72:75], v[184:187], v[168:171], v[72:75]\n"
"ds_read_b64_tr_b16 v[192:193], v200 offset:12672\n"
"v_mul_f32 v60, v28, v60\n"
"v_mfma_f32_16x16x32_bf16 v[76:79], v[184:187], v[172:175], v[76:79]\n"
"s_waitcnt lgkmcnt(1)\n"
"ds_read_b64_tr_b16 v[194:195], v200 offset:12928\n"
"v_mul_f32 v61, v29, v61\n"
"v_mfma_f32_16x16x32_bf16 v[92:95], v[188:191], v[172:175], v[92:95]\n"
"v_rcp_f32 v246, v246\n"
"v_mfma_f32_16x16x32_bf16 v[88:91], v[188:191], v[168:171], v[88:91]\n"
"v_rcp_f32 v247, v247\n"
"v_mfma_f32_16x16x32_bf16 v[84:87], v[188:191], v[164:167], v[84:87]\n"
"ds_read_b64_tr_b16 v[196:197], v200 offset:12704\n"
"v_mul_f32 v62, v30, v62\n"
"v_mfma_f32_16x16x32_bf16 v[80:83], v[188:191], v[160:163], v[80:83]\n"
"s_waitcnt lgkmcnt(1)\n"
"ds_read_b64_tr_b16 v[198:199], v200 offset:12960\n"
"v_mul_f32 v63, v31, v63\n"
"v_mfma_f32_16x16x32_bf16 v[96:99], v[192:195], v[160:163], v[96:99]\n"
"ds_read_b64_tr_b16 v[176:177], v200 offset:16896\n"
"v_mul_f32 v44, v44, v240\n"
"v_mfma_f32_16x16x32_bf16 v[100:103], v[192:195], v[164:167], v[100:103]\n"
"ds_read_b64_tr_b16 v[178:179], v200 offset:17152\n"
"v_mul_f32 v45, v45, v241\n"
"v_mfma_f32_16x16x32_bf16 v[104:107], v[192:195], v[168:171], v[104:107]\n"
"ds_read_b64_tr_b16 v[180:181], v200 offset:16928\n"
"v_mul_f32 v46, v46, v242\n"
"v_mfma_f32_16x16x32_bf16 v[108:111], v[192:195], v[172:175], v[108:111]\n"
"s_waitcnt lgkmcnt(3)\n"
"ds_read_b64_tr_b16 v[182:183], v200 offset:17184\n"
"v_mul_f32 v47, v47, v243\n"
"v_mfma_f32_16x16x32_bf16 v[124:127], v[196:199], v[172:175], v[124:127]\n"
"buffer_load_dwordx4 v207, s[24:27], s66 offen lds\n"
"v_mfma_f32_16x16x32_bf16 v[120:123], v[196:199], v[168:171], v[120:123]\n"
"s_add_u32 m0, s59, 0x22080\n"
"ds_read_b64_tr_b16 v[184:185], v200 offset:21120\n"
"v_mul_f32 v60, v60, v244\n"
"v_mfma_f32_16x16x32_bf16 v[116:119], v[196:199], v[164:167], v[116:119]\n"
"ds_read_b64_tr_b16 v[186:187], v200 offset:21376\n"
"v_mul_f32 v61, v61, v245\n"
"v_mfma_f32_16x16x32_bf16 v[112:115], v[196:199], v[160:163], v[112:115]\n"
"s_waitcnt lgkmcnt(4)\n"
"ds_read_b64_tr_b16 v[188:189], v200 offset:21152\n"
"v_mul_f32 v62, v62, v246\n"
"v_mul_f32 v63, v63, v247\n"
"v_mfma_f32_16x16x32_bf16 v[0:3], v[176:179], a[0:3], 0\n"
"ds_read_b64_tr_b16 v[190:191], v200 offset:21408\n"
"v_cvt_pk_bf16_f32 v251, v58, v59\n"
"v_cvt_pk_bf16_f32 v252, v44, v45\n"
"v_mfma_f32_16x16x32_bf16 v[4:7], v[176:179], a[4:7], 0\n"
"buffer_store_dwordx4 v[248:251], v139, s[44:47], s52 offen offset:0 sc0 sc1\n"
"v_cvt_pk_bf16_f32 v253, v46, v47\n"
"v_mfma_f32_16x16x32_bf16 v[8:11], v[176:179], a[8:11], 0\n"
"s_add_u32 s52, s71, 0x600000\n"
"ds_read_b64_tr_b16 v[192:193], v200 offset:25344\n"
"v_cvt_pk_bf16_f32 v254, v60, v61\n"
"v_mfma_f32_16x16x32_bf16 v[12:15], v[176:179], a[12:15], 0\n"
"s_waitcnt lgkmcnt(4)\n"
"ds_read_b64_tr_b16 v[194:195], v200 offset:25600\n"
"v_cvt_pk_bf16_f32 v255, v62, v63\n"
"v_mfma_f32_16x16x32_bf16 v[28:31], v[180:183], a[12:15], 0\n"
"ds_read_b64_tr_b16 v[196:197], v200 offset:25376\n"
"v_add_u32 v206, 0xe700, v206\n"
"v_mfma_f32_16x16x32_bf16 v[24:27], v[180:183], a[8:11], 0\n"
"buffer_load_dwordx4 v207, s[28:31], s66 offen lds\n"
"v_mfma_f32_16x16x32_bf16 v[20:23], v[180:183], a[4:7], 0\n"
"s_add_u32 s66, s60, 0x318180\n"
"s_add_u32 m0, s59, 0\n"
"ds_write_b128 v206, v[248:251] offset:25344\n"
"v_mfma_f32_16x16x32_bf16 v[16:19], v[180:183], a[0:3], 0\n"
"s_waitcnt lgkmcnt(4)\n"
"ds_write_b128 v206, v[252:255] offset:26432\n"
"v_mfma_f32_16x16x32_bf16 v[32:35], v[184:187], a[0:3], 0\n"
"buffer_store_dwordx4 v[252:255], v139, s[44:47], s52 offen offset:0 sc0 sc1\n"
"v_lshl_add_u32 v229, v234, 1, v229\n"
"v_mfma_f32_16x16x32_bf16 v[36:39], v[184:187], a[4:7], 0\n"
"s_add_u32 s52, s70, 0x800000\n"
"ds_read_b64_tr_b16 v[198:199], v200 offset:25632\n"
"v_cndmask_b32 v137, v232, v229, s[10:11]\n"
"v_mfma_f32_16x16x32_bf16 v[40:43], v[184:187], a[8:11], 0\n"
"ds_read_b64_tr_b16 v[176:177], v200 offset:29568\n"
"v_add_u32 v232, s67, v139\n"
"v_mfma_f32_16x16x32_bf16 v[44:47], v[184:187], a[12:15], 0\n"
"ds_read_b64_tr_b16 v[178:179], v200 offset:29824\n"
"v_lshlrev_b32 v229, 4, v234\n"
"v_mfma_f32_16x16x32_bf16 v[60:63], v[188:191], a[12:15], 0\n"
"ds_read_b64_tr_b16 v[180:181], v200 offset:29600\n"
"v_lshl_add_u32 v229, v235, 3, v229\n"
"v_mfma_f32_16x16x32_bf16 v[56:59], v[188:191], a[8:11], 0\n"
"ds_read_b64_tr_b16 v[182:183], v200 offset:29856\n"
"v_add_u32 v229, s67, v229\n"
"v_mfma_f32_16x16x32_bf16 v[52:55], v[188:191], a[4:7], 0\n"
"s_lshl_b32 s67, s58, 1\n"
"v_mul_f32 v240, v220, v64\n"
"v_mul_f32 v96, v64, v96\n"
"v_mfma_f32_16x16x32_bf16 v[48:51], v[188:191], a[0:3], 0\n"
"s_waitcnt lgkmcnt(4)\n"
"v_exp_f32 v240, v240\n"
"v_mfma_f32_16x16x32_bf16 v[0:3], v[192:195], a[16:19], v[0:3]\n"
"s_sub_u32 s67, s67, 1\n"
"v_fma_f32 v240, v240, v224, v224\n"
"v_mul_f32 v241, v220, v65\n"
"v_mfma_f32_16x16x32_bf16 v[4:7], v[192:195], a[20:23], v[4:7]\n"
"s_mul_i32 s67, s67, 0x2000\n"
"v_rcp_f32 v240, v240\n"
"v_mfma_f32_16x16x32_bf16 v[8:11], v[192:195], a[24:27], v[8:11]\n"
"s_add_u32 s12, s85, s67\n"
"v_exp_f32 v241, v241\n"
"v_mfma_f32_16x16x32_bf16 v[12:15], v[192:195], a[28:31], v[12:15]\n"
"s_add_u32 s94, s12, 0\n"
"v_mul_f32 v96, v96, v240\n"
"v_fma_f32 v241, v241, v224, v224\n"
"v_mfma_f32_16x16x32_bf16 v[28:31], v[196:199], a[28:31], v[28:31]\n"
"s_add_u32 s13, s61, s67\n"
"v_rcp_f32 v241, v241\n"
"v_mfma_f32_16x16x32_bf16 v[24:27], v[196:199], a[24:27], v[24:27]\n"
"s_waitcnt vmcnt(14)\n"
"v_mul_f32 v97, v65, v97\n"
"v_mul_f32 v97, v97, v241\n"
"v_mfma_f32_16x16x32_bf16 v[20:23], v[196:199], a[20:23], v[20:23]\n"
"s_barrier\n"
"v_cvt_pk_bf16_f32 v248, v96, v97\n"
"v_mul_f32 v242, v220, v66\n"
"v_mfma_f32_16x16x32_bf16 v[16:19], v[196:199], a[16:19], v[16:19]\n"
"s_waitcnt lgkmcnt(0)\n"
"s_lshl_b32 s67, s55, 6\n"
"buffer_load_dwordx4 v207, s[24:27], s66 offen lds\n"
"v_mfma_f32_16x16x32_bf16 v[32:35], v[176:179], a[16:19], v[32:35]\n"
"s_add_u32 m0, s59, 0x1080\n"
"v_exp_f32 v242, v242\n"
"v_mfma_f32_16x16x32_bf16 v[36:39], v[176:179], a[20:23], v[36:39]\n"
"ds_read_b64_tr_b16 v[184:185], v200 offset:33792\n"
"v_fma_f32 v242, v242, v224, v224\n"
"v_mfma_f32_16x16x32_bf16 v[40:43], v[176:179], a[24:27], v[40:43]\n"
"v_rcp_f32 v242, v242\n"
"v_mfma_f32_16x16x32_bf16 v[44:47], v[176:179], a[28:31], v[44:47]\n"
"ds_read_b64_tr_b16 v[186:187], v200 offset:34048\n"
"v_mul_f32 v98, v66, v98\n"
"v_mfma_f32_16x16x32_bf16 v[60:63], v[180:183], a[28:31], v[60:63]\n"
"buffer_load_dwordx4 v207, s[28:31], s66 offen lds\n"
"v_mfma_f32_16x16x32_bf16 v[56:59], v[180:183], a[24:27], v[56:59]\n"
"s_add_u32 s66, s60, 0x360180\n"
"s_add_u32 m0, s59, 0x2100\n"
"ds_read_b64_tr_b16 v[188:189], v200 offset:33824\n"
"v_mul_f32 v98, v98, v242\n"
"v_mfma_f32_16x16x32_bf16 v[52:55], v[180:183], a[20:23], v[52:55]\n"
"ds_read_b64_tr_b16 v[190:191], v200 offset:34080\n"
"v_mul_f32 v243, v220, v67\n"
"v_mfma_f32_16x16x32_bf16 v[48:51], v[180:183], a[16:19], v[48:51]\n"
"s_waitcnt lgkmcnt(2)\n"
"v_exp_f32 v243, v243\n"
"v_mfma_f32_16x16x32_bf16 v[0:3], v[184:187], a[32:35], v[0:3]\n"
"s_mov_b32 exec_lo, 0x1fffff\n s_mov_b32 exec_hi, 0x0\n buffer_load_dword v215, v230, s[36:39], s73 offen offset:0\n s_mov_b64 exec, -1\n"
"v_mfma_f32_16x16x32_bf16 v[4:7], v[184:187], a[36:39], v[4:7]\n"
"ds_read_b64_tr_b16 v[192:193], v200 offset:38016\n"
"v_fma_f32 v243, v243, v224, v224\n"
"v_mfma_f32_16x16x32_bf16 v[8:11], v[184:187], a[40:43], v[8:11]\n"
"v_rcp_f32 v243, v243\n"
"v_mfma_f32_16x16x32_bf16 v[12:15], v[184:187], a[44:47], v[12:15]\n"
"s_waitcnt lgkmcnt(1)\n"
"ds_read_b64_tr_b16 v[194:195], v200 offset:38272\n"
"v_mul_f32 v99, v67, v99\n"
"v_mfma_f32_16x16x32_bf16 v[28:31], v[188:191], a[44:47], v[28:31]\n"
"ds_read_b64_tr_b16 v[196:197], v200 offset:38048\n"
"v_mul_f32 v99, v99, v243\n"
"v_mfma_f32_16x16x32_bf16 v[24:27], v[188:191], a[40:43], v[24:27]\n"
"ds_read_b64_tr_b16 v[198:199], v200 offset:38304\n"
"v_cvt_pk_bf16_f32 v249, v98, v99\n"
"v_mfma_f32_16x16x32_bf16 v[20:23], v[188:191], a[36:39], v[20:23]\n"
"ds_read_b64_tr_b16 v[176:177], v200 offset:50688\n"
"v_mul_f32 v244, v220, v80\n"
"v_mfma_f32_16x16x32_bf16 v[16:19], v[188:191], a[32:35], v[16:19]\n"
"s_waitcnt lgkmcnt(3)\n"
"v_exp_f32 v244, v244\n"
"v_mfma_f32_16x16x32_bf16 v[32:35], v[192:195], a[32:35], v[32:35]\n"
"ds_read_b64_tr_b16 v[178:179], v200 offset:50944\n"
"v_fma_f32 v244, v244, v224, v224\n"
"v_mfma_f32_16x16x32_bf16 v[36:39], v[192:195], a[36:39], v[36:39]\n"
"v_rcp_f32 v244, v244\n"
"v_mfma_f32_16x16x32_bf16 v[40:43], v[192:195], a[40:43], v[40:43]\n"
"ds_read_b64_tr_b16 v[180:181], v200 offset:50720\n"
"v_mul_f32 v112, v80, v112\n"
"v_mfma_f32_16x16x32_bf16 v[44:47], v[192:195], a[44:47], v[44:47]\n"
"s_waitcnt lgkmcnt(3)\n"
"ds_read_b64_tr_b16 v[182:183], v200 offset:50976\n"
"v_mul_f32 v112, v112, v244\n"
"v_mfma_f32_16x16x32_bf16 v[60:63], v[196:199], a[44:47], v[60:63]\n"
"ds_read_b64_tr_b16 v[184:185], v200 offset:54912\n"
"v_mul_f32 v245, v220, v81\n"
"v_mfma_f32_16x16x32_bf16 v[56:59], v[196:199], a[40:43], v[56:59]\n"
"v_exp_f32 v245, v245\n"
"v_mfma_f32_16x16x32_bf16 v[52:55], v[196:199], a[36:39], v[52:55]\n"
"ds_read_b64_tr_b16 v[186:187], v200 offset:55168\n"
"v_fma_f32 v245, v245, v224, v224\n"
"v_mfma_f32_16x16x32_bf16 v[48:51], v[196:199], a[32:35], v[48:51]\n"
"s_waitcnt lgkmcnt(4)\n"
"v_rcp_f32 v245, v245\n"
"v_mfma_f32_16x16x32_bf16 v[0:3], v[176:179], a[48:51], v[0:3]\n"
"ds_read_b64_tr_b16 v[188:189], v200 offset:54944\n"
"v_mul_f32 v113, v81, v113\n"
"v_mfma_f32_16x16x32_bf16 v[4:7], v[176:179], a[52:55], v[4:7]\n"
"ds_read_b64_tr_b16 v[190:191], v200 offset:55200\n"
"v_mul_f32 v113, v113, v245\n"
"v_mfma_f32_16x16x32_bf16 v[8:11], v[176:179], a[56:59], v[8:11]\n"
"ds_read_b64_tr_b16 v[192:193], v200 offset:42240\n"
"v_cvt_pk_bf16_f32 v250, v112, v113\n"
"v_mfma_f32_16x16x32_bf16 v[12:15], v[176:179], a[60:63], v[12:15]\n"
"s_waitcnt lgkmcnt(4)\n"
"ds_read_b64_tr_b16 v[194:195], v200 offset:42496\n"
"v_mul_f32 v246, v220, v82\n"
"v_mfma_f32_16x16x32_bf16 v[28:31], v[180:183], a[60:63], v[28:31]\n"
"v_exp_f32 v246, v246\n"
"v_mfma_f32_16x16x32_bf16 v[24:27], v[180:183], a[56:59], v[24:27]\n"
"ds_read_b64_tr_b16 v[196:197], v200 offset:42272\n"
"v_fma_f32 v246, v246, v224, v224\n"
"v_mfma_f32_16x16x32_bf16 v[20:23], v[180:183], a[52:55], v[20:23]\n"
"v_rcp_f32 v246, v246\n"
"v_mfma_f32_16x16x32_bf16 v[16:19], v[180:183], a[48:51], v[16:19]\n"
"s_waitcnt lgkmcnt(3)\n"
"ds_read_b64_tr_b16 v[198:199], v200 offset:42528\n"
"v_mul_f32 v114, v82, v114\n"
"v_mfma_f32_16x16x32_bf16 v[32:35], v[184:187], a[48:51], v[32:35]\n"
"ds_read_b64_tr_b16 v[176:177], v200 offset:46464\n"
"v_mul_f32 v114, v114, v246\n"
"v_mfma_f32_16x16x32_bf16 v[36:39], v[184:187], a[52:55], v[36:39]\n"
"ds_read_b64_tr_b16 v[178:179], v200 offset:46720\n"
"v_mul_f32 v247, v220, v83\n"
"v_mfma_f32_16x16x32_bf16 v[40:43], v[184:187], a[56:59], v[40:43]\n"
"v_exp_f32 v247, v247\n"
"v_mfma_f32_16x16x32_bf16 v[44:47], v[184:187], a[60:63], v[44:47]\n"
"ds_read_b64_tr_b16 v[180:181], v200 offset:46496\n"
"v_fma_f32 v247, v247, v224, v224\n"
"v_mfma_f32_16x16x32_bf16 v[60:63], v[188:191], a[60:63], v[60:63]\n"
"v_rcp_f32 v247, v247\n"
"v_mfma_f32_16x16x32_bf16 v[56:59], v[188:191], a[56:59], v[56:59]\n"
"ds_read_b64_tr_b16 v[182:183], v200 offset:46752\n"
"v_mul_f32 v115, v83, v115\n"
"v_mfma_f32_16x16x32_bf16 v[52:55], v[188:191], a[52:55], v[52:55]\n"
"buffer_store_dwordx2 v[128:129], v142, s[44:47], s17 offen offset:512 sc0 sc1\n"
"v_mov_b32 v228, 0x7e\n"
"v_mfma_f32_16x16x32_bf16 v[48:51], v[188:191], a[48:51], v[48:51]\n"
"s_waitcnt lgkmcnt(2)\n"
"v_mul_f32 v115, v115, v247\n"
"v_cvt_pk_bf16_f32 v251, v114, v115\n"
"v_mfma_f32_16x16x32_bf16 v[0:3], v[192:195], a[64:67], v[0:3]\n"
"buffer_load_dwordx4 v207, s[24:27], s66 offen lds\n"
"v_mfma_f32_16x16x32_bf16 v[4:7], v[192:195], a[68:71], v[4:7]\n"
"s_add_u32 m0, s59, 0x3180\n"
"v_add_u32 v205, 0xffff1900, v205\n"
"v_mul_f32 v240, v221, v68\n"
"v_mfma_f32_16x16x32_bf16 v[8:11], v[192:195], a[72:75], v[8:11]\n"
"ds_write_b128 v205, v[248:251] offset:8448\n"
"v_mfma_f32_16x16x32_bf16 v[12:15], v[192:195], a[76:79], v[12:15]\n"
"v_exp_f32 v240, v240\n"
"v_mfma_f32_16x16x32_bf16 v[28:31], v[196:199], a[76:79], v[28:31]\n"
"s_waitcnt vmcnt(5)\n"
"buffer_store_dwordx2 v[236:237], v142, s[44:47], s17 offen offset:0 sc0 sc1\n"
"v_fma_f32 v240, v240, v225, v225\n"
"v_mfma_f32_16x16x32_bf16 v[24:27], v[196:199], a[72:75], v[24:27]\n"
"v_rcp_f32 v240, v240\n"
"v_mfma_f32_16x16x32_bf16 v[20:23], v[196:199], a[68:71], v[20:23]\n"
"s_barrier\n"
"v_mul_f32 v100, v68, v100\n"
"v_mul_f32 v100, v100, v240\n"
"v_mfma_f32_16x16x32_bf16 v[16:19], v[196:199], a[64:67], v[16:19]\n"
"buffer_store_dwordx2 v[128:129], v142, s[44:47], s17 offen offset:768 sc0 sc1\n"
"v_mul_f32 v241, v221, v69\n"
"v_mfma_f32_16x16x32_bf16 v[32:35], v[176:179], a[64:67], v[32:35]\n"
"v_exp_f32 v241, v241\n"
"v_mfma_f32_16x16x32_bf16 v[36:39], v[176:179], a[68:71], v[36:39]\n"
"buffer_load_dwordx4 v207, s[28:31], s66 offen lds\n"
"v_mfma_f32_16x16x32_bf16 v[40:43], v[176:179], a[72:75], v[40:43]\n"
"s_add_u32 s66, s60, 0x3a8180\n"
"s_add_u32 m0, s59, 0x4200\n"
"ds_read_b64_tr_b16 v[184:185], v201 offset:0\n"
"v_fma_f32 v241, v241, v225, v225\n"
"v_mfma_f32_16x16x32_bf16 v[44:47], v[176:179], a[76:79], v[44:47]\n"
"s_waitcnt lgkmcnt(1)\n"
"ds_read_b64_tr_b16 v[186:187], v201 offset:256\n"
"v_mul_f32 v101, v69, v101\n"
"v_mfma_f32_16x16x32_bf16 v[60:63], v[180:183], a[76:79], v[60:63]\n"
"v_rcp_f32 v241, v241\n"
"v_mfma_f32_16x16x32_bf16 v[56:59], v[180:183], a[72:75], v[56:59]\n"
"ds_read_b64_tr_b16 v[188:189], v201 offset:32\n"
"v_mul_f32 v101, v101, v241\n"
"v_mfma_f32_16x16x32_bf16 v[52:55], v[180:183], a[68:71], v[52:55]\n"
"buffer_load_dwordx4 v207, s[24:27], s66 offen lds\n"
"v_mfma_f32_16x16x32_bf16 v[48:51], v[180:183], a[64:67], v[48:51]\n"
"s_waitcnt lgkmcnt(1)\n"
"s_add_u32 m0, s59, 0x5280\n"
"ds_read_b64_tr_b16 v[190:191], v201 offset:288\n"
"v_cvt_pk_bf16_f32 v252, v100, v101\n"
"v_mfma_f32_16x16x32_bf16 v[0:3], v[184:187], a[80:83], v[0:3]\n"
"ds_read_b64_tr_b16 v[192:193], v201 offset:4224\n"
"v_mul_f32 v242, v221, v70\n"
"v_mfma_f32_16x16x32_bf16 v[4:7], v[184:187], a[84:87], v[4:7]\n"
"v_exp_f32 v242, v242\n"
"v_mfma_f32_16x16x32_bf16 v[8:11], v[184:187], a[88:91], v[8:11]\n"
"ds_read_b64_tr_b16 v[194:195], v201 offset:4480\n"
"v_fma_f32 v242, v242, v225, v225\n"
"v_mfma_f32_16x16x32_bf16 v[12:15], v[184:187], a[92:95], v[12:15]\n"
"L_st7_%=:\n s_cbranch_execz L_st11_%=\n"
"s_waitcnt lgkmcnt(2)\n"
"buffer_store_dwordx4 v[248:251], v139, s[44:47], s52 offen offset:0 sc0 sc1\n"
"v_mul_f32 v102, v70, v102\n"
"v_mfma_f32_16x16x32_bf16 v[28:31], v[188:191], a[92:95], v[28:31]\n"
"s_add_u32 s52, s70, 0xa00000\n"
"v_rcp_f32 v242, v242\n"
"v_mfma_f32_16x16x32_bf16 v[24:27], v[188:191], a[88:91], v[24:27]\n"
"ds_read_b64_tr_b16 v[196:197], v201 offset:4256\n"
"v_mul_f32 v102, v102, v242\n"
"v_mfma_f32_16x16x32_bf16 v[20:23], v[188:191], a[84:87], v[20:23]\n"
"ds_read_b64_tr_b16 v[198:199], v201 offset:4512\n"
"v_mul_f32 v243, v221, v71\n"
"v_mfma_f32_16x16x32_bf16 v[16:19], v[188:191], a[80:83], v[16:19]\n"
"s_waitcnt lgkmcnt(2)\n"
"buffer_load_dwordx4 v207, s[28:31], s66 offen lds\n"
"v_mfma_f32_16x16x32_bf16 v[32:35], v[192:195], a[80:83], v[32:35]\n"
"s_add_u32 s66, s60, 0x3f0180\n"
"s_add_u32 m0, s59, 0x6300\n"
"v_exp_f32 v243, v243\n"
"v_mfma_f32_16x16x32_bf16 v[36:39], v[192:195], a[84:87], v[36:39]\n"
"ds_read_b64_tr_b16 v[176:177], v201 offset:42240\n"
"v_fma_f32 v243, v243, v225, v225\n"
"v_mfma_f32_16x16x32_bf16 v[40:43], v[192:195], a[88:91], v[40:43]\n"
"v_rcp_f32 v243, v243\n"
"v_mfma_f32_16x16x32_bf16 v[44:47], v[192:195], a[92:95], v[44:47]\n"
"s_waitcnt lgkmcnt(1)\n"
"ds_read_b64_tr_b16 v[178:179], v201 offset:42496\n"
"v_mul_f32 v103, v71, v103\n"
"v_mfma_f32_16x16x32_bf16 v[60:63], v[196:199], a[92:95], v[60:63]\n"
"buffer_load_dwordx4 v207, s[24:27], s66 offen lds\n"
"v_mfma_f32_16x16x32_bf16 v[56:59], v[196:199], a[88:91], v[56:59]\n"
"s_add_u32 m0, s59, 0x7380\n"
"ds_read_b64_tr_b16 v[180:181], v201 offset:42272\n"
"v_mul_f32 v103, v103, v243\n"
"v_mfma_f32_16x16x32_bf16 v[52:55], v[196:199], a[84:87], v[52:55]\n"
"ds_read_b64_tr_b16 v[182:183], v201 offset:42528\n"
"v_cvt_pk_bf16_f32 v253, v102, v103\n"
"v_mfma_f32_16x16x32_bf16 v[48:51], v[196:199], a[80:83], v[48:51]\n"
"s_waitcnt lgkmcnt(2)\n"
"ds_read_b64_tr_b16 v[184:185], v201 offset:46464\n"
"v_mul_f32 v244, v221, v84\n"
"v_mfma_f32_16x16x32_bf16 v[0:3], v[176:179], a[96:99], v[0:3]\n"
"v_exp_f32 v244, v244\n"
"v_mfma_f32_16x16x32_bf16 v[4:7], v[176:179], a[100:103], v[4:7]\n"
"buffer_load_dwordx4 v207, s[28:31], s66 offen lds\n"
"v_mfma_f32_16x16x32_bf16 v[8:11], v[176:179], a[104:107], v[8:11]\n"
"s_add_u32 s66, s60, 0x438180\n"
"s_add_u32 m0, s59, 0x8400\n"
"ds_read_b64_tr_b16 v[186:187], v201 offset:46720\n"
"v_fma_f32 v244, v244, v225, v225\n"
"v_mfma_f32_16x16x32_bf16 v[12:15], v[176:179], a[108:111], v[12:15]\n"
"s_waitcnt lgkmcnt(2)\n"
"v_rcp_f32 v244, v244\n"
"v_mfma_f32_16x16x32_bf16 v[28:31], v[180:183], a[108:111], v[28:31]\n"
"ds_read_b64_tr_b16 v[188:189], v201 offset:46496\n"
"v_mul_f32 v116, v84, v116\n"
"v_mfma_f32_16x16x32_bf16 v[24:27], v[180:183], a[104:107], v[24:27]\n"
"ds_read_b64_tr_b16 v[190:191], v201 offset:46752\n"
"v_mul_f32 v116, v116, v244\n"
"v_mfma_f32_16x16x32_bf16 v[20:23], v[180:183], a[100:103], v[20:23]\n"
"buffer_store_dwordx2 v[128:129], v137, s[44:47], s88 offen offset:1024 sc0 sc1\n"
"v_mul_f32 v245, v221, v85\n"
"v_mfma_f32_16x16x32_bf16 v[16:19], v[180:183], a[96:99], v[16:19]\n"
"s_waitcnt lgkmcnt(2)\n"
"v_exp_f32 v245, v245\n"
"v_mfma_f32_16x16x32_bf16 v[32:35], v[184:187], a[96:99], v[32:35]\n"
"buffer_load_dwordx4 v207, s[24:27], s66 offen lds\n"
"v_fma_f32 v245, v245, v225, v225\n"
"v_mfma_f32_16x16x32_bf16 v[36:39], v[184:187], a[100:103], v[36:39]\n"
"s_add_u32 m0, s59, 0x9480\n"
"v_rcp_f32 v245, v245\n"
"v_mfma_f32_16x16x32_bf16 v[40:43], v[184:187], a[104:107], v[40:43]\n"
"ds_read_b64_tr_b16 v[192:193], v201 offset:50688\n"
"v_mul_f32 v117, v85, v117\n"
"v_mul_f32 v117, v117, v245\n"
"v_mfma_f32_16x16x32_bf16 v[44:47], v[184:187], a[108:111], v[44:47]\n"
"s_waitcnt lgkmcnt(1)\n"
"ds_read_b64_tr_b16 v[194:195], v201 offset:50944\n"
"v_cvt_pk_bf16_f32 v254, v116, v117\n"
"v_mfma_f32_16x16x32_bf16 v[60:63], v[188:191], a[108:111], v[60:63]\n"
"ds_read_b64_tr_b16 v[196:197], v201 offset:50720\n"
"v_mul_f32 v246, v221, v86\n"
"v_mfma_f32_16x16x32_bf16 v[56:59], v[188:191], a[104:107], v[56:59]\n"
"buffer_load_dwordx4 v207, s[28:31], s66 offen lds\n"
"v_exp_f32 v246, v246\n"
"v_mfma_f32_16x16x32_bf16 v[52:55], v[188:191], a[100:103], v[52:55]\n"
"s_add_u32 s66, s60, 0x480180\n"
"s_add_u32 m0, s59, 0xc600\n"
"ds_read_b64_tr_b16 v[198:199], v201 offset:50976\n"
"v_fma_f32 v246, v246, v225, v225\n"
"v_mfma_f32_16x16x32_bf16 v[48:51], v[188:191], a[96:99], v[48:51]\n"
"s_waitcnt lgkmcnt(2)\n"
"v_rcp_f32 v246, v246\n"
"v_mfma_f32_16x16x32_bf16 v[0:3], v[192:195], a[112:115], v[0:3]\n"
"ds_read_b64_tr_b16 v[176:177], v201 offset:54912\n"
"v_mul_f32 v118, v86, v118\n"
"v_mfma_f32_16x16x32_bf16 v[4:7], v[192:195], a[116:119], v[4:7]\n"
"ds_read_b64_tr_b16 v[178:179], v201 offset:55168\n"
"v_mul_f32 v118, v118, v246\n"
"v_mfma_f32_16x16x32_bf16 v[8:11], v[192:195], a[120:123], v[8:11]\n"
"buffer_load_dwordx4 v207, s[24:27], s66 offen lds\n"
"v_mul_f32 v247, v221, v87\n"
"v_exp_f32 v247, v247\n"
"v_mfma_f32_16x16x32_bf16 v[12:15], v[192:195], a[124:127], v[12:15]\n"
"s_waitcnt lgkmcnt(2)\n"
"s_add_u32 m0, s59, 0xd680\n"
"ds_read_b64_tr_b16 v[180:181], v201 offset:54944\n"
"v_fma_f32 v247, v247, v225, v225\n"
"v_mfma_f32_16x16x32_bf16 v[28:31], v[196:199], a[124:127], v[28:31]\n"
"v_rcp_f32 v247, v247\n"
"v_mfma_f32_16x16x32_bf16 v[24:27], v[196:199], a[120:123], v[24:27]\n"
"ds_read_b64_tr_b16 v[182:183], v201 offset:55200\n"
"v_mul_f32 v119, v87, v119\n"
"v_mfma_f32_16x16x32_bf16 v[20:23], v[196:199], a[116:119], v[20:23]\n"
"s_barrier\n"
"v_mul_f32 v119, v119, v247\n"
"v_cvt_pk_bf16_f32 v255, v118, v119\n"
"v_mfma_f32_16x16x32_bf16 v[16:19], v[196:199], a[112:115], v[16:19]\n"
"s_waitcnt lgkmcnt(2)\n"
"buffer_store_dwordx2 v[128:129], v135, s[44:47], s84 offen offset:0 sc0 sc1\n"
"v_mul_f32 v240, v222, v72\n"
"v_exp_f32 v240, v240\n"
"v_mfma_f32_16x16x32_bf16 v[32:35], v[176:179], a[112:115], v[32:35]\n"
"s_mul_i32 s84, s68, 0x210\n"
"ds_write_b128 v205, v[252:255] offset:9536\n"
"v_fma_f32 v240, v240, v226, v226\n"
"v_mfma_f32_16x16x32_bf16 v[36:39], v[176:179], a[116:119], v[36:39]\n"
"s_add_u32 s84, s84, s88\n"
"buffer_load_dwordx4 v207, s[28:31], s66 offen lds\n"
"v_mul_f32 v104, v72, v104\n"
"v_mfma_f32_16x16x32_bf16 v[40:43], v[176:179], a[120:123], v[40:43]\n"
"s_add_u32 s66, s60, 0x4c8180\n"
"s_add_u32 m0, s59, 0xa500\n"
"ds_read_b64_tr_b16 v[184:185], v202 offset:0\n"
"v_rcp_f32 v240, v240\n"
"v_mfma_f32_16x16x32_bf16 v[44:47], v[176:179], a[124:127], v[44:47]\n"
"s_waitcnt lgkmcnt(2)\n"
"ds_read_b64_tr_b16 v[186:187], v202 offset:256\n"
"v_mul_f32 v104, v104, v240\n"
"v_mfma_f32_16x16x32_bf16 v[60:63], v[180:183], a[124:127], v[60:63]\n"
"s_mul_i32 s68, s58, 0x280\n"
"ds_read_b64_tr_b16 v[188:189], v202 offset:32\n"
"v_mul_f32 v241, v222, v73\n"
"v_mfma_f32_16x16x32_bf16 v[56:59], v[180:183], a[120:123], v[56:59]\n"
"s_sub_u32 s68, s76, s68\n"
"v_exp_f32 v241, v241\n"
"v_mfma_f32_16x16x32_bf16 v[52:55], v[180:183], a[116:119], v[52:55]\n"
"buffer_load_dwordx4 v207, s[24:27], s66 offen lds\n"
"v_fma_f32 v241, v241, v226, v226\n"
"v_mfma_f32_16x16x32_bf16 v[48:51], v[180:183], a[112:115], v[48:51]\n"
"s_waitcnt lgkmcnt(1)\n"
"s_add_u32 m0, s59, 0xb580\n"
"ds_read_b64_tr_b16 v[190:191], v202 offset:288\n"
"v_rcp_f32 v241, v241\n"
"v_mfma_f32_16x16x32_bf16 v[0:3], v[184:187], a[128:131], v[0:3]\n"
"s_add_u32 s24, s36, s74\n s_addc_u32 s25, s37, 0\n"
"ds_read_b64_tr_b16 v[192:193], v202 offset:4224\n"
"v_mul_f32 v105, v73, v105\n"
"v_mfma_f32_16x16x32_bf16 v[4:7], v[184:187], a[132:135], v[4:7]\n"
"s_sub_u32 s26, s38, s74\n"
"ds_read_b64_tr_b16 v[194:195], v202 offset:4480\n"
"v_mul_f32 v105, v105, v241\n"
"v_mfma_f32_16x16x32_bf16 v[8:11], v[184:187], a[136:139], v[8:11]\n"
"ds_read_b64_tr_b16 v[196:197], v202 offset:4256\n"
"v_cvt_pk_bf16_f32 v248, v104, v105\n"
"v_mfma_f32_16x16x32_bf16 v[12:15], v[184:187], a[140:143], v[12:15]\n"
"s_waitcnt lgkmcnt(3)\n"
"buffer_store_dwordx4 v[252:255], v139, s[44:47], s52 offen offset:0 sc0 sc1\n"
"v_mul_f32 v242, v222, v74\n"
"v_exp_f32 v242, v242\n"
"v_mfma_f32_16x16x32_bf16 v[28:31], v[188:191], a[140:143], v[28:31]\n"
"s_add_u32 s52, s71, 0x800000\n"
"ds_read_b64_tr_b16 v[198:199], v202 offset:4512\n"
"v_fma_f32 v242, v242, v226, v226\n"
"v_mfma_f32_16x16x32_bf16 v[24:27], v[188:191], a[136:139], v[24:27]\n"
"v_rcp_f32 v242, v242\n"
"v_mfma_f32_16x16x32_bf16 v[20:23], v[188:191], a[132:135], v[20:23]\n"
"ds_read_b64_tr_b16 v[176:177], v202 offset:8448\n"
"v_mul_f32 v106, v74, v106\n"
"v_mfma_f32_16x16x32_bf16 v[16:19], v[188:191], a[128:131], v[16:19]\n"
"s_waitcnt lgkmcnt(3)\n"
"buffer_load_dwordx4 v207, s[28:31], s66 offen lds\n"
"v_mul_f32 v106, v106, v242\n"
"v_mfma_f32_16x16x32_bf16 v[32:35], v[192:195], a[128:131], v[32:35]\n"
"s_add_u32 s66, s62, 0\n"
"s_add_u32 m0, s59, 0xe700\n"
"ds_read_b64_tr_b16 v[178:179], v202 offset:8704\n"
"v_mul_f32 v243, v222, v75\n"
"v_exp_f32 v243, v243\n"
"v_mfma_f32_16x16x32_bf16 v[36:39], v[192:195], a[132:135], v[36:39]\n"
"ds_read_b64_tr_b16 v[180:181], v202 offset:8480\n"
"v_fma_f32 v243, v243, v226, v226\n"
"v_mfma_f32_16x16x32_bf16 v[40:43], v[192:195], a[136:139], v[40:43]\n"
"v_rcp_f32 v243, v243\n"
"v_mfma_f32_16x16x32_bf16 v[44:47], v[192:195], a[140:143], v[44:47]\n"
"s_waitcnt lgkmcnt(3)\n"
"ds_read_b64_tr_b16 v[182:183], v202 offset:8736\n"
"v_mul_f32 v107, v75, v107\n"
"v_mfma_f32_16x16x32_bf16 v[60:63], v[196:199], a[140:143], v[60:63]\n"
"buffer_load_dwordx4 v210, s[48:51], s66 offen lds\n"
"v_mul_f32 v107, v107, v243\n"
"v_mfma_f32_16x16x32_bf16 v[56:59], v[196:199], a[136:139], v[56:59]\n"
"s_add_u32 s66, s62, 0x48000\n"
"s_add_u32 m0, s59, 0xf780\n"
"ds_read_b64_tr_b16 v[184:185], v202 offset:12672\n"
"v_cvt_pk_bf16_f32 v249, v106, v107\n"
"v_mul_f32 v244, v222, v88\n"
"v_mfma_f32_16x16x32_bf16 v[52:55], v[196:199], a[132:135], v[52:55]\n"
"v_exp_f32 v244, v244\n"
"v_mfma_f32_16x16x32_bf16 v[48:51], v[196:199], a[128:131], v[48:51]\n"
"s_waitcnt lgkmcnt(2)\n"
"ds_read_b64_tr_b16 v[186:187], v202 offset:12928\n"
"v_fma_f32 v244, v244, v226, v226\n"
"v_mfma_f32_16x16x32_bf16 v[0:3], v[176:179], a[144:147], v[0:3]\n"
"v_rcp_f32 v244, v244\n"
"v_mfma_f32_16x16x32_bf16 v[4:7], v[176:179], a[148:151], v[4:7]\n"
"buffer_load_dwordx4 v210, s[48:51], s66 offen lds\n"
"v_mul_f32 v120, v88, v120\n"
"v_mul_f32 v120, v120, v244\n"
"v_mfma_f32_16x16x32_bf16 v[8:11], v[176:179], a[152:155], v[8:11]\n"
"s_add_u32 s66, s62, 0x90000\n"
"s_add_u32 m0, s59, 0x18c00\n"
"ds_read_b64_tr_b16 v[188:189], v202 offset:12704\n"
"v_mul_f32 v245, v222, v89\n"
"v_mfma_f32_16x16x32_bf16 v[12:15], v[176:179], a[156:159], v[12:15]\n"
"s_waitcnt lgkmcnt(2)\n"
"v_exp_f32 v245, v245\n"
"v_mfma_f32_16x16x32_bf16 v[28:31], v[180:183], a[156:159], v[28:31]\n"
"ds_read_b64_tr_b16 v[190:191], v202 offset:12960\n"
"v_fma_f32 v245, v245, v226, v226\n"
"v_mfma_f32_16x16x32_bf16 v[24:27], v[180:183], a[152:155], v[24:27]\n"
"v_rcp_f32 v245, v245\n"
"v_mul_f32 v121, v89, v121\n"
"v_mfma_f32_16x16x32_bf16 v[20:23], v[180:183], a[148:151], v[20:23]\n"
"buffer_store_dwordx2 v[128:129], v135, s[44:47], s84 offen offset:256 sc0 sc1\n"
"v_mul_f32 v121, v121, v245\n"
"v_mfma_f32_16x16x32_bf16 v[16:19], v[180:183], a[144:147], v[16:19]\n"
"s_waitcnt lgkmcnt(2)\n"
"ds_read_b64_tr_b16 v[192:193], v202 offset:16896\n"
"v_cvt_pk_bf16_f32 v250, v120, v121\n"
"v_mfma_f32_16x16x32_bf16 v[32:35], v[184:187], a[144:147], v[32:35]\n"
"s_mul_i32 s84, s68, 0x210\n"
"buffer_load_dwordx4 v210, s[48:51], s66 offen lds\n"
"v_mul_f32 v246, v222, v90\n"
"v_mfma_f32_16x16x32_bf16 v[36:39], v[184:187], a[148:151], v[36:39]\n"
"s_add_u32 s66, s62, 0xd8000\n"
"s_add_u32 m0, s59, 0x19c80\n"
"v_exp_f32 v246, v246\n"
"v_mul_f32 v122, v90, v122\n"
"v_mfma_f32_16x16x32_bf16 v[40:43], v[184:187], a[152:155], v[40:43]\n"
"s_add_u32 s84, s84, s88\n"
"ds_read_b64_tr_b16 v[194:195], v202 offset:17152\n"
"v_fma_f32 v246, v246, v226, v226\n"
"v_mfma_f32_16x16x32_bf16 v[44:47], v[184:187], a[156:159], v[44:47]\n"
"s_waitcnt lgkmcnt(2)\n"
"v_rcp_f32 v246, v246\n"
"v_mfma_f32_16x16x32_bf16 v[60:63], v[188:191], a[156:159], v[60:63]\n"
"s_mul_i32 s68, s58, 0x280\n"
"ds_read_b64_tr_b16 v[196:197], v202 offset:16928\n"
"v_mul_f32 v122, v122, v246\n"
"v_mfma_f32_16x16x32_bf16 v[56:59], v[188:191], a[152:155], v[56:59]\n"
"buffer_load_dwordx4 v210, s[48:51], s66 offen lds\n"
"v_mul_f32 v247, v222, v91\n"
"v_exp_f32 v247, v247\n"
"v_mfma_f32_16x16x32_bf16 v[52:55], v[188:191], a[148:151], v[52:55]\n"
"s_add_u32 s66, s62, 0x120000\n"
"s_add_u32 m0, s59, 0x1ef00\n"
"ds_read_b64_tr_b16 v[198:199], v202 offset:17184\n"
"v_fma_f32 v247, v247, v226, v226\n"
"v_mfma_f32_16x16x32_bf16 v[48:51], v[188:191], a[144:147], v[48:51]\n"
"s_waitcnt lgkmcnt(2)\n"
"v_rcp_f32 v247, v247\n"
"v_mfma_f32_16x16x32_bf16 v[0:3], v[192:195], a[160:163], v[0:3]\n"
"s_sub_u32 s68, s77, s68\n"
"ds_read_b64_tr_b16 v[176:177], v202 offset:21120\n"
"v_mul_f32 v123, v91, v123\n"
"v_mfma_f32_16x16x32_bf16 v[4:7], v[192:195], a[164:167], v[4:7]\n"
"ds_read_b64_tr_b16 v[178:179], v202 offset:21376\n"
"v_mul_f32 v123, v123, v247\n"
"v_mfma_f32_16x16x32_bf16 v[8:11], v[192:195], a[168:171], v[8:11]\n"
"buffer_store_dwordx2 v[128:129], v135, s[44:47], s84 offen offset:512 sc0 sc1\n"
"v_cvt_pk_bf16_f32 v251, v122, v123\n"
"v_mfma_f32_16x16x32_bf16 v[12:15], v[192:195], a[172:175], v[12:15]\n"
"s_waitcnt lgkmcnt(2)\n"
"ds_read_b64_tr_b16 v[180:181], v202 offset:21152\n"
"v_add_u32 v206, 0xffff1900, v206\n"
"v_mul_f32 v240, v223, v76\n"
"v_mfma_f32_16x16x32_bf16 v[28:31], v[196:199], a[172:175], v[28:31]\n"
"s_mul_i32 s84, s68, 0x210\n"
"ds_write_b128 v206, v[248:251] offset:8448\n"
"v_mul_f32 v108, v76, v108\n"
"v_mfma_f32_16x16x32_bf16 v[24:27], v[196:199], a[168:171], v[24:27]\n"
"s_waitcnt vmcnt(17)\n"
"v_exp_f32 v240, v240\n"
"v_mfma_f32_16x16x32_bf16 v[20:23], v[196:199], a[164:167], v[20:23]\n"
"s_barrier\n"
"ds_read_b64_tr_b16 v[182:183], v202 offset:21408\n"
"v_fma_f32 v240, v240, v227, v227\n"
"v_mfma_f32_16x16x32_bf16 v[16:19], v[196:199], a[160:163], v[16:19]\n"
"s_waitcnt lgkmcnt(3)\n"
"buffer_load_dwordx4 v210, s[48:51], s66 offen lds\n"
"v_rcp_f32 v240, v240\n"
"v_mfma_f32_16x16x32_bf16 v[32:35], v[176:179], a[160:163], v[32:35]\n"
"s_add_u32 s66, s62, 0x168000\n"
"s_add_u32 m0, s59, 0x1ff80\n"
"ds_read_b64_tr_b16 v[184:185], v200 offset:0\n"
"v_mul_f32 v108, v108, v240\n"
"v_mfma_f32_16x16x32_bf16 v[36:39], v[176:179], a[164:167], v[36:39]\n"
"s_add_u32 s84, s84, s88\n"
"ds_read_b64_tr_b16 v[186:187], v200 offset:256\n"
"v_mul_f32 v241, v223, v77\n"
"v_mfma_f32_16x16x32_bf16 v[40:43], v[176:179], a[168:171], v[40:43]\n"
"s_mul_i32 s68, s58, 0x280\n"
"v_exp_f32 v241, v241\n"
"v_mfma_f32_16x16x32_bf16 v[44:47], v[176:179], a[172:175], v[44:47]\n"
"s_waitcnt lgkmcnt(2)\n"
"buffer_store_dwordx4 v[248:251], v139, s[44:47], s52 offen offset:0 sc0 sc1\n"
"v_fma_f32 v241, v241, v227, v227\n"
"v_rcp_f32 v241, v241\n"
"v_mfma_f32_16x16x32_bf16 v[60:63], v[180:183], a[172:175], v[60:63]\n"
"s_add_u32 s52, s71, 0xa00000\n"
"ds_read_b64_tr_b16 v[188:189], v200 offset:32\n"
"v_mul_f32 v109, v77, v109\n"
"v_mfma_f32_16x16x32_bf16 v[56:59], v[180:183], a[168:171], v[56:59]\n"
"s_sub_u32 s68, s74, s68\n"
"ds_read_b64_tr_b16 v[190:191], v200 offset:288\n"
"v_mul_f32 v109, v109, v241\n"
"v_mfma_f32_16x16x32_bf16 v[52:55], v[180:183], a[164:167], v[52:55]\n"
"s_mul_i32 s69, s68, 0x80\n"
"ds_read_b64_tr_b16 v[192:193], v200 offset:4224\n"
"v_cvt_pk_bf16_f32 v252, v108, v109\n"
"v_mfma_f32_16x16x32_bf16 v[48:51], v[180:183], a[160:163], v[48:51]\n"
"s_waitcnt lgkmcnt(3)\n"
"buffer_load_dwordx4 v210, s[48:51], s66 offen lds\n"
"v_mul_f32 v242, v223, v78\n"
"v_mfma_f32_16x16x32_bf16 v[0:3], v[184:187], a[176:179], v[0:3]\n"
"s_add_u32 s66, s62, 0x1b0000\n"
"s_add_u32 m0, s59, 0\n"
"v_exp_f32 v242, v242\n"
"v_mfma_f32_16x16x32_bf16 v[4:7], v[184:187], a[180:183], v[4:7]\n"
"ds_read_b64_tr_b16 v[194:195], v200 offset:4480\n"
"v_fma_f32 v242, v242, v227, v227\n"
"v_rcp_f32 v242, v242\n"
"v_mfma_f32_16x16x32_bf16 v[8:11], v[184:187], a[184:187], v[8:11]\n"
"s_add_u32 s69, s69, s19\n"
"ds_read_b64_tr_b16 v[196:197], v200 offset:4256\n"
"v_mul_f32 v110, v78, v110\n"
"v_mfma_f32_16x16x32_bf16 v[12:15], v[184:187], a[188:191], v[12:15]\n"
"s_waitcnt lgkmcnt(3)\n"
"ds_read_b64_tr_b16 v[198:199], v200 offset:4512\n"
"v_mul_f32 v110, v110, v242\n"
"v_mfma_f32_16x16x32_bf16 v[28:31], v[188:191], a[188:191], v[28:31]\n"
"s_add_u32 s28, s36, s75\n s_addc_u32 s29, s37, 0\n"
"ds_read_b64_tr_b16 v[176:177], v200 offset:8448\n"
"v_mul_f32 v243, v223, v79\n"
"v_mfma_f32_16x16x32_bf16 v[24:27], v[188:191], a[184:187], v[24:27]\n"
"v_exp_f32 v243, v243\n"
"v_mul_f32 v111, v79, v111\n"
"v_mfma_f32_16x16x32_bf16 v[20:23], v[188:191], a[180:183], v[20:23]\n"
"s_sub_u32 s30, s38, s75\n"
"ds_read_b64_tr_b16 v[178:179], v200 offset:8704\n"
"v_fma_f32 v243, v243, v227, v227\n"
"v_mfma_f32_16x16x32_bf16 v[16:19], v[188:191], a[176:179], v[16:19]\n"
"s_waitcnt lgkmcnt(4)\n"
"v_rcp_f32 v243, v243\n"
"v_mul_f32 v244, v223, v92\n"
"v_mfma_f32_16x16x32_bf16 v[32:35], v[192:195], a[176:179], v[32:35]\n"
"s_add_u32 s89, s69, s9\n"
"v_exp_f32 v244, v244\n"
"v_mfma_f32_16x16x32_bf16 v[36:39], v[192:195], a[180:183], v[36:39]\n"
"ds_read_b64_tr_b16 v[180:181], v200 offset:8480\n"
"v_mul_f32 v111, v111, v243\n"
"v_cvt_pk_bf16_f32 v253, v110, v111\n"
"v_mfma_f32_16x16x32_bf16 v[40:43], v[192:195], a[184:187], v[40:43]\n"
"s_mul_i32 s60, s68, 0x210\n"
"ds_read_b64_tr_b16 v[182:183], v200 offset:8736\n"
"v_fma_f32 v244, v244, v227, v227\n"
"v_mfma_f32_16x16x32_bf16 v[44:47], v[192:195], a[188:191], v[44:47]\n"
"s_waitcnt lgkmcnt(3)\n"
"buffer_store_dwordx2 v[128:129], v135, s[44:47], s84 offen offset:768 sc0 sc1\n"
"v_rcp_f32 v244, v244\n"
"v_mfma_f32_16x16x32_bf16 v[60:63], v[196:199], a[188:191], v[60:63]\n"
"s_mul_i32 s68, s58, 0x280\n"
"ds_read_b64_tr_b16 v[184:185], v200 offset:12672\n"
"v_mul_f32 v124, v92, v124\n"
"v_mfma_f32_16x16x32_bf16 v[56:59], v[196:199], a[184:187], v[56:59]\n"
"s_sub_u32 s68, s75, s68\n"
"ds_read_b64_tr_b16 v[186:187], v200 offset:12928\n"
"v_mul_f32 v124, v124, v244\n"
"v_mfma_f32_16x16x32_bf16 v[52:55], v[196:199], a[180:183], v[52:55]\n"
"ds_read_b64_tr_b16 v[188:189], v200 offset:12704\n"
"v_mul_f32 v245, v223, v93\n"
"v_exp_f32 v245, v245\n"
"v_mfma_f32_16x16x32_bf16 v[48:51], v[196:199], a[176:179], v[48:51]\n"
"s_waitcnt lgkmcnt(4)\n"
"ds_read_b64_tr_b16 v[190:191], v200 offset:12960\n"
"v_fma_f32 v245, v245, v227, v227\n"
"v_mfma_f32_16x16x32_bf16 v[0:3], v[176:179], a[192:195], v[0:3]\n"
"s_add_u32 s60, s60, s88\n"
"v_rcp_f32 v245, v245\n"
"v_mfma_f32_16x16x32_bf16 v[4:7], v[176:179], a[196:199], v[4:7]\n"
"s_mul_i32 s14, s68, 0x210\n"
"ds_read_b64_tr_b16 v[192:193], v200 offset:16896\n"
"v_mul_f32 v246, v223, v94\n"
"v_mfma_f32_16x16x32_bf16 v[8:11], v[176:179], a[200:203], v[8:11]\n"
"v_exp_f32 v246, v246\n"
"v_mul_f32 v247, v223, v95\n"
"v_mfma_f32_16x16x32_bf16 v[12:15], v[176:179], a[204:207], v[12:15]\n"
"s_waitcnt lgkmcnt(3)\n"
"v_exp_f32 v247, v247\n"
"v_mfma_f32_16x16x32_bf16 v[28:31], v[180:183], a[204:207], v[28:31]\n"
"ds_read_b64_tr_b16 v[194:195], v200 offset:17152\n"
"v_fma_f32 v246, v246, v227, v227\n"
"v_fma_f32 v247, v247, v227, v227\n"
"v_mfma_f32_16x16x32_bf16 v[24:27], v[180:183], a[200:203], v[24:27]\n"
"s_add_u32 s14, s14, s88\n"
"v_rcp_f32 v246, v246\n"
"v_mfma_f32_16x16x32_bf16 v[20:23], v[180:183], a[196:199], v[20:23]\n"
"v_rcp_f32 v247, v247\n"
"v_mul_f32 v125, v93, v125\n"
"v_mfma_f32_16x16x32_bf16 v[16:19], v[180:183], a[192:195], v[16:19]\n"
"ds_read_b64_tr_b16 v[196:197], v200 offset:16928\n"
"v_mul_f32 v126, v94, v126\n"
"v_mfma_f32_16x16x32_bf16 v[32:35], v[184:187], a[192:195], v[32:35]\n"
"s_mov_b64 exec, 0x30000\n buffer_load_dwordx2 v[230:231], v138, s[44:47], s16 offen offset:0 sc0 sc1\n s_mov_b64 exec, -1\n"
"v_mul_f32 v127, v95, v127\n"
"v_mul_f32 v125, v125, v245\n"
"v_mfma_f32_16x16x32_bf16 v[36:39], v[184:187], a[196:199], v[36:39]\n"
"ds_read_b64_tr_b16 v[198:199], v200 offset:17184\n"
"v_mul_f32 v126, v126, v246\n"
"v_mfma_f32_16x16x32_bf16 v[40:43], v[184:187], a[200:203], v[40:43]\n"
"ds_read_b64_tr_b16 v[176:177], v200 offset:21120\n"
"v_mul_f32 v127, v127, v247\n"
"v_mfma_f32_16x16x32_bf16 v[44:47], v[184:187], a[204:207], v[44:47]\n"
"s_waitcnt lgkmcnt(3)\n"
"ds_read_b64_tr_b16 v[178:179], v200 offset:21376\n"
"v_cvt_pk_bf16_f32 v254, v124, v125\n"
"v_cvt_pk_bf16_f32 v255, v126, v127\n"
"v_mfma_f32_16x16x32_bf16 v[60:63], v[188:191], a[204:207], v[60:63]\n"
"buffer_store_dwordx4 v[252:255], v139, s[44:47], s52 offen offset:0 sc0 sc1\n"
"v_subrev_u32 v201, s67, v201\n"
"v_mfma_f32_16x16x32_bf16 v[56:59], v[188:191], a[200:203], v[56:59]\n"
"s_add_u32 s52, s70, 0xc00000\n"
"ds_write_b128 v206, v[252:255] offset:9536\n"
"v_subrev_u32 v202, s67, v202\n"
"v_mfma_f32_16x16x32_bf16 v[52:55], v[188:191], a[196:199], v[52:55]\n"
"ds_read_b64_tr_b16 v[180:181], v200 offset:21152\n"
"v_mfma_f32_16x16x32_bf16 v[48:51], v[188:191], a[192:195], v[48:51]\n"
"ds_read_b64_tr_b16 v[182:183], v200 offset:21408\n"
"v_mfma_f32_16x16x32_bf16 v[0:3], v[192:195], a[208:211], v[0:3]\n"
"v_mfma_f32_16x16x32_bf16 v[4:7], v[192:195], a[212:215], v[4:7]\n"
"v_mfma_f32_16x16x32_bf16 v[8:11], v[192:195], a[216:219], v[8:11]\n"
"v_mfma_f32_16x16x32_bf16 v[12:15], v[192:195], a[220:223], v[12:15]\n"
"s_waitcnt lgkmcnt(2)\n"
"v_mfma_f32_16x16x32_bf16 v[28:31], v[196:199], a[220:223], v[28:31]\n"
"v_mfma_f32_16x16x32_bf16 v[24:27], v[196:199], a[216:219], v[24:27]\n"
"s_waitcnt vmcnt(15)\n"
"v_mfma_f32_16x16x32_bf16 v[20:23], v[196:199], a[212:215], v[20:23]\n"
"s_barrier\n"
"v_mfma_f32_16x16x32_bf16 v[16:19], v[196:199], a[208:211], v[16:19]\n"
"buffer_load_dwordx4 v210, s[48:51], s66 offen lds\n"
"v_mfma_f32_16x16x32_bf16 v[32:35], v[176:179], a[208:211], v[32:35]\n"
"s_add_u32 s66, s62, 0x1f8000\n"
"s_add_u32 m0, s59, 0x1080\n"
"ds_read_b64_tr_b16 v[184:185], v200 offset:25344\n"
"v_mfma_f32_16x16x32_bf16 v[36:39], v[176:179], a[212:215], v[36:39]\n"
"ds_read_b64_tr_b16 v[186:187], v200 offset:25600\n"
"v_mfma_f32_16x16x32_bf16 v[40:43], v[176:179], a[216:219], v[40:43]\n"
"ds_read_b64_tr_b16 v[188:189], v200 offset:25376\n"
"v_mfma_f32_16x16x32_bf16 v[44:47], v[176:179], a[220:223], v[44:47]\n"
"s_waitcnt lgkmcnt(3)\n"
"buffer_load_dwordx4 v210, s[48:51], s66 offen lds\n"
"v_mfma_f32_16x16x32_bf16 v[60:63], v[180:183], a[220:223], v[60:63]\n"
"s_add_u32 s66, s62, 0x240000\n"
"s_add_u32 m0, s59, 0x2100\n"
"ds_read_b64_tr_b16 v[190:191], v200 offset:25632\n"
"v_mfma_f32_16x16x32_bf16 v[56:59], v[180:183], a[216:219], v[56:59]\n"
"ds_read_b64_tr_b16 v[192:193], v200 offset:29568\n"
"v_mfma_f32_16x16x32_bf16 v[52:55], v[180:183], a[212:215], v[52:55]\n"
"ds_read_b64_tr_b16 v[194:195], v200 offset:29824\n"
"v_mfma_f32_16x16x32_bf16 v[48:51], v[180:183], a[208:211], v[48:51]\n"
"s_waitcnt lgkmcnt(4)\n"
"ds_read_b64_tr_b16 v[196:197], v200 offset:29600\n"
"v_mfma_f32_16x16x32_bf16 v[0:3], v[184:187], a[224:227], v[0:3]\n"
"buffer_load_dwordx4 v210, s[48:51], s66 offen lds\n"
"v_mfma_f32_16x16x32_bf16 v[4:7], v[184:187], a[228:231], v[4:7]\n"
"s_add_u32 s66, s62, 0x288000\n"
"s_add_u32 m0, s59, 0x3180\n"
"ds_read_b64_tr_b16 v[198:199], v200 offset:29856\n"
"v_mfma_f32_16x16x32_bf16 v[8:11], v[184:187], a[232:235], v[8:11]\n"
"ds_read_b64_tr_b16 v[176:177], v200 offset:33792\n"
"v_mfma_f32_16x16x32_bf16 v[12:15], v[184:187], a[236:239], v[12:15]\n"
"s_waitcnt lgkmcnt(4)\n"
"ds_read_b64_tr_b16 v[178:179], v200 offset:34048\n"
"v_mfma_f32_16x16x32_bf16 v[28:31], v[188:191], a[236:239], v[28:31]\n"
"ds_read_b64_tr_b16 v[180:181], v200 offset:33824\n"
"v_mfma_f32_16x16x32_bf16 v[24:27], v[188:191], a[232:235], v[24:27]\n"
"buffer_load_dwordx4 v210, s[48:51], s66 offen lds\n"
"v_mfma_f32_16x16x32_bf16 v[20:23], v[188:191], a[228:231], v[20:23]\n"
"s_add_u32 s66, s62, 0x2d0000\n"
"s_add_u32 m0, s59, 0x21000\n"
"ds_read_b64_tr_b16 v[182:183], v200 offset:34080\n"
"v_mfma_f32_16x16x32_bf16 v[16:19], v[188:191], a[224:227], v[16:19]\n"
"s_waitcnt lgkmcnt(4)\n"
"ds_read_b64_tr_b16 v[184:185], v200 offset:38016\n"
"v_mfma_f32_16x16x32_bf16 v[32:35], v[192:195], a[224:227], v[32:35]\n"
"ds_read_b64_tr_b16 v[186:187], v200 offset:38272\n"
"v_mfma_f32_16x16x32_bf16 v[36:39], v[192:195], a[228:231], v[36:39]\n"
"ds_read_b64_tr_b16 v[188:189], v200 offset:38048\n"
"v_mfma_f32_16x16x32_bf16 v[40:43], v[192:195], a[232:235], v[40:43]\n"
"buffer_load_dwordx4 v210, s[48:51], s66 offen lds\n"
"v_mfma_f32_16x16x32_bf16 v[44:47], v[192:195], a[236:239], v[44:47]\n"
"s_add_u32 s66, s62, 0x318000\n"
"s_add_u32 m0, s59, 0x22080\n"
"ds_read_b64_tr_b16 v[190:191], v200 offset:38304\n"
"v_mfma_f32_16x16x32_bf16 v[60:63], v[196:199], a[236:239], v[60:63]\n"
"ds_read_b64_tr_b16 v[192:193], v200 offset:50688\n"
"v_mfma_f32_16x16x32_bf16 v[56:59], v[196:199], a[232:235], v[56:59]\n"
"ds_read_b64_tr_b16 v[194:195], v200 offset:50944\n"
"v_mfma_f32_16x16x32_bf16 v[52:55], v[196:199], a[228:231], v[52:55]\n"
"v_mfma_f32_16x16x32_bf16 v[48:51], v[196:199], a[224:227], v[48:51]\n"
"s_waitcnt lgkmcnt(3)\n"
"buffer_load_dwordx4 v210, s[48:51], s66 offen lds\n"
"v_mfma_f32_16x16x32_bf16 v[0:3], v[176:179], a[240:243], v[0:3]\n"
"s_add_u32 s66, s62, 0x360000\n"
"s_add_u32 m0, s59, 0x4200\n"
"ds_read_b64_tr_b16 v[196:197], v200 offset:50720\n"
"v_mfma_f32_16x16x32_bf16 v[4:7], v[176:179], a[244:247], v[4:7]\n"
"ds_read_b64_tr_b16 v[198:199], v200 offset:50976\n"
"v_mfma_f32_16x16x32_bf16 v[8:11], v[176:179], a[248:251], v[8:11]\n"
"v_mfma_f32_16x16x32_bf16 v[12:15], v[176:179], a[252:255], v[12:15]\n"
"ds_read_b64_tr_b16 v[176:177], v200 offset:54912\n"
"v_mfma_f32_16x16x32_bf16 v[28:31], v[180:183], a[252:255], v[28:31]\n"
"ds_read_b64_tr_b16 v[178:179], v200 offset:55168\n"
"v_mfma_f32_16x16x32_bf16 v[24:27], v[180:183], a[248:251], v[24:27]\n"
"v_mfma_f32_16x16x32_bf16 v[20:23], v[180:183], a[244:247], v[20:23]\n"
"v_mfma_f32_16x16x32_bf16 v[16:19], v[180:183], a[240:243], v[16:19]\n"
"ds_read_b64_tr_b16 v[180:181], v200 offset:54944\n"
"v_mfma_f32_16x16x32_bf16 v[32:35], v[184:187], a[240:243], v[32:35]\n"
"ds_read_b64_tr_b16 v[182:183], v200 offset:55200\n"
"v_mfma_f32_16x16x32_bf16 v[36:39], v[184:187], a[244:247], v[36:39]\n"
"v_mfma_f32_16x16x32_bf16 v[40:43], v[184:187], a[248:251], v[40:43]\n"
"s_waitcnt vmcnt(7)\n"
"v_subrev_u32 v238, s18, v230\n"
"v_xor_b32 v239, v230, v231\n"
"v_xor_b32 v239, 0x5bd1e995, v239\n"
"v_max_u32 v238, v238, v239\n"
"v_cmp_ge_u32 s[10:11], v228, v238\n"
"v_mfma_f32_16x16x32_bf16 v[44:47], v[184:187], a[252:255], v[44:47]\n"
"s_waitcnt lgkmcnt(2)\n"
"s_andn2_b32 s10, 0x30000, s10\n s_cbranch_scc1 L_slow0_%=\n L_back0_%=:\n"
"v_mfma_f32_16x16x32_bf16 v[60:63], v[188:191], a[252:255], v[60:63]\n"
"v_mfma_f32_16x16x32_bf16 v[56:59], v[188:191], a[248:251], v[56:59]\n"
"v_mfma_f32_16x16x32_bf16 v[52:55], v[188:191], a[244:247], v[52:55]\n"
"v_mfma_f32_16x16x32_bf16 v[48:51], v[188:191], a[240:243], v[48:51]\n"
"v_mfma_f32_16x16x32_bf16 v[0:3], v[192:195], v[144:147], v[0:3]\n"
"v_mfma_f32_16x16x32_bf16 v[4:7], v[192:195], v[148:151], v[4:7]\n"
"v_mfma_f32_16x16x32_bf16 v[8:11], v[192:195], v[152:155], v[8:11]\n"
"v_mfma_f32_16x16x32_bf16 v[12:15], v[192:195], v[156:159], v[12:15]\n"
"v_mfma_f32_16x16x32_bf16 v[28:31], v[196:199], v[156:159], v[28:31]\n"
"v_mfma_f32_16x16x32_bf16 v[24:27], v[196:199], v[152:155], v[24:27]\n"
"v_mfma_f32_16x16x32_bf16 v[20:23], v[196:199], v[148:151], v[20:23]\n"
"s_barrier\n"
"v_mfma_f32_16x16x32_bf16 v[16:19], v[196:199], v[144:147], v[16:19]\n"
"buffer_load_dwordx4 v210, s[48:51], s66 offen lds\n"
"v_mfma_f32_16x16x32_bf16 v[32:35], v[176:179], v[144:147], v[32:35]\n"
"s_add_u32 s66, s62, 0x3a8000\n"
"s_add_u32 m0, s59, 0x5280\n"
"ds_read_b64_tr_b16 v[184:185], v200 offset:42240\n"
"v_mfma_f32_16x16x32_bf16 v[36:39], v[176:179], v[148:151], v[36:39]\n"
"ds_read_b64_tr_b16 v[186:187], v200 offset:42496\n"
"v_mfma_f32_16x16x32_bf16 v[40:43], v[176:179], v[152:155], v[40:43]\n"
"ds_read_b64_tr_b16 v[188:189], v200 offset:42272\n"
"v_mfma_f32_16x16x32_bf16 v[44:47], v[176:179], v[156:159], v[44:47]\n"
"s_waitcnt vmcnt(7) lgkmcnt(3)\n"
"buffer_store_dwordx2 v[236:237], v142, s[44:47], s17 offen offset:256 sc0 sc1\n"
"v_mfma_f32_16x16x32_bf16 v[60:63], v[180:183], v[156:159], v[60:63]\n"
"ds_read_b64_tr_b16 v[190:191], v200 offset:42528\n"
"v_mfma_f32_16x16x32_bf16 v[56:59], v[180:183], v[152:155], v[56:59]\n"
"buffer_load_dwordx4 v210, s[48:51], s66 offen lds\n"
"v_mfma_f32_16x16x32_bf16 v[52:55], v[180:183], v[148:151], v[52:55]\n"
"s_add_u32 s66, s62, 0x3f0000\n"
"s_add_u32 m0, s59, 0x6300\n"
"ds_read_b64_tr_b16 v[192:193], v200 offset:46464\n"
"v_mfma_f32_16x16x32_bf16 v[48:51], v[180:183], v[144:147], v[48:51]\n"
"s_waitcnt lgkmcnt(3)\n"
"ds_read_b64_tr_b16 v[194:195], v200 offset:46720\n"
"v_mfma_f32_16x16x32_bf16 v[0:3], v[184:187], v[160:163], v[0:3]\n"
"ds_read_b64_tr_b16 v[196:197], v200 offset:46496\n"
"v_mfma_f32_16x16x32_bf16 v[4:7], v[184:187], v[164:167], v[4:7]\n"
"ds_read_b64_tr_b16 v[198:199], v200 offset:46752\n"
"v_subrev_u32 v200, s67, v200\n"
"v_mfma_f32_16x16x32_bf16 v[8:11], v[184:187], v[168:171], v[8:11]\n"
"s_xor_b32 s67, s9, 0x2000\n"
"s_add_u32 s95, s69, s67\n"
"buffer_load_dwordx4 v210, s[48:51], s66 offen lds\n"
"v_mfma_f32_16x16x32_bf16 v[12:15], v[184:187], v[172:175], v[12:15]\n"
"L_st8_%=:\n s_cbranch_execz L_st12_%=\n"
"s_waitcnt lgkmcnt(4)\n"
"s_add_u32 s66, s62, 0x438000\n"
"ds_read_b64_tr_b16 v[176:177], v219 offset:59136\n"
"v_mul_f32 v240, v220, v0\n"
"v_exp_f32 v240, v240\n"
"v_mul_f32 v241, v220, v1\n"
"v_exp_f32 v241, v241\n"
"v_mfma_f32_16x16x32_bf16 v[28:31], v[188:191], v[172:175], v[28:31]\n"
"s_add_u32 m0, s59, 0x7380\n"
"ds_read_b64_tr_b16 v[178:179], v219 offset:59392\n"
"v_fma_f32 v240, v240, v224, v224\n"
"v_mfma_f32_16x16x32_bf16 v[24:27], v[188:191], v[168:171], v[24:27]\n"
"v_rcp_f32 v240, v240\n"
"v_fma_f32 v241, v241, v224, v224\n"
"v_mfma_f32_16x16x32_bf16 v[20:23], v[188:191], v[164:167], v[20:23]\n"
"v_rcp_f32 v241, v241\n"
"v_mul_f32 v242, v220, v2\n"
"v_mfma_f32_16x16x32_bf16 v[16:19], v[188:191], v[160:163], v[16:19]\n"
"s_waitcnt lgkmcnt(2)\n"
"buffer_load_dwordx4 v210, s[48:51], s66 offen lds\n"
"v_exp_f32 v242, v242\n"
"v_mfma_f32_16x16x32_bf16 v[32:35], v[192:195], v[160:163], v[32:35]\n"
"s_add_u32 s66, s62, 0x480000\n"
"s_add_u32 m0, s59, 0x8400\n"
"ds_read_b64_tr_b16 v[180:181], v219 offset:61248\n"
"v_fma_f32 v242, v242, v224, v224\n"
"v_rcp_f32 v242, v242\n"
"v_mfma_f32_16x16x32_bf16 v[36:39], v[192:195], v[164:167], v[36:39]\n"
"s_add_u32 s84, s95, 0x800000\n"
"ds_read_b64_tr_b16 v[182:183], v219 offset:61504\n"
"v_add_u32 v219, 0xe700, v219\n"
"v_mfma_f32_16x16x32_bf16 v[40:43], v[192:195], v[168:171], v[40:43]\n"
"ds_read_b64_tr_b16 v[184:185], v219 offset:4224\n"
"v_mul_f32 v32, v0, v32\n"
"v_mul_f32 v32, v32, v240\n"
"v_mfma_f32_16x16x32_bf16 v[44:47], v[192:195], v[172:175], v[44:47]\n"
"ds_read_b64_tr_b16 v[186:187], v219 offset:4480\n"
"v_mul_f32 v33, v1, v33\n"
"v_mul_f32 v33, v33, v241\n"
"v_mfma_f32_16x16x32_bf16 v[60:63], v[196:199], v[172:175], v[60:63]\n"
"buffer_load_dwordx4 v210, s[48:51], s66 offen lds\n"
"v_cvt_pk_bf16_f32 v248, v32, v33\n"
"v_mul_f32 v34, v2, v34\n"
"v_mfma_f32_16x16x32_bf16 v[56:59], v[196:199], v[168:171], v[56:59]\n"
"s_add_u32 s66, s62, 0x4c8000\n"
"s_add_u32 m0, s59, 0x9480\n"
"ds_read_b64_tr_b16 v[188:189], v219 offset:6336\n"
"v_mul_f32 v34, v34, v242\n"
"v_mul_f32 v243, v220, v3\n"
"v_mfma_f32_16x16x32_bf16 v[52:55], v[196:199], v[164:167], v[52:55]\n"
"v_exp_f32 v243, v243\n"
"v_mul_f32 v35, v3, v35\n"
"v_mfma_f32_16x16x32_bf16 v[48:51], v[196:199], v[160:163], v[48:51]\n"
"s_waitcnt lgkmcnt(3)\n"
"ds_read_b64_tr_b16 v[190:191], v219 offset:6592\n"
"v_fma_f32 v243, v243, v224, v224\n"
"v_rcp_f32 v243, v243\n"
"v_mfma_f32_16x16x32_bf16 v[64:67], v[176:179], a[0:3], 0\n"
"s_mul_i32 s62, s53, 0xc00\n"
"ds_read_b64_tr_b16 v[192:193], v219 offset:42240\n"
"v_mul_f32 v35, v35, v243\n"
"v_mfma_f32_16x16x32_bf16 v[68:71], v[176:179], a[4:7], 0\n"
"buffer_load_dwordx4 v210, s[48:51], s66 offen lds\n"
"v_cvt_pk_bf16_f32 v249, v34, v35\n"
"v_mul_f32 v244, v220, v16\n"
"v_mfma_f32_16x16x32_bf16 v[72:75], v[176:179], a[8:11], 0\n"
"s_add_u32 s66, s63, 0\n"
"s_add_u32 m0, s59, 0xa500\n"
"v_exp_f32 v244, v244\n"
"v_mul_f32 v48, v16, v48\n"
"v_mfma_f32_16x16x32_bf16 v[76:79], v[176:179], a[12:15], 0\n"
"ds_read_b64_tr_b16 v[194:195], v219 offset:42496\n"
"v_fma_f32 v244, v244, v224, v224\n"
"v_rcp_f32 v244, v244\n"
"v_mfma_f32_16x16x32_bf16 v[92:95], v[180:183], a[12:15], 0\n"
"s_mul_i32 s69, s68, 0x80\n"
"ds_read_b64_tr_b16 v[196:197], v219 offset:44352\n"
"v_mul_f32 v48, v48, v244\n"
"v_mfma_f32_16x16x32_bf16 v[88:91], v[180:183], a[8:11], 0\n"
"ds_read_b64_tr_b16 v[198:199], v219 offset:44608\n"
"v_mul_f32 v245, v220, v17\n"
"v_exp_f32 v245, v245\n"
"v_mfma_f32_16x16x32_bf16 v[84:87], v[180:183], a[4:7], 0\n"
"s_add_u32 s69, s69, s19\n"
"ds_read_b64_tr_b16 v[176:177], v219 offset:46464\n"
"v_fma_f32 v245, v245, v224, v224\n"
"v_mfma_f32_16x16x32_bf16 v[80:83], v[180:183], a[0:3], 0\n"
"s_waitcnt lgkmcnt(4)\n"
"buffer_load_dwordx4 a[0:3], v139, s[44:47], s94 offen offset:0 sc0 sc1\n"
"v_rcp_f32 v245, v245\n"
"v_mfma_f32_16x16x32_bf16 v[64:67], v[184:187], a[16:19], v[64:67]\n"
"ds_read_b64_tr_b16 v[178:179], v219 offset:46720\n"
"v_mul_f32 v49, v17, v49\n"
"v_mul_f32 v49, v49, v245\n"
"v_mfma_f32_16x16x32_bf16 v[68:71], v[184:187], a[20:23], v[68:71]\n"
"ds_read_b64_tr_b16 v[180:181], v219 offset:48576\n"
"v_cvt_pk_bf16_f32 v250, v48, v49\n"
"v_mul_f32 v246, v220, v18\n"
"v_mfma_f32_16x16x32_bf16 v[72:75], v[184:187], a[24:27], v[72:75]\n"
"v_exp_f32 v246, v246\n"
"v_mul_f32 v50, v18, v50\n"
"v_mfma_f32_16x16x32_bf16 v[76:79], v[184:187], a[28:31], v[76:79]\n"
"ds_read_b64_tr_b16 v[182:183], v219 offset:48832\n"
"v_add_u32 v219, 0xe700, v219\n"
"v_fma_f32 v246, v246, v224, v224\n"
"v_mfma_f32_16x16x32_bf16 v[92:95], v[188:191], a[28:31], v[92:95]\n"
"v_rcp_f32 v246, v246\n"
"v_mul_f32 v247, v220, v19\n"
"v_mfma_f32_16x16x32_bf16 v[88:91], v[188:191], a[24:27], v[88:91]\n"
"v_exp_f32 v247, v247\n"
"v_mul_f32 v50, v50, v246\n"
"v_mfma_f32_16x16x32_bf16 v[84:87], v[188:191], a[20:23], v[84:87]\n"
"ds_read_b128 a[20:23], v208 offset:25344\n"
"v_fma_f32 v247, v247, v224, v224\n"
"v_rcp_f32 v247, v247\n"
"v_mfma_f32_16x16x32_bf16 v[80:83], v[188:191], a[16:19], v[80:83]\n"
"s_waitcnt lgkmcnt(2)\n"
"ds_read_b128 a[16:19], v214 offset:25344\n"
"v_mul_f32 v51, v19, v51\n"
"v_mfma_f32_16x16x32_bf16 v[64:67], v[192:195], a[32:35], v[64:67]\n"
"v_mul_f32 v51, v51, v247\n"
"v_cvt_pk_bf16_f32 v251, v50, v51\n"
"v_mul_f32 v240, v221, v4\n"
"v_mfma_f32_16x16x32_bf16 v[68:71], v[192:195], a[36:39], v[68:71]\n"
"buffer_store_dwordx4 v[248:251], v139, s[44:47], s52 offen offset:0 sc0 sc1\n"
"v_exp_f32 v240, v240\n"
"v_mfma_f32_16x16x32_bf16 v[72:75], v[192:195], a[40:43], v[72:75]\n"
"v_fma_f32 v240, v240, v225, v225\n"
"v_rcp_f32 v240, v240\n"
"v_mfma_f32_16x16x32_bf16 v[76:79], v[192:195], a[44:47], v[76:79]\n"
"v_mul_f32 v36, v4, v36\n"
"v_mul_f32 v36, v36, v240\n"
"v_mul_f32 v241, v221, v5\n"
"v_mfma_f32_16x16x32_bf16 v[92:95], v[196:199], a[44:47], v[92:95]\n"
"v_exp_f32 v241, v241\n"
"v_mul_f32 v37, v5, v37\n"
"v_mfma_f32_16x16x32_bf16 v[88:91], v[196:199], a[40:43], v[88:91]\n"
"s_waitcnt vmcnt(13)\n"
"v_fma_f32 v241, v241, v225, v225\n"
"v_rcp_f32 v241, v241\n"
"v_mfma_f32_16x16x32_bf16 v[84:87], v[196:199], a[36:39], v[84:87]\n"
"s_barrier\n"
"v_mul_f32 v37, v37, v241\n"
"v_cvt_pk_bf16_f32 v252, v36, v37\n"
"v_mul_f32 v242, v221, v6\n"
"v_mfma_f32_16x16x32_bf16 v[80:83], v[196:199], a[32:35], v[80:83]\n"
"buffer_load_dwordx4 v211, s[24:27], s66 offen lds\n"
"v_exp_f32 v242, v242\n"
"v_mfma_f32_16x16x32_bf16 v[64:67], v[176:179], a[48:51], v[64:67]\n"
"s_add_u32 s66, s63, 0x9000\n"
"s_add_u32 m0, s59, 0xb580\n"
"ds_write_b128 v205, v[248:251] offset:50688\n"
"v_fma_f32 v242, v242, v225, v225\n"
"v_rcp_f32 v242, v242\n"
"v_mfma_f32_16x16x32_bf16 v[68:71], v[176:179], a[52:55], v[68:71]\n"
"s_add_u32 s94, s13, 0\n"
"ds_read_b64_tr_b16 v[184:185], v219 offset:8448\n"
"v_mul_f32 v38, v6, v38\n"
"v_mfma_f32_16x16x32_bf16 v[72:75], v[176:179], a[56:59], v[72:75]\n"
"ds_read_b64_tr_b16 v[186:187], v219 offset:8704\n"
"v_mul_f32 v38, v38, v242\n"
"v_mul_f32 v243, v221, v7\n"
"v_mfma_f32_16x16x32_bf16 v[76:79], v[176:179], a[60:63], v[76:79]\n"
"s_waitcnt lgkmcnt(3)\n"
"buffer_load_dwordx4 v211, s[24:27], s66 offen lds\n"
"v_exp_f32 v243, v243\n"
"v_mfma_f32_16x16x32_bf16 v[92:95], v[180:183], a[60:63], v[92:95]\n"
"s_add_u32 s66, s65, s74\n"
"s_add_u32 m0, s59, 0xc600\n"
"ds_read_b64_tr_b16 v[188:189], v219 offset:10560\n"
"v_fma_f32 v243, v243, v225, v225\n"
"v_rcp_f32 v243, v243\n"
"v_mfma_f32_16x16x32_bf16 v[88:91], v[180:183], a[56:59], v[88:91]\n"
"s_add_u32 s52, s70, 0xe00000\n"
"ds_read_b64_tr_b16 v[190:191], v219 offset:10816\n"
"v_add_u32 v214, 0xe700, v214\n"
"v_mfma_f32_16x16x32_bf16 v[84:87], v[180:183], a[52:55], v[84:87]\n"
"ds_read_b64_tr_b16 v[192:193], v219 offset:12672\n"
"v_add_u32 v208, 0xe700, v208\n"
"v_mul_f32 v39, v7, v39\n"
"v_mfma_f32_16x16x32_bf16 v[80:83], v[180:183], a[48:51], v[80:83]\n"
"s_waitcnt lgkmcnt(3)\n"
"ds_read_b64_tr_b16 v[194:195], v219 offset:12928\n"
"v_mul_f32 v39, v39, v243\n"
"v_cvt_pk_bf16_f32 v253, v38, v39\n"
"v_mfma_f32_16x16x32_bf16 v[64:67], v[184:187], a[64:67], v[64:67]\n"
"buffer_load_dwordx4 v212, s[40:43], s66 offen lds\n"
"v_mul_f32 v244, v221, v20\n"
"v_exp_f32 v244, v244\n"
"v_mfma_f32_16x16x32_bf16 v[68:71], v[184:187], a[68:71], v[68:71]\n"
"s_add_u32 s66, s63, 0x12000\n"
"s_add_u32 m0, s59, 0xd680\n"
"ds_read_b64_tr_b16 v[196:197], v219 offset:14784\n"
"v_fma_f32 v244, v244, v225, v225\n"
"v_mfma_f32_16x16x32_bf16 v[72:75], v[184:187], a[72:75], v[72:75]\n"
"v_rcp_f32 v244, v244\n"
"v_mul_f32 v52, v20, v52\n"
"v_mfma_f32_16x16x32_bf16 v[76:79], v[184:187], a[76:79], v[76:79]\n"
"s_waitcnt lgkmcnt(3)\n"
"ds_read_b64_tr_b16 v[198:199], v219 offset:15040\n"
"v_add_u32 v219, 0xfffe3200, v219\n"
"v_mul_f32 v52, v52, v244\n"
"v_mfma_f32_16x16x32_bf16 v[92:95], v[188:191], a[76:79], v[92:95]\n"
"s_xor_b32 s67, s9, 0x2000\n"
"ds_read_b64_tr_b16 v[176:177], v219 offset:0\n"
"v_mul_f32 v245, v221, v21\n"
"v_mfma_f32_16x16x32_bf16 v[88:91], v[188:191], a[72:75], v[88:91]\n"
"buffer_load_dwordx4 v211, s[24:27], s66 offen lds\n"
"v_exp_f32 v245, v245\n"
"v_mfma_f32_16x16x32_bf16 v[84:87], v[188:191], a[68:71], v[84:87]\n"
"s_add_u32 s66, s63, 0x1b000\n"
"s_add_u32 m0, s59, 0xe700\n"
"ds_read_b64_tr_b16 v[178:179], v219 offset:256\n"
"v_fma_f32 v245, v245, v225, v225\n"
"v_rcp_f32 v245, v245\n"
"v_mfma_f32_16x16x32_bf16 v[80:83], v[188:191], a[64:67], v[80:83]\n"
"s_waitcnt lgkmcnt(3)\n"
"ds_read_b64_tr_b16 v[180:181], v219 offset:2112\n"
"v_mul_f32 v53, v21, v53\n"
"v_mfma_f32_16x16x32_bf16 v[64:67], v[192:195], a[80:83], v[64:67]\n"
"ds_read_b64_tr_b16 v[182:183], v219 offset:2368\n"
"v_mul_f32 v53, v53, v245\n"
"v_cvt_pk_bf16_f32 v254, v52, v53\n"
"v_mfma_f32_16x16x32_bf16 v[68:71], v[192:195], a[84:87], v[68:71]\n"
"ds_read_b64_tr_b16 v[184:185], v219 offset:4224\n"
"v_mul_f32 v246, v221, v22\n"
"v_exp_f32 v246, v246\n"
"v_mfma_f32_16x16x32_bf16 v[72:75], v[192:195], a[88:91], v[72:75]\n"
"s_mul_i32 s68, s58, 0x280\n"
"buffer_load_dwordx4 v211, s[24:27], s66 offen lds\n"
"v_fma_f32 v246, v246, v225, v225\n"
"v_mfma_f32_16x16x32_bf16 v[76:79], v[192:195], a[92:95], v[76:79]\n"
"s_waitcnt lgkmcnt(4)\n"
"s_add_u32 s66, s63, 0x24000\n"
"v_rcp_f32 v246, v246\n"
"v_mul_f32 v54, v22, v54\n"
"v_mfma_f32_16x16x32_bf16 v[92:95], v[196:199], a[92:95], v[92:95]\n"
"s_add_u32 m0, s59, 0xf780\n"
"ds_read_b64_tr_b16 v[186:187], v219 offset:4480\n"
"v_mul_f32 v54, v54, v246\n"
"v_mul_f32 v247, v221, v23\n"
"v_mfma_f32_16x16x32_bf16 v[88:91], v[196:199], a[88:91], v[88:91]\n"
"v_exp_f32 v247, v247\n"
"v_mul_f32 v55, v23, v55\n"
"v_mfma_f32_16x16x32_bf16 v[84:87], v[196:199], a[84:87], v[84:87]\n"
"ds_read_b64_tr_b16 v[188:189], v219 offset:6336\n"
"v_fma_f32 v247, v247, v225, v225\n"
"v_rcp_f32 v247, v247\n"
"v_mfma_f32_16x16x32_bf16 v[80:83], v[196:199], a[80:83], v[80:83]\n"
"s_waitcnt lgkmcnt(2)\n"
"buffer_load_dwordx4 v211, s[24:27], s66 offen lds\n"
"v_mul_f32 v55, v55, v247\n"
"v_mfma_f32_16x16x32_bf16 v[64:67], v[176:179], a[96:99], v[64:67]\n"
"s_add_u32 s66, s63, 0x2d000\n"
"s_add_u32 m0, s59, 0\n"
"ds_read_b64_tr_b16 v[190:191], v219 offset:6592\n"
"v_cvt_pk_bf16_f32 v255, v54, v55\n"
"v_mul_f32 v240, v222, v8\n"
"v_mfma_f32_16x16x32_bf16 v[68:71], v[176:179], a[100:103], v[68:71]\n"
"ds_write_b128 v205, v[252:255] offset:51776\n"
"v_exp_f32 v240, v240\n"
"v_mfma_f32_16x16x32_bf16 v[72:75], v[176:179], a[104:107], v[72:75]\n"
"ds_read_b128 a[24:27], v214 offset:25344\n"
"v_fma_f32 v240, v240, v226, v226\n"
"v_rcp_f32 v240, v240\n"
"v_mfma_f32_16x16x32_bf16 v[76:79], v[176:179], a[108:111], v[76:79]\n"
"s_sub_u32 s68, s76, s68\n"
"ds_read_b128 a[28:31], v208 offset:25344\n"
"v_mul_f32 v40, v8, v40\n"
"v_mfma_f32_16x16x32_bf16 v[92:95], v[180:183], a[108:111], v[92:95]\n"
"buffer_store_dwordx4 v[252:255], v139, s[44:47], s52 offen offset:0 sc0 sc1\n"
"v_mul_f32 v40, v40, v240\n"
"v_mul_f32 v241, v222, v9\n"
"v_mfma_f32_16x16x32_bf16 v[88:91], v[180:183], a[104:107], v[88:91]\n"
"s_waitcnt vmcnt(16)\n"
"v_exp_f32 v241, v241\n"
"v_mul_f32 v41, v9, v41\n"
"v_mfma_f32_16x16x32_bf16 v[84:87], v[180:183], a[100:103], v[84:87]\n"
"s_barrier\n"
"ds_read_b128 a[32:35], v214 offset:33792\n"
"v_add_u32 v214, 0xffff1900, v214\n"
"v_fma_f32 v241, v241, v226, v226\n"
"v_mfma_f32_16x16x32_bf16 v[80:83], v[180:183], a[96:99], v[80:83]\n"
"s_waitcnt lgkmcnt(3)\n"
"v_rcp_f32 v241, v241\n"
"v_mul_f32 v242, v222, v10\n"
"v_mfma_f32_16x16x32_bf16 v[64:67], v[184:187], a[112:115], v[64:67]\n"
"buffer_load_dwordx4 v211, s[24:27], s66 offen lds\n"
"v_mul_f32 v41, v41, v241\n"
"v_cvt_pk_bf16_f32 v248, v40, v41\n"
"v_mfma_f32_16x16x32_bf16 v[68:71], v[184:187], a[116:119], v[68:71]\n"
"s_add_u32 s66, s63, 0\n"
"s_add_u32 m0, s59, 0x14a00\n"
"v_exp_f32 v242, v242\n"
"v_mul_f32 v42, v10, v42\n"
"v_mfma_f32_16x16x32_bf16 v[72:75], v[184:187], a[120:123], v[72:75]\n"
"ds_read_b64_tr_b16 v[192:193], v219 offset:8448\n"
"v_mul_f32 v243, v222, v11\n"
"v_mul_f32 v244, v222, v24\n"
"v_mfma_f32_16x16x32_bf16 v[76:79], v[184:187], a[124:127], v[76:79]\n"
"ds_read_b64_tr_b16 v[194:195], v219 offset:8704\n"
"v_mul_f32 v245, v222, v25\n"
"v_mul_f32 v246, v222, v26\n"
"v_mfma_f32_16x16x32_bf16 v[92:95], v[188:191], a[124:127], v[92:95]\n"
"v_exp_f32 v243, v243\n"
"v_mul_f32 v247, v222, v27\n"
"v_mfma_f32_16x16x32_bf16 v[88:91], v[188:191], a[120:123], v[88:91]\n"
"buffer_load_dwordx4 a[4:7], v139, s[44:47], s94 offen offset:0 sc0 sc1\n"
"v_exp_f32 v244, v244\n"
"v_mfma_f32_16x16x32_bf16 v[84:87], v[188:191], a[116:119], v[84:87]\n"
"ds_read_b64_tr_b16 v[196:197], v219 offset:10560\n"
"v_exp_f32 v245, v245\n"
"v_mfma_f32_16x16x32_bf16 v[80:83], v[188:191], a[112:115], v[80:83]\n"
"s_waitcnt lgkmcnt(1)\n"
"ds_read_b64_tr_b16 v[198:199], v219 offset:10816\n"
"v_exp_f32 v246, v246\n"
"v_mfma_f32_16x16x32_bf16 v[64:67], v[192:195], a[128:131], v[64:67]\n"
"s_add_u32 s94, s12, 0x1000\n"
"v_exp_f32 v247, v247\n"
"v_mfma_f32_16x16x32_bf16 v[68:71], v[192:195], a[132:135], v[68:71]\n"
"ds_read_b64_tr_b16 v[176:177], v219 offset:12672\n"
"v_fma_f32 v242, v242, v226, v226\n"
"v_fma_f32 v243, v243, v226, v226\n"
"v_mfma_f32_16x16x32_bf16 v[72:75], v[192:195], a[136:139], v[72:75]\n"
"buffer_load_dwordx4 v211, s[28:31], s66 offen lds\n"
"v_fma_f32 v244, v244, v226, v226\n"
"v_fma_f32 v245, v245, v226, v226\n"
"v_mfma_f32_16x16x32_bf16 v[76:79], v[192:195], a[140:143], v[76:79]\n"
"s_waitcnt lgkmcnt(1)\n"
"s_add_u32 s66, s63, 0x9000\n"
"ds_read_b64_tr_b16 v[178:179], v219 offset:12928\n"
"v_fma_f32 v246, v246, v226, v226\n"
"v_fma_f32 v247, v247, v226, v226\n"
"v_mfma_f32_16x16x32_bf16 v[92:95], v[196:199], a[140:143], v[92:95]\n"
"s_add_u32 m0, s59, 0x15a80\n"
"v_rcp_f32 v242, v242\n"
"v_rcp_f32 v243, v243\n"
"v_mfma_f32_16x16x32_bf16 v[88:91], v[196:199], a[136:139], v[88:91]\n"
"s_add_u32 s52, s71, 0xc00000\n"
"ds_read_b64_tr_b16 v[180:181], v219 offset:14784\n"
"v_mul_f32 v43, v11, v43\n"
"v_mfma_f32_16x16x32_bf16 v[84:87], v[196:199], a[132:135], v[84:87]\n"
"ds_read_b64_tr_b16 v[182:183], v219 offset:15040\n"
"v_add_u32 v219, 0x1ce00, v219\n"
"v_rcp_f32 v244, v244\n"
"v_mfma_f32_16x16x32_bf16 v[80:83], v[196:199], a[128:131], v[80:83]\n"
"s_waitcnt lgkmcnt(2)\n"
"ds_read_b64_tr_b16 v[184:185], v219 offset:16896\n"
"v_mul_f32 v56, v24, v56\n"
"v_mfma_f32_16x16x32_bf16 v[64:67], v[176:179], a[144:147], v[64:67]\n"
"ds_read_b64_tr_b16 v[186:187], v219 offset:17152\n"
"buffer_load_dwordx4 a[8:11], v139, s[44:47], s94 offen offset:0 sc0 sc1\n"
"v_rcp_f32 v245, v245\n"
"v_mfma_f32_16x16x32_bf16 v[68:71], v[176:179], a[148:151], v[68:71]\n"
"s_add_u32 s94, s13, 0x1000\n"
"ds_read_b64_tr_b16 v[188:189], v219 offset:19008\n"
"v_mul_f32 v57, v25, v57\n"
"v_mfma_f32_16x16x32_bf16 v[72:75], v[176:179], a[152:155], v[72:75]\n"
"ds_read_b64_tr_b16 v[190:191], v219 offset:19264\n"
"v_rcp_f32 v246, v246\n"
"v_mfma_f32_16x16x32_bf16 v[76:79], v[176:179], a[156:159], v[76:79]\n"
"s_waitcnt lgkmcnt(4)\n"
"ds_read_b128 a[36:39], v208 offset:33792\n"
"v_add_u32 v208, 0xffff1900, v208\n"
"v_mul_f32 v58, v26, v58\n"
"v_mfma_f32_16x16x32_bf16 v[92:95], v[180:183], a[156:159], v[92:95]\n"
"ds_read_b128 a[40:43], v214 offset:8448\n"
"v_rcp_f32 v247, v247\n"
"v_mfma_f32_16x16x32_bf16 v[88:91], v[180:183], a[152:155], v[88:91]\n"
"ds_read_b128 a[44:47], v208 offset:8448\n"
"buffer_load_dwordx4 a[12:15], v139, s[44:47], s94 offen offset:0 sc0 sc1\n"
"v_mul_f32 v59, v27, v59\n"
"v_mfma_f32_16x16x32_bf16 v[84:87], v[180:183], a[148:151], v[84:87]\n"
"ds_read_b128 a[48:51], v214 offset:16896\n"
"v_mul_f32 v42, v42, v242\n"
"v_mul_f32 v43, v43, v243\n"
"v_mfma_f32_16x16x32_bf16 v[80:83], v[180:183], a[144:147], v[80:83]\n"
"s_waitcnt lgkmcnt(5)\n"
"ds_read_b128 a[52:55], v208 offset:16896\n"
"v_mul_f32 v56, v56, v244\n"
"v_mfma_f32_16x16x32_bf16 v[64:67], v[184:187], a[160:163], v[64:67]\n"
"ds_read_b128 a[80:83], v214 offset:33792\n"
"v_mul_f32 v57, v57, v245\n"
"v_mul_f32 v58, v58, v246\n"
"v_mfma_f32_16x16x32_bf16 v[68:71], v[184:187], a[164:167], v[68:71]\n"
"ds_read_b128 a[84:87], v208 offset:33792\n"
"v_mul_f32 v59, v59, v247\n"
"v_mul_f32 v240, v223, v12\n"
"v_mfma_f32_16x16x32_bf16 v[72:75], v[184:187], a[168:171], v[72:75]\n"
"ds_read_b64_tr_b16 v[192:193], v219 offset:21120\n"
"buffer_load_dwordx4 v211, s[28:31], s66 offen lds\n"
"v_mul_f32 v241, v223, v13\n"
"v_mfma_f32_16x16x32_bf16 v[76:79], v[184:187], a[172:175], v[76:79]\n"
"s_waitcnt lgkmcnt(5)\n"
"s_add_u32 s66, s65, s75\n"
"ds_read_b64_tr_b16 v[194:195], v219 offset:21376\n"
"v_mul_f32 v242, v223, v14\n"
"v_mul_f32 v243, v223, v15\n"
"v_mfma_f32_16x16x32_bf16 v[92:95], v[188:191], a[172:175], v[92:95]\n"
"s_add_u32 m0, s59, 0x1ef00\n"
"v_exp_f32 v240, v240\n"
"v_mul_f32 v244, v223, v28\n"
"v_mfma_f32_16x16x32_bf16 v[88:91], v[188:191], a[168:171], v[88:91]\n"
"s_waitcnt vmcnt(17) lgkmcnt(2)\n"
"v_exp_f32 v241, v241\n"
"v_mul_f32 v245, v223, v29\n"
"v_mfma_f32_16x16x32_bf16 v[84:87], v[188:191], a[164:167], v[84:87]\n"
"s_barrier\n"
"ds_read_b64_tr_b16 v[196:197], v219 offset:23232\n"
"v_mul_f32 v246, v223, v30\n"
"v_mul_f32 v247, v223, v31\n"
"v_mfma_f32_16x16x32_bf16 v[80:83], v[188:191], a[160:163], v[80:83]\n"
"s_waitcnt lgkmcnt(1)\n"
"ds_read_b64_tr_b16 v[198:199], v219 offset:23488\n"
"buffer_load_dwordx4 v212, s[40:43], s66 offen lds\n"
"v_add_u32 v219, 0xfffe3200, v219\n"
"v_mfma_f32_16x16x32_bf16 v[64:67], v[192:195], a[176:179], v[64:67]\n"
"s_add_u32 s66, s63, 0x12000\n"
"s_add_u32 m0, s59, 0x1ff80\n"
"v_exp_f32 v242, v242\n"
"v_exp_f32 v243, v243\n"
"v_mfma_f32_16x16x32_bf16 v[68:71], v[192:195], a[180:183], v[68:71]\n"
"s_add_u32 s94, s85, 0\n"
"v_exp_f32 v244, v244\n"
"v_mfma_f32_16x16x32_bf16 v[72:75], v[192:195], a[184:187], v[72:75]\n"
"ds_read_b64_tr_b16 v[176:177], v219 offset:16896\n"
"v_exp_f32 v245, v245\n"
"v_mfma_f32_16x16x32_bf16 v[76:79], v[192:195], a[188:191], v[76:79]\n"
"s_waitcnt lgkmcnt(1)\n"
"ds_read_b64_tr_b16 v[178:179], v219 offset:17152\n"
"v_exp_f32 v246, v246\n"
"v_mfma_f32_16x16x32_bf16 v[92:95], v[196:199], a[188:191], v[92:95]\n"
"buffer_load_dwordx4 v211, s[28:31], s66 offen lds\n"
"v_exp_f32 v247, v247\n"
"v_mfma_f32_16x16x32_bf16 v[88:91], v[196:199], a[184:187], v[88:91]\n"
"s_add_u32 s66, s63, 0x1b000\n"
"s_add_u32 m0, s59, 0x1080\n"
"ds_read_b64_tr_b16 v[180:181], v219 offset:19008\n"
"v_fma_f32 v240, v240, v227, v227\n"
"v_mfma_f32_16x16x32_bf16 v[84:87], v[196:199], a[180:183], v[84:87]\n"
"v_rcp_f32 v240, v240\n"
"v_fma_f32 v241, v241, v227, v227\n"
"v_mfma_f32_16x16x32_bf16 v[80:83], v[196:199], a[176:179], v[80:83]\n"
"s_waitcnt lgkmcnt(1)\n"
"ds_read_b64_tr_b16 v[182:183], v219 offset:19264\n"
"v_fma_f32 v242, v242, v227, v227\n"
"v_fma_f32 v243, v243, v227, v227\n"
"v_mfma_f32_16x16x32_bf16 v[64:67], v[176:179], a[192:195], v[64:67]\n"
"buffer_load_dwordx4 v211, s[28:31], s66 offen lds\n"
"v_fma_f32 v244, v244, v227, v227\n"
"v_fma_f32 v245, v245, v227, v227\n"
"v_mfma_f32_16x16x32_bf16 v[68:71], v[176:179], a[196:199], v[68:71]\n"
"s_add_u32 s66, s63, 0x24000\n"
"s_add_u32 m0, s59, 0x2100\n"
"v_rcp_f32 v241, v241\n"
"v_fma_f32 v246, v246, v227, v227\n"
"v_mfma_f32_16x16x32_bf16 v[72:75], v[176:179], a[200:203], v[72:75]\n"
"ds_read_b64_tr_b16 v[184:185], v219 offset:21120\n"
"v_fma_f32 v247, v247, v227, v227\n"
"v_mul_f32 v44, v12, v44\n"
"v_mfma_f32_16x16x32_bf16 v[76:79], v[176:179], a[204:207], v[76:79]\n"
"s_waitcnt lgkmcnt(1)\n"
"ds_read_b64_tr_b16 v[186:187], v219 offset:21376\n"
"v_mul_f32 v45, v13, v45\n"
"v_mfma_f32_16x16x32_bf16 v[92:95], v[180:183], a[204:207], v[92:95]\n"
"v_rcp_f32 v242, v242\n"
"v_mul_f32 v46, v14, v46\n"
"v_mfma_f32_16x16x32_bf16 v[88:91], v[180:183], a[200:203], v[88:91]\n"
"s_mov_b64 exec, 0xc0000\n buffer_load_dwordx2 v[230:231], v138, s[44:47], s16 offen offset:0 sc0 sc1\n s_mov_b64 exec, -1\n"
"v_rcp_f32 v243, v243\n"
"v_mfma_f32_16x16x32_bf16 v[84:87], v[180:183], a[196:199], v[84:87]\n"
"ds_read_b64_tr_b16 v[188:189], v219 offset:23232\n"
"v_mul_f32 v47, v15, v47\n"
"v_rcp_f32 v244, v244\n"
"v_mfma_f32_16x16x32_bf16 v[80:83], v[180:183], a[192:195], v[80:83]\n"
"s_waitcnt lgkmcnt(1)\n"
"ds_read_b64_tr_b16 v[190:191], v219 offset:23488\n"
"v_mul_f32 v60, v28, v60\n"
"v_mfma_f32_16x16x32_bf16 v[64:67], v[184:187], a[208:211], v[64:67]\n"
"buffer_load_dwordx4 v211, s[28:31], s66 offen lds\n"
"v_rcp_f32 v245, v245\n"
"v_mfma_f32_16x16x32_bf16 v[68:71], v[184:187], a[212:215], v[68:71]\n"
"s_add_u32 s66, s63, 0x2d000\n"
"s_add_u32 m0, s59, 0x3180\n"
"v_rcp_f32 v246, v246\n"
"v_mul_f32 v61, v29, v61\n"
"v_mfma_f32_16x16x32_bf16 v[72:75], v[184:187], a[216:219], v[72:75]\n"
"ds_read_b64_tr_b16 v[192:193], v219 offset:25344\n"
"v_mul_f32 v62, v30, v62\n"
"v_rcp_f32 v247, v247\n"
"v_mfma_f32_16x16x32_bf16 v[76:79], v[184:187], a[220:223], v[76:79]\n"
"s_waitcnt lgkmcnt(1)\n"
"ds_read_b64_tr_b16 v[194:195], v219 offset:25600\n"
"v_mul_f32 v63, v31, v63\n"
"v_mfma_f32_16x16x32_bf16 v[92:95], v[188:191], a[220:223], v[92:95]\n"
"ds_read_b64_tr_b16 v[196:197], v219 offset:27456\n"
"v_mul_f32 v44, v44, v240\n"
"v_mfma_f32_16x16x32_bf16 v[88:91], v[188:191], a[216:219], v[88:91]\n"
"buffer_load_dwordx4 v211, s[28:31], s66 offen lds\n"
"v_mul_f32 v45, v45, v241\n"
"v_mul_f32 v46, v46, v242\n"
"v_mfma_f32_16x16x32_bf16 v[84:87], v[188:191], a[212:215], v[84:87]\n"
"s_add_u32 s66, s78, 0\n"
"s_add_u32 m0, s59, 0x10800\n"
"ds_read_b64_tr_b16 v[198:199], v219 offset:27712\n"
"v_mul_f32 v47, v47, v243\n"
"v_mul_f32 v60, v60, v244\n"
"v_mfma_f32_16x16x32_bf16 v[80:83], v[188:191], a[208:211], v[80:83]\n"
"s_waitcnt lgkmcnt(2)\n"
"ds_read_b64_tr_b16 v[176:177], v219 offset:29568\n"
"v_mul_f32 v61, v61, v245\n"
"v_mul_f32 v62, v62, v246\n"
"v_mfma_f32_16x16x32_bf16 v[64:67], v[192:195], a[224:227], v[64:67]\n"
"ds_read_b64_tr_b16 v[178:179], v219 offset:29824\n"
"v_mul_f32 v63, v63, v247\n"
"v_cvt_pk_bf16_f32 v249, v42, v43\n"
"v_mfma_f32_16x16x32_bf16 v[68:71], v[192:195], a[228:231], v[68:71]\n"
"ds_read_b64_tr_b16 v[180:181], v219 offset:31680\n"
"v_cvt_pk_bf16_f32 v250, v56, v57\n"
"v_cvt_pk_bf16_f32 v251, v58, v59\n"
"v_mfma_f32_16x16x32_bf16 v[72:75], v[192:195], a[232:235], v[72:75]\n"
"buffer_store_dwordx4 v[248:251], v139, s[44:47], s52 offen offset:0 sc0 sc1\n"
"v_cvt_pk_bf16_f32 v252, v44, v45\n"
"v_cvt_pk_bf16_f32 v253, v46, v47\n"
"v_mfma_f32_16x16x32_bf16 v[76:79], v[192:195], a[236:239], v[76:79]\n"
"s_waitcnt lgkmcnt(3)\n"
"ds_write_b128 v206, v[248:251] offset:50688\n"
"v_cvt_pk_bf16_f32 v254, v60, v61\n"
"v_cvt_pk_bf16_f32 v255, v62, v63\n"
"v_mfma_f32_16x16x32_bf16 v[92:95], v[196:199], a[236:239], v[92:95]\n"
"s_add_u32 s52, s71, 0xe00000\n"
"ds_write_b128 v206, v[252:255] offset:51776\n"
"v_mfma_f32_16x16x32_bf16 v[88:91], v[196:199], a[232:235], v[88:91]\n"
"s_waitcnt vmcnt(18)\n"
"ds_read_b64_tr_b16 v[182:183], v219 offset:31936\n"
"v_mfma_f32_16x16x32_bf16 v[84:87], v[196:199], a[228:231], v[84:87]\n"
"s_barrier\n"
"buffer_load_dwordx4 v211, s[24:27], s66 offen lds\n"
"v_mfma_f32_16x16x32_bf16 v[80:83], v[196:199], a[224:227], v[80:83]\n"
"s_waitcnt lgkmcnt(3)\n"
"s_add_u32 s66, s78, 0\n"
"ds_read_b64_tr_b16 v[184:185], v219 offset:33792\n"
"v_mfma_f32_16x16x32_bf16 v[64:67], v[176:179], a[240:243], v[64:67]\n"
"s_add_u32 m0, s59, 0x11880\n"
"ds_read_b64_tr_b16 v[186:187], v219 offset:34048\n"
"v_mfma_f32_16x16x32_bf16 v[68:71], v[176:179], a[244:247], v[68:71]\n"
"ds_read_b64_tr_b16 v[188:189], v219 offset:35904\n"
"v_mfma_f32_16x16x32_bf16 v[72:75], v[176:179], a[248:251], v[72:75]\n"
"ds_read_b64_tr_b16 v[190:191], v219 offset:36160\n"
"v_mfma_f32_16x16x32_bf16 v[76:79], v[176:179], a[252:255], v[76:79]\n"
"s_waitcnt lgkmcnt(4)\n"
"buffer_load_dwordx4 v211, s[28:31], s66 offen lds\n"
"v_mfma_f32_16x16x32_bf16 v[92:95], v[180:183], a[252:255], v[92:95]\n"
"s_add_u32 s66, s78, 0x9000\n"
"s_add_u32 m0, s59, 0x12900\n"
"ds_read_b64_tr_b16 v[192:193], v219 offset:38016\n"
"v_mfma_f32_16x16x32_bf16 v[88:91], v[180:183], a[248:251], v[88:91]\n"
"ds_read_b64_tr_b16 v[194:195], v219 offset:38272\n"
"v_mfma_f32_16x16x32_bf16 v[84:87], v[180:183], a[244:247], v[84:87]\n"
"buffer_store_dwordx4 v[252:255], v139, s[44:47], s52 offen offset:0 sc0 sc1\n"
"v_mfma_f32_16x16x32_bf16 v[80:83], v[180:183], a[240:243], v[80:83]\n"
"s_waitcnt lgkmcnt(3)\n"
"s_add_u32 s52, s70, 0x1400000\n"
"ds_read_b64_tr_b16 v[196:197], v219 offset:40128\n"
"v_mfma_f32_16x16x32_bf16 v[64:67], v[184:187], v[144:147], v[64:67]\n"
"ds_read_b64_tr_b16 v[198:199], v219 offset:40384\n"
"v_mfma_f32_16x16x32_bf16 v[68:71], v[184:187], v[148:151], v[68:71]\n"
"ds_read_b64_tr_b16 a[168:169], v200 offset:42240\n"
"v_mfma_f32_16x16x32_bf16 v[72:75], v[184:187], v[152:155], v[72:75]\n"
"ds_read_b64_tr_b16 a[170:171], v200 offset:42496\n"
"v_mfma_f32_16x16x32_bf16 v[76:79], v[184:187], v[156:159], v[76:79]\n"
"s_waitcnt lgkmcnt(4)\n"
"ds_read_b64_tr_b16 a[172:173], v200 offset:42272\n"
"v_mfma_f32_16x16x32_bf16 v[92:95], v[188:191], v[156:159], v[92:95]\n"
"buffer_load_dwordx4 v211, s[24:27], s66 offen lds\n"
"v_mfma_f32_16x16x32_bf16 v[88:91], v[188:191], v[152:155], v[88:91]\n"
"s_add_u32 s66, s78, 0x9000\n"
"s_add_u32 m0, s59, 0x13980\n"
"ds_read_b64_tr_b16 a[174:175], v200 offset:42528\n"
"v_mfma_f32_16x16x32_bf16 v[84:87], v[188:191], v[148:151], v[84:87]\n"
"ds_read_b64_tr_b16 a[240:241], v200 offset:42304\n"
"v_mfma_f32_16x16x32_bf16 v[80:83], v[188:191], v[144:147], v[80:83]\n"
"ds_read_b64_tr_b16 a[242:243], v200 offset:42560\n"
"v_mfma_f32_16x16x32_bf16 v[64:67], v[192:195], v[160:163], v[64:67]\n"
"ds_read_b64_tr_b16 a[244:245], v200 offset:42336\n"
"v_mfma_f32_16x16x32_bf16 v[68:71], v[192:195], v[164:167], v[68:71]\n"
"buffer_load_dwordx4 v211, s[28:31], s66 offen lds\n"
"v_mfma_f32_16x16x32_bf16 v[72:75], v[192:195], v[168:171], v[72:75]\n"
"s_add_u32 s66, s80, s74\n"
"s_add_u32 m0, s59, 0x16b00\n"
"ds_read_b64_tr_b16 a[246:247], v200 offset:42592\n"
"v_mfma_f32_16x16x32_bf16 v[76:79], v[192:195], v[172:175], v[76:79]\n"
"s_waitcnt lgkmcnt(4)\n"
"ds_read_b64_tr_b16 a[248:249], v200 offset:46464\n"
"v_mul_f32 v240, v220, v64\n"
"v_mul_f32 v241, v220, v65\n"
"v_mul_f32 v242, v220, v66\n"
"v_mul_f32 v243, v220, v67\n"
"v_mul_f32 v244, v221, v68\n"
"v_mfma_f32_16x16x32_bf16 v[92:95], v[196:199], v[172:175], v[92:95]\n"
"v_exp_f32 v240, v240\n"
"v_mul_f32 v245, v221, v69\n"
"v_mfma_f32_16x16x32_bf16 v[88:91], v[196:199], v[168:171], v[88:91]\n"
"v_exp_f32 v241, v241\n"
"v_mul_f32 v246, v221, v70\n"
"v_mul_f32 v247, v221, v71\n"
"v_mfma_f32_16x16x32_bf16 v[84:87], v[196:199], v[164:167], v[84:87]\n"
"buffer_load_dwordx4 a[56:59], v139, s[44:47], s94 offen offset:0 sc0 sc1\n"
"v_exp_f32 v242, v242\n"
"v_mfma_f32_16x16x32_bf16 v[80:83], v[196:199], v[160:163], v[80:83]\n"
"v_exp_f32 v243, v243\n"
"v_exp_f32 v244, v244\n"
"v_mfma_f32_16x16x32_bf16 v[96:99], a[168:171], a[0:3], 0\n"
"s_waitcnt vmcnt(17)\n"
"v_exp_f32 v245, v245\n"
"v_exp_f32 v246, v246\n"
"v_mfma_f32_16x16x32_bf16 v[100:103], a[168:171], a[4:7], 0\n"
"v_exp_f32 v247, v247\n"
"v_fma_f32 v240, v240, v224, v224\n"
"v_mfma_f32_16x16x32_bf16 v[108:111], a[172:175], a[4:7], 0\n"
"v_rcp_f32 v240, v240\n"
"v_fma_f32 v241, v241, v224, v224\n"
"v_fma_f32 v242, v242, v224, v224\n"
"v_mfma_f32_16x16x32_bf16 v[104:107], a[172:175], a[0:3], 0\n"
"L_st9_%=:\n s_cbranch_execz L_st13_%=\n"
"s_waitcnt lgkmcnt(0)\n"
"ds_read_b64_tr_b16 a[250:251], v200 offset:46720\n"
"v_fma_f32 v243, v243, v224, v224\n"
"v_fma_f32 v244, v244, v225, v225\n"
"v_mfma_f32_16x16x32_bf16 v[112:115], a[240:243], a[0:3], 0\n"
"ds_read_b64_tr_b16 a[252:253], v200 offset:46496\n"
"buffer_load_dwordx4 v212, s[40:43], s66 offen lds\n"
"v_fma_f32 v245, v245, v225, v225\n"
"v_fma_f32 v246, v246, v225, v225\n"
"v_mfma_f32_16x16x32_bf16 v[116:119], a[240:243], a[4:7], 0\n"
"s_add_u32 s66, s80, s75\n"
"s_add_u32 m0, s59, 0x17b80\n"
"ds_read_b64_tr_b16 a[254:255], v200 offset:46752\n"
"v_fma_f32 v247, v247, v225, v225\n"
"v_mul_f32 v80, v64, v80\n"
"v_mfma_f32_16x16x32_bf16 v[124:127], a[244:247], a[4:7], 0\n"
"s_barrier\n"
"ds_read_b64_tr_b16 v[176:177], v200 offset:46528\n"
"v_rcp_f32 v241, v241\n"
"v_mul_f32 v81, v65, v81\n"
"v_mfma_f32_16x16x32_bf16 v[120:123], a[244:247], a[0:3], 0\n"
"s_waitcnt vmcnt(16) lgkmcnt(3)\n"
"ds_read_b64_tr_b16 v[178:179], v200 offset:46784\n"
"v_rcp_f32 v242, v242\n"
"v_mfma_f32_16x16x32_bf16 v[96:99], a[248:251], a[8:11], v[96:99]\n"
"s_waitcnt vmcnt(15)\n"
"ds_read_b64_tr_b16 v[180:181], v200 offset:46560\n"
"v_mul_f32 v82, v66, v82\n"
"v_rcp_f32 v243, v243\n"
"v_mfma_f32_16x16x32_bf16 v[100:103], a[248:251], a[12:15], v[100:103]\n"
"s_waitcnt lgkmcnt(3)\n"
"ds_read_b64_tr_b16 v[182:183], v200 offset:46816\n"
"buffer_load_dwordx4 v212, s[40:43], s66 offen lds\n"
"v_mul_f32 v83, v67, v83\n"
"v_mfma_f32_16x16x32_bf16 v[108:111], a[252:255], a[12:15], v[108:111]\n"
"s_add_u32 s66, s81, s74\n"
"s_add_u32 m0, s59, 0x21000\n"
"ds_read_b64_tr_b16 v[184:185], v200 offset:50688\n"
"v_rcp_f32 v244, v244\n"
"v_mul_f32 v84, v68, v84\n"
"v_mfma_f32_16x16x32_bf16 v[104:107], a[252:255], a[8:11], v[104:107]\n"
"s_waitcnt lgkmcnt(3)\n"
"ds_read_b64_tr_b16 v[186:187], v200 offset:50944\n"
"v_rcp_f32 v245, v245\n"
"v_mul_f32 v85, v69, v85\n"
"v_mfma_f32_16x16x32_bf16 v[112:115], v[176:179], a[8:11], v[112:115]\n"
"ds_read_b64_tr_b16 v[188:189], v200 offset:50720\n"
"v_rcp_f32 v246, v246\n"
"v_mfma_f32_16x16x32_bf16 v[116:119], v[176:179], a[12:15], v[116:119]\n"
"s_waitcnt lgkmcnt(3)\n"
"ds_read_b64_tr_b16 v[190:191], v200 offset:50976\n"
"v_mul_f32 v86, v70, v86\n"
"v_rcp_f32 v247, v247\n"
"v_mfma_f32_16x16x32_bf16 v[124:127], v[180:183], a[12:15], v[124:127]\n"
"ds_read_b64_tr_b16 v[192:193], v200 offset:50752\n"
"buffer_load_dwordx4 v213, s[40:43], s66 offen lds\n"
"v_mul_f32 v87, v71, v87\n"
"v_mfma_f32_16x16x32_bf16 v[120:123], v[180:183], a[8:11], v[120:123]\n"
"s_waitcnt lgkmcnt(3)\n"
"s_add_u32 s66, s81, s75\n"
"ds_read_b64_tr_b16 v[194:195], v200 offset:51008\n"
"v_mul_f32 v80, v80, v240\n"
"v_mul_f32 v81, v81, v241\n"
"v_mul_f32 v82, v82, v242\n"
"v_mfma_f32_16x16x32_bf16 v[96:99], v[184:187], a[16:19], v[96:99]\n"
"s_add_u32 m0, s59, 0x23100\n"
"ds_read_b64_tr_b16 v[196:197], v200 offset:50784\n"
"v_mul_f32 v83, v83, v243\n"
"v_mul_f32 v84, v84, v244\n"
"v_mfma_f32_16x16x32_bf16 v[100:103], v[184:187], a[20:23], v[100:103]\n"
"s_waitcnt lgkmcnt(3)\n"
"ds_read_b64_tr_b16 v[198:199], v200 offset:51040\n"
"v_mul_f32 v85, v85, v245\n"
"v_mul_f32 v86, v86, v246\n"
"v_mul_f32 v87, v87, v247\n"
"v_mfma_f32_16x16x32_bf16 v[108:111], v[188:191], a[20:23], v[108:111]\n"
"ds_read_b64_tr_b16 a[168:169], v200 offset:54912\n"
"buffer_load_dwordx4 v213, s[40:43], s66 offen lds\n"
"v_mul_f32 v240, v222, v72\n"
"v_mfma_f32_16x16x32_bf16 v[104:107], v[188:191], a[16:19], v[104:107]\n"
"s_waitcnt lgkmcnt(3)\n"
"s_add_u32 s66, s78, 0x12000\n"
"ds_read_b64_tr_b16 a[170:171], v200 offset:55168\n"
"v_mul_f32 v241, v222, v73\n"
"v_mul_f32 v242, v222, v74\n"
"v_mul_f32 v243, v222, v75\n"
"v_mfma_f32_16x16x32_bf16 v[112:115], v[192:195], a[16:19], v[112:115]\n"
"s_add_u32 m0, s59, 0x24180\n"
"ds_read_b64_tr_b16 a[172:173], v200 offset:54944\n"
"v_mul_f32 v244, v223, v76\n"
"v_mul_f32 v245, v223, v77\n"
"v_mfma_f32_16x16x32_bf16 v[116:119], v[192:195], a[20:23], v[116:119]\n"
"s_waitcnt lgkmcnt(3)\n"
"ds_read_b64_tr_b16 a[174:175], v200 offset:55200\n"
"v_mul_f32 v246, v223, v78\n"
"v_mul_f32 v247, v223, v79\n"
"v_exp_f32 v240, v240\n"
"v_mfma_f32_16x16x32_bf16 v[124:127], v[196:199], a[20:23], v[124:127]\n"
"s_add_u32 s94, s61, 0\n"
"ds_read_b64_tr_b16 a[240:241], v200 offset:54976\n"
"buffer_load_dwordx4 v211, s[24:27], s66 offen lds\n"
"v_mfma_f32_16x16x32_bf16 v[120:123], v[196:199], a[16:19], v[120:123]\n"
"s_waitcnt lgkmcnt(3)\n"
"s_add_u32 s66, s78, 0x12000\n"
"ds_read_b64_tr_b16 a[242:243], v200 offset:55232\n"
"v_exp_f32 v241, v241\n"
"v_exp_f32 v242, v242\n"
"v_mfma_f32_16x16x32_bf16 v[96:99], a[168:171], a[24:27], v[96:99]\n"
"s_add_u32 m0, s59, 0x25200\n"
"ds_read_b64_tr_b16 a[244:245], v200 offset:55008\n"
"v_exp_f32 v243, v243\n"
"v_mfma_f32_16x16x32_bf16 v[100:103], a[168:171], a[28:31], v[100:103]\n"
"s_waitcnt lgkmcnt(3)\n"
"ds_read_b64_tr_b16 a[246:247], v200 offset:55264\n"
"v_exp_f32 v244, v244\n"
"v_mfma_f32_16x16x32_bf16 v[108:111], a[172:175], a[28:31], v[108:111]\n"
"ds_read_b64_tr_b16 a[248:249], v201 offset:0\n"
"v_exp_f32 v245, v245\n"
"v_mfma_f32_16x16x32_bf16 v[104:107], a[172:175], a[24:27], v[104:107]\n"
"s_waitcnt lgkmcnt(3)\n"
"ds_read_b64_tr_b16 a[250:251], v201 offset:256\n"
"buffer_load_dwordx4 v211, s[28:31], s66 offen lds\n"
"v_exp_f32 v246, v246\n"
"v_mfma_f32_16x16x32_bf16 v[112:115], a[240:243], a[24:27], v[112:115]\n"
"s_add_u32 s66, s78, 0x1b000\n"
"s_add_u32 m0, s59, 0x26280\n"
"ds_read_b64_tr_b16 a[252:253], v201 offset:32\n"
"v_exp_f32 v247, v247\n"
"v_mfma_f32_16x16x32_bf16 v[116:119], a[240:243], a[28:31], v[116:119]\n"
"s_waitcnt lgkmcnt(3)\n"
"ds_read_b64_tr_b16 a[254:255], v201 offset:288\n"
"v_fma_f32 v240, v240, v226, v226\n"
"v_fma_f32 v241, v241, v226, v226\n"
"v_fma_f32 v242, v242, v226, v226\n"
"v_mfma_f32_16x16x32_bf16 v[124:127], a[244:247], a[28:31], v[124:127]\n"
"ds_read_b64_tr_b16 v[176:177], v201 offset:64\n"
"v_fma_f32 v243, v243, v226, v226\n"
"v_fma_f32 v244, v244, v227, v227\n"
"v_mfma_f32_16x16x32_bf16 v[120:123], a[244:247], a[24:27], v[120:123]\n"
"s_waitcnt lgkmcnt(3)\n"
"ds_read_b64_tr_b16 v[178:179], v201 offset:320\n"
"v_fma_f32 v245, v245, v227, v227\n"
"v_fma_f32 v246, v246, v227, v227\n"
"v_mfma_f32_16x16x32_bf16 v[96:99], a[248:251], a[32:35], v[96:99]\n"
"ds_read_b64_tr_b16 v[180:181], v201 offset:96\n"
"buffer_load_dwordx4 a[60:63], v139, s[44:47], s94 offen offset:0 sc0 sc1\n"
"v_fma_f32 v247, v247, v227, v227\n"
"v_rcp_f32 v240, v240\n"
"v_mfma_f32_16x16x32_bf16 v[100:103], a[248:251], a[36:39], v[100:103]\n"
"s_waitcnt lgkmcnt(3)\n"
"ds_read_b64_tr_b16 v[182:183], v201 offset:352\n"
"v_mul_f32 v88, v72, v88\n"
"v_mfma_f32_16x16x32_bf16 v[108:111], a[252:255], a[36:39], v[108:111]\n"
"ds_read_b64_tr_b16 v[184:185], v201 offset:4224\n"
"v_rcp_f32 v241, v241\n"
"v_mul_f32 v89, v73, v89\n"
"v_mfma_f32_16x16x32_bf16 v[104:107], a[252:255], a[32:35], v[104:107]\n"
"s_waitcnt lgkmcnt(3)\n"
"ds_read_b64_tr_b16 v[186:187], v201 offset:4480\n"
"v_rcp_f32 v242, v242\n"
"v_mfma_f32_16x16x32_bf16 v[112:115], v[176:179], a[32:35], v[112:115]\n"
"ds_read_b64_tr_b16 v[188:189], v201 offset:4256\n"
"v_mul_f32 v90, v74, v90\n"
"v_rcp_f32 v243, v243\n"
"v_mfma_f32_16x16x32_bf16 v[116:119], v[176:179], a[36:39], v[116:119]\n"
"s_waitcnt vmcnt(19) lgkmcnt(3)\n"
"ds_read_b64_tr_b16 v[190:191], v201 offset:4512\n"
"buffer_load_dwordx4 v211, s[24:27], s66 offen lds\n"
"v_mul_f32 v91, v75, v91\n"
"v_mfma_f32_16x16x32_bf16 v[124:127], v[180:183], a[36:39], v[124:127]\n"
"s_barrier\n"
"s_add_u32 s66, s78, 0x1b000\n"
"ds_read_b64_tr_b16 v[192:193], v201 offset:4288\n"
"v_rcp_f32 v244, v244\n"
"v_mfma_f32_16x16x32_bf16 v[120:123], v[180:183], a[32:35], v[120:123]\n"
"s_waitcnt lgkmcnt(3)\n"
"s_add_u32 m0, s59, 0x4200\n"
"ds_read_b64_tr_b16 v[194:195], v201 offset:4544\n"
"v_mul_f32 v92, v76, v92\n"
"v_rcp_f32 v245, v245\n"
"v_mfma_f32_16x16x32_bf16 v[96:99], v[184:187], a[40:43], v[96:99]\n"
"ds_read_b64_tr_b16 v[196:197], v201 offset:4320\n"
"v_mul_f32 v93, v77, v93\n"
"v_rcp_f32 v246, v246\n"
"v_mfma_f32_16x16x32_bf16 v[100:103], v[184:187], a[44:47], v[100:103]\n"
"s_waitcnt lgkmcnt(3)\n"
"ds_read_b64_tr_b16 v[198:199], v201 offset:4576\n"
"v_mul_f32 v94, v78, v94\n"
"v_mfma_f32_16x16x32_bf16 v[108:111], v[188:191], a[44:47], v[108:111]\n"
"ds_read_b64_tr_b16 a[168:169], v200 offset:0\n"
"v_rcp_f32 v247, v247\n"
"v_mul_f32 v95, v79, v95\n"
"v_mfma_f32_16x16x32_bf16 v[104:107], v[188:191], a[40:43], v[104:107]\n"
"s_waitcnt vmcnt(11) lgkmcnt(3)\n"
"ds_read_b64_tr_b16 a[170:171], v200 offset:256\n"
"buffer_store_dwordx2 v[236:237], v142, s[44:47], s17 offen offset:512 sc0 sc1\n"
"v_mul_f32 v88, v88, v240\n"
"v_mfma_f32_16x16x32_bf16 v[112:115], v[192:195], a[40:43], v[112:115]\n"
"ds_read_b64_tr_b16 a[172:173], v200 offset:32\n"
"v_mul_f32 v89, v89, v241\n"
"v_mul_f32 v90, v90, v242\n"
"v_mfma_f32_16x16x32_bf16 v[116:119], v[192:195], a[44:47], v[116:119]\n"
"s_waitcnt lgkmcnt(3)\n"
"ds_read_b64_tr_b16 a[174:175], v200 offset:288\n"
"buffer_load_dwordx4 v211, s[28:31], s66 offen lds\n"
"v_mul_f32 v91, v91, v243\n"
"v_mfma_f32_16x16x32_bf16 v[124:127], v[196:199], a[44:47], v[124:127]\n"
"s_add_u32 s66, s78, 0x24000\n"
"s_add_u32 m0, s59, 0x5280\n"
"ds_read_b64_tr_b16 a[240:241], v200 offset:64\n"
"v_mul_f32 v92, v92, v244\n"
"v_mul_f32 v93, v93, v245\n"
"v_mfma_f32_16x16x32_bf16 v[120:123], v[196:199], a[40:43], v[120:123]\n"
"s_waitcnt lgkmcnt(3)\n"
"ds_read_b64_tr_b16 a[242:243], v200 offset:320\n"
"v_mul_f32 v94, v94, v246\n"
"v_mul_f32 v95, v95, v247\n"
"v_cvt_pk_bf16_f32 v248, v80, v81\n"
"v_mfma_f32_16x16x32_bf16 v[96:99], a[168:171], a[48:51], v[96:99]\n"
"ds_read_b64_tr_b16 a[244:245], v200 offset:96\n"
"v_cvt_pk_bf16_f32 v249, v82, v83\n"
"v_cvt_pk_bf16_f32 v250, v84, v85\n"
"v_mfma_f32_16x16x32_bf16 v[100:103], a[168:171], a[52:55], v[100:103]\n"
"s_waitcnt lgkmcnt(3)\n"
"ds_read_b64_tr_b16 a[246:247], v200 offset:352\n"
"v_cvt_pk_bf16_f32 v251, v86, v87\n"
"v_cvt_pk_bf16_f32 v252, v88, v89\n"
"v_mfma_f32_16x16x32_bf16 v[108:111], a[172:175], a[52:55], v[108:111]\n"
"ds_read_b64_tr_b16 a[248:249], v201 offset:25344\n"
"buffer_store_dwordx2 v[248:249], v140, s[44:47], s52 offen offset:0 sc0 sc1\n"
"v_cvt_pk_bf16_f32 v253, v90, v91\n"
"v_mfma_f32_16x16x32_bf16 v[104:107], a[172:175], a[48:51], v[104:107]\n"
"s_waitcnt lgkmcnt(3)\n"
"s_add_u32 s52, s70, 0x1600000\n"
"ds_read_b64_tr_b16 a[250:251], v201 offset:25600\n"
"v_cvt_pk_bf16_f32 v254, v92, v93\n"
"v_mfma_f32_16x16x32_bf16 v[112:115], a[240:243], a[48:51], v[112:115]\n"
"s_add_u32 s94, s85, 0x1000\n"
"ds_read_b64_tr_b16 a[252:253], v201 offset:25376\n"
"buffer_load_dwordx4 v211, s[24:27], s66 offen lds\n"
"v_mfma_f32_16x16x32_bf16 v[116:119], a[240:243], a[52:55], v[116:119]\n"
"s_waitcnt lgkmcnt(3)\n"
"s_add_u32 s66, s78, 0x24000\n"
"ds_write_b64 v203, v[248:249] offset:42240\n"
"v_cvt_pk_bf16_f32 v255, v94, v95\n"
"v_mfma_f32_16x16x32_bf16 v[124:127], a[244:247], a[52:55], v[124:127]\n"
"s_add_u32 m0, s59, 0x6300\n"
"ds_read_b64_tr_b16 a[254:255], v201 offset:25632\n"
"ds_write_b64 v203, v[250:251] offset:43328\n"
"v_mfma_f32_16x16x32_bf16 v[120:123], a[244:247], a[48:51], v[120:123]\n"
"s_waitcnt lgkmcnt(4)\n"
"ds_read_b64_tr_b16 v[176:177], v201 offset:25408\n"
"ds_write_b64 v204, v[252:253] offset:42240\n"
"v_mfma_f32_16x16x32_bf16 v[144:147], a[248:251], a[0:3], 0\n"
"ds_read_b64_tr_b16 v[178:179], v201 offset:25664\n"
"ds_read_b64_tr_b16 v[180:181], v201 offset:25440\n"
"v_subrev_u32 v238, s18, v230\n"
"v_mfma_f32_16x16x32_bf16 v[148:151], a[248:251], a[4:7], 0\n"
"s_waitcnt lgkmcnt(5)\n"
"ds_read_b64_tr_b16 v[182:183], v201 offset:25696\n"
"buffer_load_dwordx4 a[64:67], v139, s[44:47], s94 offen offset:0 sc0 sc1\n"
"v_mfma_f32_16x16x32_bf16 v[156:159], a[252:255], a[4:7], 0\n"
"s_add_u32 s94, s61, 0x1000\n"
"ds_write_b64 v204, v[254:255] offset:43328\n"
"v_mfma_f32_16x16x32_bf16 v[152:155], a[252:255], a[0:3], 0\n"
"s_waitcnt lgkmcnt(3)\n"
"ds_read_b64_tr_b16 v[184:185], v201 offset:29568\n"
"ds_read_b64_tr_b16 v[186:187], v201 offset:29824\n"
"v_xor_b32 v239, v230, v231\n"
"v_mfma_f32_16x16x32_bf16 v[160:163], v[176:179], a[0:3], 0\n"
"ds_read_b64_tr_b16 v[188:189], v201 offset:29600\n"
"v_xor_b32 v239, 0x5bd1e995, v239\n"
"v_mfma_f32_16x16x32_bf16 v[164:167], v[176:179], a[4:7], 0\n"
"s_waitcnt lgkmcnt(4)\n"
"ds_read_b64_tr_b16 v[190:191], v201 offset:29856\n"
"v_max_u32 v238, v238, v239\n"
"v_mfma_f32_16x16x32_bf16 v[172:175], v[180:183], a[4:7], 0\n"
"ds_read_b64_tr_b16 v[192:193], v201 offset:29632\n"
"buffer_store_dwordx2 v[250:251], v140, s[44:47], s52 offen offset:0 sc0 sc1\n"
"v_mfma_f32_16x16x32_bf16 v[168:171], v[180:183], a[0:3], 0\n"
"s_waitcnt lgkmcnt(3)\n"
"s_add_u32 s52, s71, 0x1400000\n"
"ds_read_b64_tr_b16 v[194:195], v201 offset:29888\n"
"v_mfma_f32_16x16x32_bf16 v[144:147], v[184:187], a[8:11], v[144:147]\n"
"ds_read_b64_tr_b16 v[196:197], v201 offset:29664\n"
"v_cmp_ge_u32 s[10:11], v228, v238\n"
"v_mfma_f32_16x16x32_bf16 v[148:151], v[184:187], a[12:15], v[148:151]\n"
"s_waitcnt lgkmcnt(3)\n"
"ds_read_b64_tr_b16 v[198:199], v201 offset:29920\n"
"buffer_store_dwordx2 v[252:253], v140, s[44:47], s52 offen offset:0 sc0 sc1\n"
"v_mfma_f32_16x16x32_bf16 v[156:159], v[188:191], a[12:15], v[156:159]\n"
"s_add_u32 s52, s71, 0x1600000\n"
"s_andn2_b32 s10, 0xc0000, s10\n s_cbranch_scc1 L_slow1_%=\n L_back1_%=:\n"
"ds_read_b64_tr_b16 a[168:169], v202 offset:8448\n"
"v_mfma_f32_16x16x32_bf16 v[152:155], v[188:191], a[8:11], v[152:155]\n"
"s_waitcnt lgkmcnt(3)\n"
"ds_read_b64_tr_b16 a[170:171], v202 offset:8704\n"
"v_mfma_f32_16x16x32_bf16 v[160:163], v[192:195], a[8:11], v[160:163]\n"
"ds_read_b64_tr_b16 a[172:173], v202 offset:8480\n"
"buffer_store_dwordx2 v[254:255], v140, s[44:47], s52 offen offset:0 sc0 sc1\n"
"v_mfma_f32_16x16x32_bf16 v[164:167], v[192:195], a[12:15], v[164:167]\n"
"s_waitcnt lgkmcnt(3)\n"
"ds_read_b64_tr_b16 a[174:175], v202 offset:8736\n"
"v_mfma_f32_16x16x32_bf16 v[172:175], v[196:199], a[12:15], v[172:175]\n"
"s_barrier\n"
"ds_read_b64_tr_b16 a[240:241], v202 offset:8512\n"
"buffer_load_dwordx4 a[68:71], v139, s[44:47], s94 offen offset:0 sc0 sc1\n"
"v_mfma_f32_16x16x32_bf16 v[168:171], v[196:199], a[8:11], v[168:171]\n"
"s_waitcnt lgkmcnt(3)\n"
"s_add_u32 s94, s85, 0x1000000\n"
"ds_read_b64_tr_b16 a[242:243], v202 offset:8768\n"
"v_mfma_f32_16x16x32_bf16 v[144:147], a[168:171], a[16:19], v[144:147]\n"
"ds_read_b64_tr_b16 a[244:245], v202 offset:8544\n"
"v_mfma_f32_16x16x32_bf16 v[148:151], a[168:171], a[20:23], v[148:151]\n"
"s_waitcnt lgkmcnt(3)\n"
"ds_read_b64_tr_b16 a[246:247], v202 offset:8800\n"
"v_mfma_f32_16x16x32_bf16 v[156:159], a[172:175], a[20:23], v[156:159]\n"
"ds_read_b64_tr_b16 a[248:249], v202 offset:12672\n"
"v_mfma_f32_16x16x32_bf16 v[152:155], a[172:175], a[16:19], v[152:155]\n"
"s_waitcnt lgkmcnt(3)\n"
"ds_read_b64_tr_b16 a[250:251], v202 offset:12928\n"
"buffer_load_dwordx2 a[72:73], v140, s[44:47], s94 offen offset:0 sc0 sc1\n"
"v_mfma_f32_16x16x32_bf16 v[160:163], a[240:243], a[16:19], v[160:163]\n"
"s_add_u32 s94, s85, 0x1001000\n"
"ds_read_b64_tr_b16 a[252:253], v202 offset:12704\n"
"v_mfma_f32_16x16x32_bf16 v[164:167], a[240:243], a[20:23], v[164:167]\n"
"s_waitcnt lgkmcnt(3)\n"
"ds_read_b64_tr_b16 a[254:255], v202 offset:12960\n"
"v_mfma_f32_16x16x32_bf16 v[172:175], a[244:247], a[20:23], v[172:175]\n"
"ds_read_b64_tr_b16 v[176:177], v202 offset:12736\n"
"buffer_load_dwordx2 a[74:75], v140, s[44:47], s94 offen offset:0 sc0 sc1\n"
"v_mfma_f32_16x16x32_bf16 v[168:171], a[244:247], a[16:19], v[168:171]\n"
"s_waitcnt lgkmcnt(3)\n"
"s_add_u32 s94, s61, 0x1000000\n"
"ds_read_b64_tr_b16 v[178:179], v202 offset:12992\n"
"v_mfma_f32_16x16x32_bf16 v[144:147], a[248:251], a[24:27], v[144:147]\n"
"ds_read_b64_tr_b16 v[180:181], v202 offset:12768\n"
"v_mfma_f32_16x16x32_bf16 v[148:151], a[248:251], a[28:31], v[148:151]\n"
"s_waitcnt lgkmcnt(3)\n"
"ds_read_b64_tr_b16 v[182:183], v202 offset:13024\n"
"buffer_load_dwordx2 a[76:77], v140, s[44:47], s94 offen offset:0 sc0 sc1\n"
"v_mfma_f32_16x16x32_bf16 v[156:159], a[252:255], a[28:31], v[156:159]\n"
"s_add_u32 s94, s61, 0x1001000\n"
"ds_read_b64_tr_b16 v[184:185], v200 offset:4224\n"
"v_mfma_f32_16x16x32_bf16 v[152:155], a[252:255], a[24:27], v[152:155]\n"
"s_waitcnt lgkmcnt(3)\n"
"ds_read_b64_tr_b16 v[186:187], v200 offset:4480\n"
"ds_read_b64_tr_b16 v[188:189], v200 offset:4256\n"
"v_mfma_f32_16x16x32_bf16 v[160:163], v[176:179], a[24:27], v[160:163]\n"
"ds_read_b64_tr_b16 v[190:191], v200 offset:4512\n"
"buffer_load_dwordx2 a[78:79], v140, s[44:47], s94 offen offset:0 sc0 sc1\n"
"v_mfma_f32_16x16x32_bf16 v[164:167], v[176:179], a[28:31], v[164:167]\n"
"s_waitcnt lgkmcnt(4)\n"
"ds_read_b64_tr_b16 v[192:193], v200 offset:4288\n"
"ds_read_b64_tr_b16 v[194:195], v200 offset:4544\n"
"v_mfma_f32_16x16x32_bf16 v[172:175], v[180:183], a[28:31], v[172:175]\n"
"s_add_u32 s94, s85, 0x1000000\n"
"ds_read_b64_tr_b16 v[196:197], v200 offset:4320\n"
"ds_read_b64_tr_b16 v[198:199], v200 offset:4576\n"
"v_mfma_f32_16x16x32_bf16 v[168:171], v[180:183], a[24:27], v[168:171]\n"
"s_waitcnt lgkmcnt(6)\n"
"ds_read_b64_tr_b16 a[168:169], v200 offset:8448\n"
"ds_read_b64_tr_b16 a[170:171], v200 offset:8704\n"
"v_mfma_f32_16x16x32_bf16 v[144:147], v[184:187], a[32:35], v[144:147]\n"
"ds_read_b64_tr_b16 a[172:173], v200 offset:8480\n"
"buffer_load_dword a81, v141, s[44:47], s94 offen offset:512 sc0 sc1\n"
"v_mfma_f32_16x16x32_bf16 v[148:151], v[184:187], a[36:39], v[148:151]\n"
"s_waitcnt lgkmcnt(7)\n"
"s_add_u32 s94, s85, 0x1001000\n"
"ds_read_b64_tr_b16 a[174:175], v200 offset:8736\n"
"v_mfma_f32_16x16x32_bf16 v[156:159], v[188:191], a[36:39], v[156:159]\n"
"ds_read_b64_tr_b16 a[240:241], v200 offset:8512\n"
"v_mfma_f32_16x16x32_bf16 v[152:155], v[188:191], a[32:35], v[152:155]\n"
"s_waitcnt lgkmcnt(7)\n"
"ds_read_b64_tr_b16 a[242:243], v200 offset:8768\n"
"buffer_load_dword a83, v141, s[44:47], s94 offen offset:512 sc0 sc1\n"
"v_mfma_f32_16x16x32_bf16 v[160:163], v[192:195], a[32:35], v[160:163]\n"
"s_add_u32 s94, s61, 0x1000000\n"
"ds_read_b128 a[120:123], v214 offset:50688\n"
"v_mfma_f32_16x16x32_bf16 v[164:167], v[192:195], a[36:39], v[164:167]\n"
"s_waitcnt lgkmcnt(5)\n"
"ds_read_b128 a[124:127], v208 offset:50688\n"
"v_mfma_f32_16x16x32_bf16 v[172:175], v[196:199], a[36:39], v[172:175]\n"
"ds_read_b128 a[128:131], v214 offset:59136\n"
"buffer_load_dword a85, v141, s[44:47], s94 offen offset:512 sc0 sc1\n"
"v_mfma_f32_16x16x32_bf16 v[168:171], v[196:199], a[32:35], v[168:171]\n"
"s_add_u32 s94, s61, 0x1001000\n"
"ds_read_b128 a[132:135], v208 offset:59136\n"
"v_mfma_f32_16x16x32_bf16 v[144:147], a[168:171], a[40:43], v[144:147]\n"
"ds_read_b64_tr_b16 a[244:245], v200 offset:8544\n"
"v_mfma_f32_16x16x32_bf16 v[148:151], a[168:171], a[44:47], v[148:151]\n"
"s_waitcnt lgkmcnt(5)\n"
"ds_read_b64_tr_b16 a[246:247], v200 offset:8800\n"
"buffer_load_dword a87, v141, s[44:47], s94 offen offset:512 sc0 sc1\n"
"v_mfma_f32_16x16x32_bf16 v[156:159], a[172:175], a[44:47], v[156:159]\n"
"s_add_u32 s94, s85, 0x400000\n"
"ds_read_b64_tr_b16 a[248:249], v200 offset:12672\n"
"v_mfma_f32_16x16x32_bf16 v[152:155], a[172:175], a[40:43], v[152:155]\n"
"ds_read_b64_tr_b16 a[250:251], v200 offset:12928\n"
"v_mfma_f32_16x16x32_bf16 v[160:163], a[240:243], a[40:43], v[160:163]\n"
"ds_read_b64_tr_b16 a[252:253], v200 offset:12704\n"
"buffer_load_dwordx4 a[88:91], v139, s[44:47], s94 offen offset:0 sc0 sc1\n"
"v_mfma_f32_16x16x32_bf16 v[164:167], a[240:243], a[44:47], v[164:167]\n"
"s_waitcnt vmcnt(27) lgkmcnt(3)\n"
"s_add_u32 s94, s61, 0x400000\n"
"ds_read_b64_tr_b16 a[254:255], v200 offset:12960\n"
"v_mfma_f32_16x16x32_bf16 v[172:175], a[244:247], a[44:47], v[172:175]\n"
"s_barrier\n"
"ds_read_b64_tr_b16 v[176:177], v200 offset:12736\n"
"v_mfma_f32_16x16x32_bf16 v[168:171], a[244:247], a[40:43], v[168:171]\n"
"s_waitcnt lgkmcnt(3)\n"
"ds_read_b64_tr_b16 v[178:179], v200 offset:12992\n"
"v_mfma_f32_16x16x32_bf16 v[144:147], a[248:251], a[48:51], v[144:147]\n"
"ds_read_b64_tr_b16 v[180:181], v200 offset:12768\n"
"v_mfma_f32_16x16x32_bf16 v[148:151], a[248:251], a[52:55], v[148:151]\n"
"s_waitcnt lgkmcnt(3)\n"
"ds_read_b64_tr_b16 v[182:183], v200 offset:13024\n"
"buffer_load_dwordx4 a[92:95], v139, s[44:47], s94 offen offset:0 sc0 sc1\n"
"v_mfma_f32_16x16x32_bf16 v[156:159], a[252:255], a[52:55], v[156:159]\n"
"s_add_u32 s94, s85, 0x401000\n"
"ds_read_b64_tr_b16 v[184:185], v201 offset:8448\n"
"v_mfma_f32_16x16x32_bf16 v[152:155], a[252:255], a[48:51], v[152:155]\n"
"s_waitcnt lgkmcnt(3)\n"
"ds_read_b64_tr_b16 v[186:187], v201 offset:8704\n"
"v_mfma_f32_16x16x32_bf16 v[160:163], v[176:179], a[48:51], v[160:163]\n"
"ds_read_b64_tr_b16 v[188:189], v201 offset:8480\n"
"v_mfma_f32_16x16x32_bf16 v[164:167], v[176:179], a[52:55], v[164:167]\n"
"s_waitcnt lgkmcnt(3)\n"
"ds_read_b64_tr_b16 v[190:191], v201 offset:8736\n"
"v_mfma_f32_16x16x32_bf16 v[172:175], v[180:183], a[52:55], v[172:175]\n"
"ds_read_b64_tr_b16 v[192:193], v201 offset:8512\n"
"buffer_load_dwordx4 a[96:99], v139, s[44:47], s94 offen offset:0 sc0 sc1\n"
"v_mfma_f32_16x16x32_bf16 v[168:171], v[180:183], a[48:51], v[168:171]\n"
"s_waitcnt vmcnt(28) lgkmcnt(3)\n"
"s_add_u32 s94, s61, 0x401000\n"
"ds_read_b64_tr_b16 v[194:195], v201 offset:8768\n"
"v_mfma_f32_16x16x32_bf16 v[96:99], v[184:187], a[56:59], v[96:99]\n"
"s_waitcnt vmcnt(21)\n"
"ds_read_b64_tr_b16 v[196:197], v201 offset:8544\n"
"v_mfma_f32_16x16x32_bf16 v[100:103], v[184:187], a[60:63], v[100:103]\n"
"s_waitcnt lgkmcnt(3)\n"
"ds_read_b64_tr_b16 v[198:199], v201 offset:8800\n"
"v_mfma_f32_16x16x32_bf16 v[108:111], v[188:191], a[60:63], v[108:111]\n"
"ds_read_b64_tr_b16 a[168:169], v201 offset:12672\n"
"v_mfma_f32_16x16x32_bf16 v[104:107], v[188:191], a[56:59], v[104:107]\n"
"s_waitcnt lgkmcnt(3)\n"
"ds_read_b64_tr_b16 a[170:171], v201 offset:12928\n"
"buffer_load_dwordx4 a[100:103], v139, s[44:47], s94 offen offset:0 sc0 sc1\n"
"v_mfma_f32_16x16x32_bf16 v[112:115], v[192:195], a[56:59], v[112:115]\n"
"s_add_u32 s94, s85, 0x800000\n"
"ds_read_b64_tr_b16 a[172:173], v201 offset:12704\n"
"v_mfma_f32_16x16x32_bf16 v[116:119], v[192:195], a[60:63], v[116:119]\n"
"s_waitcnt lgkmcnt(3)\n"
"ds_read_b64_tr_b16 a[174:175], v201 offset:12960\n"
"v_mfma_f32_16x16x32_bf16 v[124:127], v[196:199], a[60:63], v[124:127]\n"
"ds_read_b64_tr_b16 a[240:241], v201 offset:12736\n"
"v_mfma_f32_16x16x32_bf16 v[120:123], v[196:199], a[56:59], v[120:123]\n"
"s_waitcnt lgkmcnt(3)\n"
"ds_read_b64_tr_b16 a[242:243], v201 offset:12992\n"
"v_mfma_f32_16x16x32_bf16 v[144:147], a[168:171], a[56:59], v[144:147]\n"
"ds_read_b64_tr_b16 a[244:245], v201 offset:12768\n"
"buffer_load_dwordx4 v211, s[28:31], s66 offen lds\n"
"v_mfma_f32_16x16x32_bf16 v[148:151], a[168:171], a[60:63], v[148:151]\n"
"s_waitcnt lgkmcnt(3)\n"
"s_add_u32 s66, s78, 0x2d000\n"
"ds_read_b64_tr_b16 a[246:247], v201 offset:13024\n"
"v_mfma_f32_16x16x32_bf16 v[156:159], a[172:175], a[60:63], v[156:159]\n"
"s_add_u32 m0, s59, 0x22080\n"
"ds_read_b64_tr_b16 a[248:249], v201 offset:16896\n"
"v_mfma_f32_16x16x32_bf16 v[152:155], a[172:175], a[56:59], v[152:155]\n"
"s_waitcnt lgkmcnt(3)\n"
"ds_read_b64_tr_b16 a[250:251], v201 offset:17152\n"
"v_mfma_f32_16x16x32_bf16 v[160:163], a[240:243], a[56:59], v[160:163]\n"
"ds_read_b64_tr_b16 a[252:253], v201 offset:16928\n"
"v_mfma_f32_16x16x32_bf16 v[164:167], a[240:243], a[60:63], v[164:167]\n"
"s_waitcnt lgkmcnt(3)\n"
"ds_read_b64_tr_b16 a[254:255], v201 offset:17184\n"
"buffer_load_dwordx4 a[104:107], v139, s[44:47], s94 offen offset:0 sc0 sc1\n"
"v_mfma_f32_16x16x32_bf16 v[172:175], a[244:247], a[60:63], v[172:175]\n"
"s_add_u32 s94, s61, 0x800000\n"
"ds_read_b64_tr_b16 v[176:177], v201 offset:16960\n"
"v_mfma_f32_16x16x32_bf16 v[168:171], a[244:247], a[56:59], v[168:171]\n"
"s_waitcnt vmcnt(18) lgkmcnt(3)\n"
"ds_read_b64_tr_b16 v[178:179], v201 offset:17216\n"
"v_mfma_f32_16x16x32_bf16 v[96:99], a[248:251], a[64:67], v[96:99]\n"
"s_waitcnt vmcnt(14)\n"
"ds_read_b64_tr_b16 v[180:181], v201 offset:16992\n"
"v_mfma_f32_16x16x32_bf16 v[100:103], a[248:251], a[68:71], v[100:103]\n"
"s_waitcnt lgkmcnt(3)\n"
"ds_read_b64_tr_b16 v[182:183], v201 offset:17248\n"
"v_mfma_f32_16x16x32_bf16 v[108:111], a[252:255], a[68:71], v[108:111]\n"
"ds_read_b64_tr_b16 v[184:185], v201 offset:21120\n"
"buffer_store_dwordx2 v[236:237], v142, s[44:47], s17 offen offset:768 sc0 sc1\n"
"v_mfma_f32_16x16x32_bf16 v[104:107], a[252:255], a[64:67], v[104:107]\n"
"s_waitcnt lgkmcnt(3)\n"
"ds_read_b64_tr_b16 v[186:187], v201 offset:21376\n"
"v_mfma_f32_16x16x32_bf16 v[112:115], v[176:179], a[64:67], v[112:115]\n"
"ds_read_b64_tr_b16 v[188:189], v201 offset:21152\n"
"buffer_load_dwordx4 a[108:111], v139, s[44:47], s94 offen offset:0 sc0 sc1\n"
"v_mfma_f32_16x16x32_bf16 v[116:119], v[176:179], a[68:71], v[116:119]\n"
"s_waitcnt lgkmcnt(3)\n"
"s_add_u32 s94, s85, 0x801000\n"
"ds_read_b64_tr_b16 v[190:191], v201 offset:21408\n"
"v_mfma_f32_16x16x32_bf16 v[124:127], v[180:183], a[68:71], v[124:127]\n"
"s_barrier\n"
"ds_read_b64_tr_b16 v[192:193], v201 offset:21184\n"
"v_mfma_f32_16x16x32_bf16 v[120:123], v[180:183], a[64:67], v[120:123]\n"
"s_waitcnt lgkmcnt(3)\n"
"ds_read_b64_tr_b16 v[194:195], v201 offset:21440\n"
"v_mfma_f32_16x16x32_bf16 v[144:147], v[184:187], a[64:67], v[144:147]\n"
"ds_read_b64_tr_b16 v[196:197], v201 offset:21216\n"
"v_mfma_f32_16x16x32_bf16 v[148:151], v[184:187], a[68:71], v[148:151]\n"
"s_waitcnt lgkmcnt(3)\n"
"ds_read_b64_tr_b16 v[198:199], v201 offset:21472\n"
"buffer_load_dwordx4 v211, s[24:27], s66 offen lds\n"
"v_mfma_f32_16x16x32_bf16 v[156:159], v[188:191], a[68:71], v[156:159]\n"
"s_add_u32 s66, s78, 0x2d000\n"
"s_add_u32 m0, s59, 0x7380\n"
"ds_read_b64_tr_b16 a[168:169], v201 offset:33792\n"
"v_mfma_f32_16x16x32_bf16 v[152:155], v[188:191], a[64:67], v[152:155]\n"
"s_waitcnt lgkmcnt(3)\n"
"ds_read_b64_tr_b16 a[170:171], v201 offset:34048\n"
"v_mfma_f32_16x16x32_bf16 v[160:163], v[192:195], a[64:67], v[160:163]\n"
"ds_read_b64_tr_b16 a[172:173], v201 offset:33824\n"
"v_mfma_f32_16x16x32_bf16 v[164:167], v[192:195], a[68:71], v[164:167]\n"
"s_waitcnt lgkmcnt(3)\n"
"ds_read_b64_tr_b16 a[174:175], v201 offset:34080\n"
"v_mfma_f32_16x16x32_bf16 v[172:175], v[196:199], a[68:71], v[172:175]\n"
"ds_read_b64_tr_b16 a[240:241], v201 offset:33856\n"
"buffer_load_dwordx4 a[112:115], v139, s[44:47], s94 offen offset:0 sc0 sc1\n"
"v_mfma_f32_16x16x32_bf16 v[168:171], v[196:199], a[64:67], v[168:171]\n"
"s_waitcnt vmcnt(16) lgkmcnt(3)\n"
"s_add_u32 s94, s61, 0x801000\n"
"ds_read_b64_tr_b16 a[242:243], v201 offset:34112\n"
"v_mfma_f32_16x16x32_bf16 v[96:99], a[168:171], a[72:75], v[96:99]\n"
"s_waitcnt vmcnt(14)\n"
"ds_read_b64_tr_b16 a[244:245], v201 offset:33888\n"
"v_mfma_f32_16x16x32_bf16 v[100:103], a[168:171], a[76:79], v[100:103]\n"
"s_waitcnt lgkmcnt(3)\n"
"ds_read_b64_tr_b16 a[246:247], v201 offset:34144\n"
"v_mfma_f32_16x16x32_bf16 v[108:111], a[172:175], a[76:79], v[108:111]\n"
"ds_read_b64_tr_b16 a[248:249], v201 offset:38016\n"
"v_mfma_f32_16x16x32_bf16 v[104:107], a[172:175], a[72:75], v[104:107]\n"
"s_waitcnt lgkmcnt(3)\n"
"ds_read_b64_tr_b16 a[250:251], v201 offset:38272\n"
"buffer_load_dwordx4 a[116:119], v139, s[44:47], s94 offen offset:0 sc0 sc1\n"
"v_mfma_f32_16x16x32_bf16 v[112:115], a[240:243], a[72:75], v[112:115]\n"
"s_add_u32 s94, s85, 0xc00000\n"
"ds_read_b64_tr_b16 a[252:253], v201 offset:38048\n"
"ds_read_b64_tr_b16 a[254:255], v201 offset:38304\n"
"v_mfma_f32_16x16x32_bf16 v[116:119], a[240:243], a[76:79], v[116:119]\n"
"s_waitcnt lgkmcnt(4)\n"
"ds_read_b64_tr_b16 v[176:177], v201 offset:38080\n"
"ds_read_b64_tr_b16 v[178:179], v201 offset:38336\n"
"v_mfma_f32_16x16x32_bf16 v[124:127], a[244:247], a[76:79], v[124:127]\n"
"ds_read_b64_tr_b16 v[180:181], v201 offset:38112\n"
"v_mfma_f32_16x16x32_bf16 v[120:123], a[244:247], a[72:75], v[120:123]\n"
"s_waitcnt lgkmcnt(5)\n"
"ds_read_b64_tr_b16 v[182:183], v201 offset:38368\n"
"v_mfma_f32_16x16x32_bf16 v[144:147], a[248:251], a[72:75], v[144:147]\n"
"ds_read_b64_tr_b16 v[184:185], v202 offset:16896\n"
"buffer_load_dwordx4 v211, s[28:31], s66 offen lds\n"
"v_mfma_f32_16x16x32_bf16 v[148:151], a[248:251], a[76:79], v[148:151]\n"
"s_waitcnt lgkmcnt(5)\n"
"s_add_u32 s66, s63, 0x36000\n"
"ds_read_b64_tr_b16 v[186:187], v202 offset:17152\n"
"v_mfma_f32_16x16x32_bf16 v[156:159], a[252:255], a[76:79], v[156:159]\n"
"s_add_u32 m0, s59, 0x8400\n"
"ds_read_b64_tr_b16 v[188:189], v202 offset:16928\n"
"v_mfma_f32_16x16x32_bf16 v[152:155], a[252:255], a[72:75], v[152:155]\n"
"s_waitcnt lgkmcnt(5)\n"
"ds_read_b64_tr_b16 v[190:191], v202 offset:17184\n"
"v_mfma_f32_16x16x32_bf16 v[160:163], v[176:179], a[72:75], v[160:163]\n"
"ds_read_b64_tr_b16 v[192:193], v202 offset:16960\n"
"v_mfma_f32_16x16x32_bf16 v[164:167], v[176:179], a[76:79], v[164:167]\n"
"s_waitcnt lgkmcnt(5)\n"
"ds_read_b64_tr_b16 v[194:195], v202 offset:17216\n"
"buffer_load_dwordx4 v211, s[24:27], s66 offen lds\n"
"v_mfma_f32_16x16x32_bf16 v[172:175], v[180:183], a[76:79], v[172:175]\n"
"s_add_u32 s66, s63, 0x36000\n"
"s_add_u32 m0, s59, 0x9480\n"
"ds_read_b128 a[136:139], v214 offset:42240\n"
"v_mfma_f32_16x16x32_bf16 v[168:171], v[180:183], a[72:75], v[168:171]\n"
"s_waitcnt vmcnt(15) lgkmcnt(5)\n"
"ds_read_b128 a[140:143], v208 offset:42240\n"
"v_mfma_f32_16x16x32_bf16 v[96:99], v[184:187], a[80:83], v[96:99]\n"
"s_waitcnt vmcnt(13)\n"
"ds_read_b64_tr_b16 v[196:197], v202 offset:16992\n"
"v_mfma_f32_16x16x32_bf16 v[100:103], v[184:187], a[84:87], v[100:103]\n"
"L_st10_%=:\n s_cbranch_execz L_ret_%=\n"
"s_waitcnt lgkmcnt(5)\n"
"ds_read_b64_tr_b16 v[198:199], v202 offset:17248\n"
"v_mfma_f32_16x16x32_bf16 v[108:111], v[188:191], a[84:87], v[108:111]\n"
"ds_read_b64_tr_b16 a[168:169], v202 offset:25344\n"
"buffer_load_dwordx4 v211, s[28:31], s66 offen lds\n"
"v_mfma_f32_16x16x32_bf16 v[104:107], v[188:191], a[80:83], v[104:107]\n"
"s_waitcnt lgkmcnt(5)\n"
"s_add_u32 s66, s63, 0x3f000\n"
"ds_read_b64_tr_b16 a[170:171], v202 offset:25600\n"
"v_mfma_f32_16x16x32_bf16 v[112:115], v[192:195], a[80:83], v[112:115]\n"
"s_add_u32 m0, s59, 0xa500\n"
"ds_read_b64_tr_b16 a[172:173], v202 offset:25376\n"
"v_mfma_f32_16x16x32_bf16 v[116:119], v[192:195], a[84:87], v[116:119]\n"
"s_waitcnt lgkmcnt(3)\n"
"ds_read_b64_tr_b16 a[174:175], v202 offset:25632\n"
"v_mfma_f32_16x16x32_bf16 v[124:127], v[196:199], a[84:87], v[124:127]\n"
"s_barrier\n"
"ds_read_b64_tr_b16 a[240:241], v202 offset:25408\n"
"v_mfma_f32_16x16x32_bf16 v[120:123], v[196:199], a[80:83], v[120:123]\n"
"s_waitcnt lgkmcnt(3)\n"
"ds_read_b64_tr_b16 a[242:243], v202 offset:25664\n"
"buffer_load_dwordx4 v211, s[24:27], s66 offen lds\n"
"v_mfma_f32_16x16x32_bf16 v[144:147], a[168:171], a[80:83], v[144:147]\n"
"s_add_u32 s66, s63, 0x3f000\n"
"s_add_u32 m0, s59, 0xb580\n"
"ds_read_b64_tr_b16 a[244:245], v202 offset:25440\n"
"v_mfma_f32_16x16x32_bf16 v[148:151], a[168:171], a[84:87], v[148:151]\n"
"s_waitcnt lgkmcnt(3)\n"
"ds_read_b64_tr_b16 a[246:247], v202 offset:25696\n"
"v_mfma_f32_16x16x32_bf16 v[156:159], a[172:175], a[84:87], v[156:159]\n"
"ds_read_b64_tr_b16 a[248:249], v202 offset:29568\n"
"v_mfma_f32_16x16x32_bf16 v[152:155], a[172:175], a[80:83], v[152:155]\n"
"s_waitcnt lgkmcnt(3)\n"
"ds_read_b64_tr_b16 a[250:251], v202 offset:29824\n"
"v_mfma_f32_16x16x32_bf16 v[160:163], a[240:243], a[80:83], v[160:163]\n"
"ds_read_b64_tr_b16 a[252:253], v202 offset:29600\n"
"buffer_load_dwordx4 v211, s[28:31], s66 offen lds\n"
"v_mfma_f32_16x16x32_bf16 v[164:167], a[240:243], a[84:87], v[164:167]\n"
"s_waitcnt lgkmcnt(3)\n"
"s_add_u32 s66, s64, 0\n"
"ds_read_b64_tr_b16 a[254:255], v202 offset:29856\n"
"v_mfma_f32_16x16x32_bf16 v[172:175], a[244:247], a[84:87], v[172:175]\n"
"s_add_u32 m0, s59, 0xc600\n"
"ds_read_b64_tr_b16 v[176:177], v202 offset:29632\n"
"v_mfma_f32_16x16x32_bf16 v[168:171], a[244:247], a[80:83], v[168:171]\n"
"s_waitcnt vmcnt(15) lgkmcnt(3)\n"
"ds_read_b64_tr_b16 v[178:179], v202 offset:29888\n"
"v_mfma_f32_16x16x32_bf16 v[96:99], a[248:251], a[88:91], v[96:99]\n"
"s_waitcnt vmcnt(14)\n"
"ds_read_b64_tr_b16 v[180:181], v202 offset:29664\n"
"v_mfma_f32_16x16x32_bf16 v[100:103], a[248:251], a[92:95], v[100:103]\n"
"s_waitcnt lgkmcnt(3)\n"
"ds_read_b64_tr_b16 v[182:183], v202 offset:29920\n"
"buffer_load_dwordx4 v211, s[24:27], s66 offen lds\n"
"v_mfma_f32_16x16x32_bf16 v[108:111], a[252:255], a[92:95], v[108:111]\n"
"s_add_u32 s66, s64, 0\n"
"s_add_u32 m0, s59, 0xd680\n"
"ds_read_b64_tr_b16 v[184:185], v202 offset:33792\n"
"v_mfma_f32_16x16x32_bf16 v[104:107], a[252:255], a[88:91], v[104:107]\n"
"s_waitcnt lgkmcnt(3)\n"
"ds_read_b64_tr_b16 v[186:187], v202 offset:34048\n"
"v_mfma_f32_16x16x32_bf16 v[112:115], v[176:179], a[88:91], v[112:115]\n"
"ds_read_b64_tr_b16 v[188:189], v202 offset:33824\n"
"v_mfma_f32_16x16x32_bf16 v[116:119], v[176:179], a[92:95], v[116:119]\n"
"s_waitcnt lgkmcnt(3)\n"
"ds_read_b64_tr_b16 v[190:191], v202 offset:34080\n"
"v_mfma_f32_16x16x32_bf16 v[124:127], v[180:183], a[92:95], v[124:127]\n"
"ds_read_b64_tr_b16 v[192:193], v202 offset:33856\n"
"buffer_load_dwordx4 v211, s[28:31], s66 offen lds\n"
"v_mfma_f32_16x16x32_bf16 v[120:123], v[180:183], a[88:91], v[120:123]\n"
"s_waitcnt lgkmcnt(3)\n"
"s_add_u32 s66, s78, 0x36000\n"
"ds_read_b64_tr_b16 v[194:195], v202 offset:34112\n"
"v_mfma_f32_16x16x32_bf16 v[144:147], v[184:187], a[88:91], v[144:147]\n"
"s_add_u32 m0, s59, 0xe700\n"
"ds_read_b64_tr_b16 v[196:197], v202 offset:33888\n"
"v_mfma_f32_16x16x32_bf16 v[148:151], v[184:187], a[92:95], v[148:151]\n"
"s_waitcnt lgkmcnt(3)\n"
"ds_read_b64_tr_b16 v[198:199], v202 offset:34144\n"
"v_mfma_f32_16x16x32_bf16 v[156:159], v[188:191], a[92:95], v[156:159]\n"
"ds_read_b64_tr_b16 a[168:169], v202 offset:38016\n"
"v_mfma_f32_16x16x32_bf16 v[152:155], v[188:191], a[88:91], v[152:155]\n"
"s_waitcnt lgkmcnt(3)\n"
"ds_read_b64_tr_b16 a[170:171], v202 offset:38272\n"
"s_mov_b64 exec, 0x300000\n buffer_load_dwordx2 v[230:231], v138, s[44:47], s16 offen offset:0 sc0 sc1\n s_mov_b64 exec, -1\n"
"v_mfma_f32_16x16x32_bf16 v[160:163], v[192:195], a[88:91], v[160:163]\n"
"ds_read_b64_tr_b16 a[172:173], v202 offset:38048\n"
"v_mfma_f32_16x16x32_bf16 v[164:167], v[192:195], a[92:95], v[164:167]\n"
"s_waitcnt lgkmcnt(3)\n"
"ds_read_b64_tr_b16 a[174:175], v202 offset:38304\n"
"v_mfma_f32_16x16x32_bf16 v[172:175], v[196:199], a[92:95], v[172:175]\n"
"ds_read_b64_tr_b16 a[240:241], v202 offset:38080\n"
"buffer_load_dwordx4 v211, s[24:27], s66 offen lds\n"
"v_mfma_f32_16x16x32_bf16 v[168:171], v[196:199], a[88:91], v[168:171]\n"
"s_waitcnt vmcnt(17) lgkmcnt(3)\n"
"s_add_u32 s66, s78, 0x36000\n"
"ds_read_b64_tr_b16 a[242:243], v202 offset:38336\n"
"v_mfma_f32_16x16x32_bf16 v[96:99], a[168:171], a[96:99], v[96:99]\n"
"s_waitcnt vmcnt(16)\n"
"s_add_u32 m0, s59, 0\n"
"ds_read_b64_tr_b16 a[244:245], v202 offset:38112\n"
"v_mfma_f32_16x16x32_bf16 v[100:103], a[168:171], a[100:103], v[100:103]\n"
"s_waitcnt lgkmcnt(3)\n"
"ds_read_b64_tr_b16 a[246:247], v202 offset:38368\n"
"v_mfma_f32_16x16x32_bf16 v[108:111], a[172:175], a[100:103], v[108:111]\n"
"ds_read_b64_tr_b16 a[248:249], v200 offset:16896\n"
"v_mfma_f32_16x16x32_bf16 v[104:107], a[172:175], a[96:99], v[104:107]\n"
"s_waitcnt lgkmcnt(3)\n"
"ds_read_b64_tr_b16 a[250:251], v200 offset:17152\n"
"buffer_load_dwordx4 v211, s[28:31], s66 offen lds\n"
"v_mfma_f32_16x16x32_bf16 v[112:115], a[240:243], a[96:99], v[112:115]\n"
"s_add_u32 s66, s78, 0x3f000\n"
"s_add_u32 m0, s59, 0xf780\n"
"ds_read_b64_tr_b16 a[252:253], v200 offset:16928\n"
"v_mfma_f32_16x16x32_bf16 v[116:119], a[240:243], a[100:103], v[116:119]\n"
"s_waitcnt vmcnt(9) lgkmcnt(3)\n"
"ds_read_b64_tr_b16 a[254:255], v200 offset:17184\n"
"v_mfma_f32_16x16x32_bf16 v[124:127], a[244:247], a[100:103], v[124:127]\n"
"s_barrier\n"
"ds_read_b64_tr_b16 v[176:177], v200 offset:16960\n"
"v_mfma_f32_16x16x32_bf16 v[120:123], a[244:247], a[96:99], v[120:123]\n"
"s_waitcnt lgkmcnt(3)\n"
"ds_read_b64_tr_b16 v[178:179], v200 offset:17216\n"
"v_mfma_f32_16x16x32_bf16 v[144:147], a[248:251], a[96:99], v[144:147]\n"
"ds_read_b64_tr_b16 v[180:181], v200 offset:16992\n"
"buffer_load_dwordx4 v211, s[24:27], s66 offen lds\n"
"v_mfma_f32_16x16x32_bf16 v[148:151], a[248:251], a[100:103], v[148:151]\n"
"s_waitcnt lgkmcnt(3)\n"
"s_add_u32 s66, s78, 0x3f000\n"
"ds_read_b64_tr_b16 v[182:183], v200 offset:17248\n"
"v_mfma_f32_16x16x32_bf16 v[156:159], a[252:255], a[100:103], v[156:159]\n"
"s_add_u32 m0, s59, 0x14a00\n"
"ds_read_b64_tr_b16 v[184:185], v200 offset:21120\n"
"v_mfma_f32_16x16x32_bf16 v[152:155], a[252:255], a[96:99], v[152:155]\n"
"s_waitcnt lgkmcnt(3)\n"
"ds_read_b64_tr_b16 v[186:187], v200 offset:21376\n"
"v_mfma_f32_16x16x32_bf16 v[160:163], v[176:179], a[96:99], v[160:163]\n"
"ds_read_b64_tr_b16 v[188:189], v200 offset:21152\n"
"v_mfma_f32_16x16x32_bf16 v[164:167], v[176:179], a[100:103], v[164:167]\n"
"s_waitcnt lgkmcnt(3)\n"
"ds_read_b64_tr_b16 v[190:191], v200 offset:21408\n"
"buffer_load_dwordx4 v211, s[28:31], s66 offen lds\n"
"v_mfma_f32_16x16x32_bf16 v[172:175], v[180:183], a[100:103], v[172:175]\n"
"s_add_u32 s66, s63, 0x200\n"
"s_add_u32 m0, s59, 0x15a80\n"
"ds_read_b64_tr_b16 v[192:193], v200 offset:21184\n"
"v_mfma_f32_16x16x32_bf16 v[168:171], v[180:183], a[96:99], v[168:171]\n"
"s_waitcnt lgkmcnt(3)\n"
"ds_read_b64_tr_b16 v[194:195], v200 offset:21440\n"
"v_mfma_f32_16x16x32_bf16 v[96:99], v[184:187], a[104:107], v[96:99]\n"
"ds_read_b64_tr_b16 v[196:197], v200 offset:21216\n"
"v_mfma_f32_16x16x32_bf16 v[100:103], v[184:187], a[108:111], v[100:103]\n"
"s_waitcnt lgkmcnt(3)\n"
"ds_read_b64_tr_b16 v[198:199], v200 offset:21472\n"
"v_mfma_f32_16x16x32_bf16 v[108:111], v[188:191], a[108:111], v[108:111]\n"
"ds_read_b64_tr_b16 a[168:169], v200 offset:25344\n"
"buffer_load_dwordx4 v216, s[36:39], s66 offen lds\n"
"v_mfma_f32_16x16x32_bf16 v[104:107], v[188:191], a[104:107], v[104:107]\n"
"s_waitcnt lgkmcnt(3)\n"
"s_add_u32 s66, s63, 0x9200\n"
"ds_read_b64_tr_b16 a[170:171], v200 offset:25600\n"
"v_mfma_f32_16x16x32_bf16 v[112:115], v[192:195], a[104:107], v[112:115]\n"
"s_add_u32 m0, s59, 0x1080\n"
"ds_read_b64_tr_b16 a[172:173], v200 offset:25376\n"
"v_mfma_f32_16x16x32_bf16 v[116:119], v[192:195], a[108:111], v[116:119]\n"
"s_waitcnt lgkmcnt(3)\n"
"ds_read_b64_tr_b16 a[174:175], v200 offset:25632\n"
"v_mfma_f32_16x16x32_bf16 v[124:127], v[196:199], a[108:111], v[124:127]\n"
"ds_read_b64_tr_b16 a[240:241], v200 offset:25408\n"
"buffer_load_dwordx4 v216, s[36:39], s66 offen lds\n"
"v_mfma_f32_16x16x32_bf16 v[120:123], v[196:199], a[104:107], v[120:123]\n"
"s_waitcnt lgkmcnt(3)\n"
"s_add_u32 s66, s65, 0x200\n"
"ds_read_b64_tr_b16 a[242:243], v200 offset:25664\n"
"v_mfma_f32_16x16x32_bf16 v[144:147], a[168:171], a[104:107], v[144:147]\n"
"s_add_u32 m0, s59, 0x2100\n"
"ds_read_b64_tr_b16 a[244:245], v200 offset:25440\n"
"v_mfma_f32_16x16x32_bf16 v[148:151], a[168:171], a[108:111], v[148:151]\n"
"s_waitcnt lgkmcnt(3)\n"
"ds_read_b64_tr_b16 a[246:247], v200 offset:25696\n"
"v_mfma_f32_16x16x32_bf16 v[156:159], a[172:175], a[108:111], v[156:159]\n"
"ds_read_b64_tr_b16 a[248:249], v202 offset:21120\n"
"buffer_load_dwordx4 v217, s[40:43], s66 offen lds\n"
"v_mfma_f32_16x16x32_bf16 v[152:155], a[172:175], a[104:107], v[152:155]\n"
"s_waitcnt lgkmcnt(3)\n"
"s_add_u32 s66, s63, 0x12200\n"
"ds_read_b64_tr_b16 a[250:251], v202 offset:21376\n"
"v_mfma_f32_16x16x32_bf16 v[160:163], a[240:243], a[104:107], v[160:163]\n"
"s_add_u32 m0, s59, 0x1ad00\n"
"ds_read_b64_tr_b16 a[252:253], v202 offset:21152\n"
"v_mfma_f32_16x16x32_bf16 v[164:167], a[240:243], a[108:111], v[164:167]\n"
"s_waitcnt lgkmcnt(3)\n"
"ds_read_b64_tr_b16 a[254:255], v202 offset:21408\n"
"v_mfma_f32_16x16x32_bf16 v[172:175], a[244:247], a[108:111], v[172:175]\n"
"ds_read_b64_tr_b16 v[176:177], v202 offset:21184\n"
"buffer_load_dwordx4 v216, s[36:39], s66 offen lds\n"
"v_mfma_f32_16x16x32_bf16 v[168:171], a[244:247], a[104:107], v[168:171]\n"
"s_waitcnt lgkmcnt(3)\n"
"s_add_u32 s66, s63, 0x1b200\n"
"ds_read_b64_tr_b16 v[178:179], v202 offset:21440\n"
"v_mfma_f32_16x16x32_bf16 v[96:99], a[248:251], a[112:115], v[96:99]\n"
"s_add_u32 m0, s59, 0x1bd80\n"
"ds_read_b64_tr_b16 v[180:181], v202 offset:21216\n"
"v_mfma_f32_16x16x32_bf16 v[100:103], a[248:251], a[116:119], v[100:103]\n"
"s_waitcnt lgkmcnt(3)\n"
"ds_read_b64_tr_b16 v[182:183], v202 offset:21472\n"
"v_mfma_f32_16x16x32_bf16 v[108:111], a[252:255], a[116:119], v[108:111]\n"
"ds_read_b64_tr_b16 v[184:185], v200 offset:29568\n"
"buffer_load_dwordx4 v216, s[36:39], s66 offen lds\n"
"v_mfma_f32_16x16x32_bf16 v[104:107], a[252:255], a[112:115], v[104:107]\n"
"s_waitcnt lgkmcnt(3)\n"
"s_add_u32 s66, s63, 0x24200\n"
"ds_read_b64_tr_b16 v[186:187], v200 offset:29824\n"
"v_mfma_f32_16x16x32_bf16 v[112:115], v[176:179], a[112:115], v[112:115]\n"
"s_add_u32 m0, s59, 0x1ce00\n"
"ds_read_b64_tr_b16 v[188:189], v200 offset:29600\n"
"v_mfma_f32_16x16x32_bf16 v[116:119], v[176:179], a[116:119], v[116:119]\n"
"s_waitcnt vmcnt(12) lgkmcnt(3)\n"
"ds_read_b64_tr_b16 v[190:191], v200 offset:29856\n"
"v_mfma_f32_16x16x32_bf16 v[124:127], v[180:183], a[116:119], v[124:127]\n"
"s_barrier\n"
"ds_read_b64_tr_b16 v[192:193], v200 offset:29632\n"
"buffer_load_dwordx4 v216, s[36:39], s66 offen lds\n"
"v_mfma_f32_16x16x32_bf16 v[120:123], v[180:183], a[112:115], v[120:123]\n"
"s_waitcnt lgkmcnt(3)\n"
"s_add_u32 s66, s79, 0\n"
"ds_read_b64_tr_b16 v[194:195], v200 offset:29888\n"
"v_mfma_f32_16x16x32_bf16 v[144:147], v[184:187], a[112:115], v[144:147]\n"
"s_add_u32 m0, s59, 0x1de80\n"
"ds_read_b64_tr_b16 v[196:197], v200 offset:29664\n"
"v_mfma_f32_16x16x32_bf16 v[148:151], v[184:187], a[116:119], v[148:151]\n"
"s_waitcnt lgkmcnt(3)\n"
"ds_read_b64_tr_b16 v[198:199], v200 offset:29920\n"
"v_mfma_f32_16x16x32_bf16 v[156:159], v[188:191], a[116:119], v[156:159]\n"
"ds_read_b64_tr_b16 a[168:169], v200 offset:33792\n"
"buffer_load_dwordx4 v211, s[24:27], s66 offen lds\n"
"v_mfma_f32_16x16x32_bf16 v[152:155], v[188:191], a[112:115], v[152:155]\n"
"s_waitcnt lgkmcnt(3)\n"
"s_add_u32 s66, s79, 0\n"
"ds_read_b64_tr_b16 a[170:171], v200 offset:34048\n"
"v_mfma_f32_16x16x32_bf16 v[160:163], v[192:195], a[112:115], v[160:163]\n"
"s_add_u32 m0, s59, 0x1ef00\n"
"s_add_u32 s24, s36, s76\n s_addc_u32 s25, s37, 0\n"
"ds_read_b64_tr_b16 a[172:173], v200 offset:33824\n"
"v_mfma_f32_16x16x32_bf16 v[164:167], v[192:195], a[116:119], v[164:167]\n"
"s_waitcnt vmcnt(11) lgkmcnt(3)\n"
"s_sub_u32 s26, s38, s76\n"
"ds_read_b64_tr_b16 a[174:175], v200 offset:34080\n"
"v_xor_b32 v239, v230, v231\n"
"v_subrev_u32 v238, s18, v230\n"
"v_xor_b32 v239, 0x5bd1e995, v239\n"
"v_max_u32 v238, v238, v239\n"
"v_cmp_ge_u32 s[10:11], v228, v238\n"
"v_mfma_f32_16x16x32_bf16 v[172:175], v[196:199], a[116:119], v[172:175]\n"
"s_andn2_b32 s10, 0x300000, s10\n s_cbranch_scc1 L_slow2_%=\n L_back2_%=:\n"
"ds_read_b64_tr_b16 a[240:241], v200 offset:33856\n"
"v_mfma_f32_16x16x32_bf16 v[168:171], v[196:199], a[112:115], v[168:171]\n"
"s_waitcnt lgkmcnt(3)\n"
"ds_read_b64_tr_b16 a[242:243], v200 offset:34112\n"
"v_mfma_f32_16x16x32_bf16 v[96:99], a[168:171], a[120:123], v[96:99]\n"
"ds_read_b64_tr_b16 a[244:245], v200 offset:33888\n"
"buffer_load_dwordx4 a[144:147], v139, s[44:47], s94 offen offset:0 sc0 sc1\n"
"v_mfma_f32_16x16x32_bf16 v[100:103], a[168:171], a[124:127], v[100:103]\n"
"s_waitcnt lgkmcnt(3)\n"
"s_add_u32 s94, s61, 0xc00000\n"
"ds_read_b64_tr_b16 a[246:247], v200 offset:34144\n"
"v_mfma_f32_16x16x32_bf16 v[108:111], a[172:175], a[124:127], v[108:111]\n"
"ds_read_b64_tr_b16 a[248:249], v200 offset:38016\n"
"v_mfma_f32_16x16x32_bf16 v[104:107], a[172:175], a[120:123], v[104:107]\n"
"s_waitcnt lgkmcnt(3)\n"
"ds_read_b64_tr_b16 a[250:251], v200 offset:38272\n"
"v_mfma_f32_16x16x32_bf16 v[112:115], a[240:243], a[120:123], v[112:115]\n"
"ds_read_b64_tr_b16 a[252:253], v200 offset:38048\n"
"v_mfma_f32_16x16x32_bf16 v[116:119], a[240:243], a[124:127], v[116:119]\n"
"s_waitcnt lgkmcnt(3)\n"
"ds_read_b64_tr_b16 a[254:255], v200 offset:38304\n"
"buffer_load_dwordx4 a[148:151], v139, s[44:47], s94 offen offset:0 sc0 sc1\n"
"v_mfma_f32_16x16x32_bf16 v[124:127], a[244:247], a[124:127], v[124:127]\n"
"s_add_u32 s94, s85, 0xc01000\n"
"ds_read_b64_tr_b16 v[176:177], v200 offset:38080\n"
"v_mfma_f32_16x16x32_bf16 v[120:123], a[244:247], a[120:123], v[120:123]\n"
"s_waitcnt lgkmcnt(3)\n"
"ds_read_b64_tr_b16 v[178:179], v200 offset:38336\n"
"v_mfma_f32_16x16x32_bf16 v[144:147], a[248:251], a[120:123], v[144:147]\n"
"ds_read_b64_tr_b16 v[180:181], v200 offset:38112\n"
"v_mfma_f32_16x16x32_bf16 v[148:151], a[248:251], a[124:127], v[148:151]\n"
"s_waitcnt lgkmcnt(3)\n"
"ds_read_b64_tr_b16 v[182:183], v200 offset:38368\n"
"v_mfma_f32_16x16x32_bf16 v[156:159], a[252:255], a[124:127], v[156:159]\n"
"ds_read_b64_tr_b16 v[184:185], v200 offset:42240\n"
"v_mfma_f32_16x16x32_bf16 v[152:155], a[252:255], a[120:123], v[152:155]\n"
"s_waitcnt lgkmcnt(3)\n"
"ds_read_b64_tr_b16 v[186:187], v200 offset:42496\n"
"s_mov_b64 exec, 0xc00000\n buffer_load_dwordx2 v[230:231], v138, s[44:47], s16 offen offset:0 sc0 sc1\n s_mov_b64 exec, -1\n"
"v_mfma_f32_16x16x32_bf16 v[160:163], v[176:179], a[120:123], v[160:163]\n"
"ds_read_b64_tr_b16 v[188:189], v200 offset:42272\n"
"v_mfma_f32_16x16x32_bf16 v[164:167], v[176:179], a[124:127], v[164:167]\n"
"s_waitcnt lgkmcnt(3)\n"
"ds_read_b64_tr_b16 v[190:191], v200 offset:42528\n"
"v_mfma_f32_16x16x32_bf16 v[172:175], v[180:183], a[124:127], v[172:175]\n"
"ds_read_b64_tr_b16 v[192:193], v200 offset:42304\n"
"buffer_load_dwordx4 a[152:155], v139, s[44:47], s94 offen offset:0 sc0 sc1\n"
"v_mfma_f32_16x16x32_bf16 v[168:171], v[180:183], a[120:123], v[168:171]\n"
"s_waitcnt lgkmcnt(3)\n"
"s_add_u32 s94, s61, 0xc01000\n"
"ds_read_b64_tr_b16 v[194:195], v200 offset:42560\n"
"v_mfma_f32_16x16x32_bf16 v[96:99], v[184:187], a[128:131], v[96:99]\n"
"ds_read_b64_tr_b16 v[196:197], v200 offset:42336\n"
"v_mfma_f32_16x16x32_bf16 v[100:103], v[184:187], a[132:135], v[100:103]\n"
"s_waitcnt lgkmcnt(3)\n"
"ds_read_b64_tr_b16 v[198:199], v200 offset:42592\n"
"v_mfma_f32_16x16x32_bf16 v[108:111], v[188:191], a[132:135], v[108:111]\n"
"ds_read_b64_tr_b16 a[168:169], v200 offset:46464\n"
"v_mfma_f32_16x16x32_bf16 v[104:107], v[188:191], a[128:131], v[104:107]\n"
"s_waitcnt lgkmcnt(3)\n"
"ds_read_b64_tr_b16 a[170:171], v200 offset:46720\n"
"buffer_load_dwordx4 a[156:159], v139, s[44:47], s94 offen offset:0 sc0 sc1\n"
"v_mfma_f32_16x16x32_bf16 v[112:115], v[192:195], a[128:131], v[112:115]\n"
"s_add_u32 s94, s85, 0x1400000\n"
"ds_read_b64_tr_b16 a[172:173], v200 offset:46496\n"
"v_mfma_f32_16x16x32_bf16 v[116:119], v[192:195], a[132:135], v[116:119]\n"
"s_waitcnt vmcnt(14) lgkmcnt(3)\n"
"ds_read_b64_tr_b16 a[174:175], v200 offset:46752\n"
"v_mfma_f32_16x16x32_bf16 v[124:127], v[196:199], a[132:135], v[124:127]\n"
"s_barrier\n"
"ds_read_b64_tr_b16 a[240:241], v200 offset:46528\n"
"v_mfma_f32_16x16x32_bf16 v[120:123], v[196:199], a[128:131], v[120:123]\n"
"s_waitcnt lgkmcnt(3)\n"
"ds_read_b64_tr_b16 a[242:243], v200 offset:46784\n"
"v_mfma_f32_16x16x32_bf16 v[144:147], a[168:171], a[128:131], v[144:147]\n"
"ds_read_b64_tr_b16 a[244:245], v200 offset:46560\n"
"buffer_load_dwordx4 v211, s[28:31], s66 offen lds\n"
"v_mfma_f32_16x16x32_bf16 v[148:151], a[168:171], a[132:135], v[148:151]\n"
"s_waitcnt lgkmcnt(3)\n"
"s_add_u32 s66, s63, 0x2d200\n"
"ds_read_b64_tr_b16 a[246:247], v200 offset:46816\n"
"v_mfma_f32_16x16x32_bf16 v[156:159], a[172:175], a[132:135], v[156:159]\n"
"s_add_u32 m0, s59, 0x1ff80\n"
"ds_read_b64_tr_b16 a[248:249], v200 offset:50688\n"
"v_mfma_f32_16x16x32_bf16 v[152:155], a[172:175], a[128:131], v[152:155]\n"
"s_waitcnt lgkmcnt(3)\n"
"ds_read_b64_tr_b16 a[250:251], v200 offset:50944\n"
"v_mfma_f32_16x16x32_bf16 v[160:163], a[240:243], a[128:131], v[160:163]\n"
"ds_read_b64_tr_b16 a[252:253], v200 offset:50720\n"
"v_mfma_f32_16x16x32_bf16 v[164:167], a[240:243], a[132:135], v[164:167]\n"
"s_waitcnt lgkmcnt(3)\n"
"ds_read_b64_tr_b16 a[254:255], v200 offset:50976\n"
"buffer_load_dwordx4 v216, s[36:39], s66 offen lds\n"
"v_mfma_f32_16x16x32_bf16 v[172:175], a[244:247], a[132:135], v[172:175]\n"
"s_add_u32 s66, s78, 0x200\n"
"s_add_u32 m0, s59, 0x3180\n"
"ds_read_b64_tr_b16 v[176:177], v200 offset:50752\n"
"v_mfma_f32_16x16x32_bf16 v[168:171], a[244:247], a[128:131], v[168:171]\n"
"s_waitcnt lgkmcnt(3)\n"
"ds_read_b64_tr_b16 v[178:179], v200 offset:51008\n"
"v_mfma_f32_16x16x32_bf16 v[96:99], a[248:251], a[136:139], v[96:99]\n"
"ds_read_b64_tr_b16 v[180:181], v200 offset:50784\n"
"buffer_load_dwordx4 v216, s[36:39], s66 offen lds\n"
"v_mfma_f32_16x16x32_bf16 v[100:103], a[248:251], a[140:143], v[100:103]\n"
"s_waitcnt lgkmcnt(3)\n"
"s_add_u32 s66, s78, 0x9200\n"
"ds_read_b64_tr_b16 v[182:183], v200 offset:51040\n"
"v_mfma_f32_16x16x32_bf16 v[108:111], a[252:255], a[140:143], v[108:111]\n"
"s_add_u32 m0, s59, 0x10800\n"
"ds_read_b64_tr_b16 v[184:185], v200 offset:54912\n"
"v_mfma_f32_16x16x32_bf16 v[104:107], a[252:255], a[136:139], v[104:107]\n"
"s_waitcnt lgkmcnt(3)\n"
"ds_read_b64_tr_b16 v[186:187], v200 offset:55168\n"
"v_mfma_f32_16x16x32_bf16 v[112:115], v[176:179], a[136:139], v[112:115]\n"
"ds_read_b64_tr_b16 v[188:189], v200 offset:54944\n"
"buffer_load_dwordx4 v216, s[36:39], s66 offen lds\n"
"v_mfma_f32_16x16x32_bf16 v[116:119], v[176:179], a[140:143], v[116:119]\n"
"s_waitcnt lgkmcnt(3)\n"
"s_add_u32 s66, s80, 0x200\n"
"ds_read_b64_tr_b16 v[190:191], v200 offset:55200\n"
"v_mfma_f32_16x16x32_bf16 v[124:127], v[180:183], a[140:143], v[124:127]\n"
"s_add_u32 m0, s59, 0x11880\n"
"ds_read_b64_tr_b16 v[192:193], v200 offset:54976\n"
"v_mfma_f32_16x16x32_bf16 v[120:123], v[180:183], a[136:139], v[120:123]\n"
"s_waitcnt lgkmcnt(3)\n"
"ds_read_b64_tr_b16 v[194:195], v200 offset:55232\n"
"v_mfma_f32_16x16x32_bf16 v[144:147], v[184:187], a[136:139], v[144:147]\n"
"s_add_u32 s28, s36, s77\n s_addc_u32 s29, s37, 0\n"
"ds_read_b64_tr_b16 v[196:197], v200 offset:55008\n"
"buffer_load_dwordx4 v217, s[40:43], s66 offen lds\n"
"v_mfma_f32_16x16x32_bf16 v[148:151], v[184:187], a[140:143], v[148:151]\n"
"s_waitcnt lgkmcnt(3)\n"
"s_add_u32 s66, s63, 0\n"
"ds_read_b64_tr_b16 v[198:199], v200 offset:55264\n"
"v_mfma_f32_16x16x32_bf16 v[156:159], v[188:191], a[140:143], v[156:159]\n"
"s_add_u32 m0, s59, 0x12900\n"
"s_sub_u32 s30, s38, s77\n"
"ds_read_b64_tr_b16 a[168:169], v201 offset:0\n"
"v_mfma_f32_16x16x32_bf16 v[152:155], v[188:191], a[136:139], v[152:155]\n"
"s_waitcnt lgkmcnt(3)\n"
"ds_read_b64_tr_b16 a[170:171], v201 offset:256\n"
"v_mfma_f32_16x16x32_bf16 v[160:163], v[192:195], a[136:139], v[160:163]\n"
"ds_read_b64_tr_b16 a[172:173], v201 offset:32\n"
"buffer_load_dwordx4 v211, s[24:27], s66 offen lds\n"
"v_mfma_f32_16x16x32_bf16 v[164:167], v[192:195], a[140:143], v[164:167]\n"
"s_waitcnt lgkmcnt(3)\n"
"s_add_u32 s66, s81, 0x200\n"
"ds_read_b64_tr_b16 a[174:175], v201 offset:288\n"
"v_mfma_f32_16x16x32_bf16 v[172:175], v[196:199], a[140:143], v[172:175]\n"
"s_add_u32 m0, s59, 0x13980\n"
"ds_read_b64_tr_b16 a[240:241], v201 offset:64\n"
"v_mfma_f32_16x16x32_bf16 v[168:171], v[196:199], a[136:139], v[168:171]\n"
"s_waitcnt vmcnt(10) lgkmcnt(3)\n"
"ds_read_b64_tr_b16 a[242:243], v201 offset:320\n"
"v_mfma_f32_16x16x32_bf16 v[96:99], a[168:171], a[144:147], v[96:99]\n"
"s_waitcnt vmcnt(9)\n"
"ds_read_b64_tr_b16 a[244:245], v201 offset:96\n"
"v_mfma_f32_16x16x32_bf16 v[100:103], a[168:171], a[148:151], v[100:103]\n"
"s_waitcnt lgkmcnt(3)\n"
"ds_read_b64_tr_b16 a[246:247], v201 offset:352\n"
"buffer_load_dwordx4 v218, s[40:43], s66 offen lds\n"
"v_mfma_f32_16x16x32_bf16 v[108:111], a[172:175], a[148:151], v[108:111]\n"
"s_add_u32 s66, s78, 0x12200\n"
"s_add_u32 m0, s59, 0x16b00\n"
"ds_read_b64_tr_b16 a[248:249], v200 offset:0\n"
"v_mfma_f32_16x16x32_bf16 v[104:107], a[172:175], a[144:147], v[104:107]\n"
"s_waitcnt lgkmcnt(3)\n"
"ds_read_b64_tr_b16 a[250:251], v200 offset:256\n"
"v_mfma_f32_16x16x32_bf16 v[112:115], a[240:243], a[144:147], v[112:115]\n"
"ds_read_b64_tr_b16 a[252:253], v200 offset:32\n"
"buffer_load_dwordx4 v216, s[36:39], s66 offen lds\n"
"v_mfma_f32_16x16x32_bf16 v[116:119], a[240:243], a[148:151], v[116:119]\n"
"s_waitcnt lgkmcnt(3)\n"
"s_add_u32 s66, s78, 0x1b200\n"
"ds_read_b64_tr_b16 a[254:255], v200 offset:288\n"
"v_mfma_f32_16x16x32_bf16 v[124:127], a[244:247], a[148:151], v[124:127]\n"
"s_barrier\n"
"s_add_u32 m0, s59, 0x17b80\n"
"ds_read_b64_tr_b16 v[176:177], v200 offset:64\n"
"v_mfma_f32_16x16x32_bf16 v[120:123], a[244:247], a[144:147], v[120:123]\n"
"s_waitcnt lgkmcnt(3)\n"
"ds_read_b64_tr_b16 v[178:179], v200 offset:320\n"
"v_mfma_f32_16x16x32_bf16 v[144:147], a[248:251], a[144:147], v[144:147]\n"
"ds_read_b64_tr_b16 v[180:181], v200 offset:96\n"
"buffer_load_dwordx4 v216, s[36:39], s66 offen lds\n"
"v_mfma_f32_16x16x32_bf16 v[148:151], a[248:251], a[148:151], v[148:151]\n"
"s_waitcnt lgkmcnt(3)\n"
"s_add_u32 s66, s78, 0x24200\n"
"ds_read_b64_tr_b16 v[182:183], v200 offset:352\n"
"v_mfma_f32_16x16x32_bf16 v[156:159], a[252:255], a[148:151], v[156:159]\n"
"s_add_u32 m0, s59, 0x18c00\n"
"ds_read_b64_tr_b16 v[184:185], v201 offset:4224\n"
"v_mfma_f32_16x16x32_bf16 v[152:155], a[252:255], a[144:147], v[152:155]\n"
"s_waitcnt lgkmcnt(3)\n"
"ds_read_b64_tr_b16 v[186:187], v201 offset:4480\n"
"v_mfma_f32_16x16x32_bf16 v[160:163], v[176:179], a[144:147], v[160:163]\n"
"ds_read_b64_tr_b16 v[188:189], v201 offset:4256\n"
"buffer_load_dwordx4 v216, s[36:39], s66 offen lds\n"
"v_mfma_f32_16x16x32_bf16 v[164:167], v[176:179], a[148:151], v[164:167]\n"
"s_waitcnt vmcnt(12) lgkmcnt(3)\n"
"s_add_u32 s66, s63, 0x9000\n"
"ds_read_b64_tr_b16 v[190:191], v201 offset:4512\n"
"v_xor_b32 v239, v230, v231\n"
"v_subrev_u32 v238, s18, v230\n"
"v_xor_b32 v239, 0x5bd1e995, v239\n"
"v_max_u32 v238, v238, v239\n"
"v_cmp_ge_u32 s[10:11], v228, v238\n"
"v_mfma_f32_16x16x32_bf16 v[172:175], v[180:183], a[148:151], v[172:175]\n"
"s_add_u32 m0, s59, 0x19c80\n"
"s_andn2_b32 s10, 0xc00000, s10\n s_cbranch_scc1 L_slow3_%=\n L_back3_%=:\n"
"ds_read_b64_tr_b16 v[192:193], v201 offset:4288\n"
"v_mfma_f32_16x16x32_bf16 v[168:171], v[180:183], a[144:147], v[168:171]\n"
"s_waitcnt vmcnt(11) lgkmcnt(3)\n"
"ds_read_b64_tr_b16 v[194:195], v201 offset:4544\n"
"v_mfma_f32_16x16x32_bf16 v[96:99], v[184:187], a[152:155], v[96:99]\n"
"s_waitcnt vmcnt(10)\n"
"ds_read_b64_tr_b16 v[196:197], v201 offset:4320\n"
"buffer_load_dwordx2 a[160:161], v140, s[44:47], s94 offen offset:0 sc0 sc1\n"
"v_mfma_f32_16x16x32_bf16 v[100:103], v[184:187], a[156:159], v[100:103]\n"
"s_waitcnt lgkmcnt(3)\n"
"s_add_u32 s94, s85, 0x1401000\n"
"ds_read_b64_tr_b16 v[198:199], v201 offset:4576\n"
"v_mfma_f32_16x16x32_bf16 v[108:111], v[188:191], a[156:159], v[108:111]\n"
"ds_read_b64_tr_b16 a[168:169], v201 offset:25344\n"
"v_mfma_f32_16x16x32_bf16 v[104:107], v[188:191], a[152:155], v[104:107]\n"
"s_waitcnt lgkmcnt(3)\n"
"ds_read_b64_tr_b16 a[170:171], v201 offset:25600\n"
"buffer_load_dwordx2 a[162:163], v140, s[44:47], s94 offen offset:0 sc0 sc1\n"
"v_mfma_f32_16x16x32_bf16 v[112:115], v[192:195], a[152:155], v[112:115]\n"
"s_add_u32 s94, s61, 0x1400000\n"
"ds_read_b64_tr_b16 a[172:173], v201 offset:25376\n"
"v_mfma_f32_16x16x32_bf16 v[116:119], v[192:195], a[156:159], v[116:119]\n"
"s_waitcnt lgkmcnt(3)\n"
"ds_read_b64_tr_b16 a[174:175], v201 offset:25632\n"
"v_mfma_f32_16x16x32_bf16 v[124:127], v[196:199], a[156:159], v[124:127]\n"
"ds_read_b64_tr_b16 a[240:241], v201 offset:25408\n"
"buffer_load_dwordx2 a[164:165], v140, s[44:47], s94 offen offset:0 sc0 sc1\n"
"v_mfma_f32_16x16x32_bf16 v[120:123], v[196:199], a[152:155], v[120:123]\n"
"s_waitcnt lgkmcnt(3)\n"
"s_add_u32 s94, s61, 0x1401000\n"
"ds_read_b64_tr_b16 a[242:243], v201 offset:25664\n"
"v_mfma_f32_16x16x32_bf16 v[144:147], a[168:171], a[152:155], v[144:147]\n"
"ds_read_b64_tr_b16 a[244:245], v201 offset:25440\n"
"v_mfma_f32_16x16x32_bf16 v[148:151], a[168:171], a[156:159], v[148:151]\n"
"s_waitcnt lgkmcnt(3)\n"
"ds_read_b64_tr_b16 a[246:247], v201 offset:25696\n"
"buffer_load_dwordx2 a[166:167], v140, s[44:47], s94 offen offset:0 sc0 sc1\n"
"v_mfma_f32_16x16x32_bf16 v[156:159], a[172:175], a[156:159], v[156:159]\n"
"s_add_u32 s94, s86, 0\n"
"ds_read_b64_tr_b16 a[248:249], v201 offset:29568\n"
"v_mfma_f32_16x16x32_bf16 v[152:155], a[172:175], a[152:155], v[152:155]\n"
"s_waitcnt lgkmcnt(3)\n"
"ds_read_b64_tr_b16 a[250:251], v201 offset:29824\n"
"v_mfma_f32_16x16x32_bf16 v[160:163], a[240:243], a[152:155], v[160:163]\n"
"ds_read_b64_tr_b16 a[252:253], v201 offset:29600\n"
"buffer_load_dwordx4 v211, s[24:27], s66 offen lds\n"
"v_mfma_f32_16x16x32_bf16 v[164:167], a[240:243], a[156:159], v[164:167]\n"
"s_waitcnt lgkmcnt(3)\n"
"s_add_u32 s66, s78, 0x2d200\n"
"ds_read_b64_tr_b16 a[254:255], v201 offset:29856\n"
"v_mfma_f32_16x16x32_bf16 v[172:175], a[244:247], a[156:159], v[172:175]\n"
"s_add_u32 m0, s59, 0x21000\n"
"ds_read_b64_tr_b16 v[176:177], v200 offset:4224\n"
"v_mfma_f32_16x16x32_bf16 v[168:171], a[244:247], a[152:155], v[168:171]\n"
"s_waitcnt lgkmcnt(3)\n"
"ds_read_b64_tr_b16 v[178:179], v200 offset:4480\n"
"v_mfma_f32_16x16x32_bf16 v[32:35], a[248:251], a[0:3], 0\n"
"ds_read_b64_tr_b16 v[180:181], v200 offset:4256\n"
"v_mfma_f32_16x16x32_bf16 v[36:39], a[248:251], a[4:7], 0\n"
"s_waitcnt lgkmcnt(3)\n"
"ds_read_b64_tr_b16 v[182:183], v200 offset:4512\n"
"buffer_load_dwordx4 v216, s[36:39], s66 offen lds\n"
"v_mfma_f32_16x16x32_bf16 v[44:47], a[252:255], a[4:7], 0\n"
"s_add_u32 s66, s63, 0x36200\n"
"s_add_u32 m0, s59, 0x23100\n"
"ds_read_b64_tr_b16 v[184:185], v200 offset:8448\n"
"v_mfma_f32_16x16x32_bf16 v[40:43], a[252:255], a[0:3], 0\n"
"s_waitcnt lgkmcnt(3)\n"
"ds_read_b64_tr_b16 v[186:187], v200 offset:8704\n"
"v_mfma_f32_16x16x32_bf16 v[32:35], v[176:179], a[8:11], v[32:35]\n"
"ds_read_b64_tr_b16 v[188:189], v200 offset:8480\n"
"buffer_load_dwordx4 v216, s[36:39], s66 offen lds\n"
"v_mfma_f32_16x16x32_bf16 v[36:39], v[176:179], a[12:15], v[36:39]\n"
"s_waitcnt vmcnt(14) lgkmcnt(3)\n"
"s_add_u32 s66, s63, 0x3f200\n"
"ds_read_b64_tr_b16 v[190:191], v200 offset:8736\n"
"v_mfma_f32_16x16x32_bf16 v[44:47], v[180:183], a[12:15], v[44:47]\n"
"s_barrier\n"
"s_add_u32 m0, s59, 0x4200\n"
"ds_read_b64_tr_b16 v[192:193], v201 offset:50688\n"
"v_mfma_f32_16x16x32_bf16 v[40:43], v[180:183], a[8:11], v[40:43]\n"
"s_waitcnt lgkmcnt(3)\n"
"ds_read_b64_tr_b16 v[194:195], v201 offset:50944\n"
"v_mfma_f32_16x16x32_bf16 v[32:35], v[184:187], a[16:19], v[32:35]\n"
"ds_read_b64_tr_b16 v[196:197], v201 offset:50720\n"
"buffer_load_dwordx4 v216, s[36:39], s66 offen lds\n"
"v_mfma_f32_16x16x32_bf16 v[36:39], v[184:187], a[20:23], v[36:39]\n"
"s_waitcnt lgkmcnt(3)\n"
"s_add_u32 s66, s64, 0x200\n"
"ds_read_b64_tr_b16 v[198:199], v201 offset:50976\n"
"v_mfma_f32_16x16x32_bf16 v[44:47], v[188:191], a[20:23], v[44:47]\n"
"s_add_u32 m0, s59, 0x5280\n"
"ds_read_b64_tr_b16 a[168:169], v201 offset:54912\n"
"v_mfma_f32_16x16x32_bf16 v[40:43], v[188:191], a[16:19], v[40:43]\n"
"s_waitcnt lgkmcnt(3)\n"
"ds_read_b64_tr_b16 a[170:171], v201 offset:55168\n"
"v_mfma_f32_16x16x32_bf16 v[32:35], v[192:195], a[24:27], v[32:35]\n"
"ds_read_b64_tr_b16 a[172:173], v201 offset:54944\n"
"buffer_load_dwordx4 v216, s[36:39], s66 offen lds\n"
"v_mfma_f32_16x16x32_bf16 v[36:39], v[192:195], a[28:31], v[36:39]\n"
"L_st11_%=:\n s_cbranch_execz L_ret_%=\n"
"s_waitcnt lgkmcnt(3)\n"
"s_add_u32 s66, s65, s76\n"
"ds_read_b64_tr_b16 a[174:175], v201 offset:55200\n"
"v_mfma_f32_16x16x32_bf16 v[44:47], v[196:199], a[28:31], v[44:47]\n"
"s_add_u32 m0, s59, 0x6300\n"
"ds_read_b64_tr_b16 a[240:241], v202 offset:0\n"
"v_mfma_f32_16x16x32_bf16 v[40:43], v[196:199], a[24:27], v[40:43]\n"
"s_waitcnt lgkmcnt(3)\n"
"ds_read_b64_tr_b16 a[242:243], v202 offset:256\n"
"v_mfma_f32_16x16x32_bf16 v[32:35], a[168:171], a[32:35], v[32:35]\n"
"ds_read_b64_tr_b16 a[244:245], v202 offset:32\n"
"buffer_load_dwordx4 v212, s[40:43], s66 offen lds\n"
"v_mfma_f32_16x16x32_bf16 v[36:39], a[168:171], a[36:39], v[36:39]\n"
"s_waitcnt lgkmcnt(3)\n"
"s_add_u32 s66, s78, 0x36200\n"
"ds_read_b64_tr_b16 a[246:247], v202 offset:288\n"
"v_mfma_f32_16x16x32_bf16 v[44:47], a[172:175], a[36:39], v[44:47]\n"
"s_add_u32 m0, s59, 0x22080\n"
"ds_read_b64_tr_b16 a[248:249], v202 offset:4224\n"
"v_mfma_f32_16x16x32_bf16 v[40:43], a[172:175], a[32:35], v[40:43]\n"
"s_waitcnt lgkmcnt(3)\n"
"ds_read_b64_tr_b16 a[250:251], v202 offset:4480\n"
"v_mfma_f32_16x16x32_bf16 v[32:35], a[240:243], a[40:43], v[32:35]\n"
"ds_read_b64_tr_b16 a[252:253], v202 offset:4256\n"
"v_mfma_f32_16x16x32_bf16 v[36:39], a[240:243], a[44:47], v[36:39]\n"
"s_waitcnt lgkmcnt(3)\n"
"ds_read_b64_tr_b16 a[254:255], v202 offset:4512\n"
"buffer_load_dwordx4 v216, s[36:39], s66 offen lds\n"
"v_mfma_f32_16x16x32_bf16 v[44:47], a[244:247], a[44:47], v[44:47]\n"
"s_add_u32 s66, s78, 0x3f200\n"
"s_add_u32 m0, s59, 0x7380\n"
"ds_read_b64_tr_b16 v[176:177], v202 offset:4288\n"
"v_mfma_f32_16x16x32_bf16 v[40:43], a[244:247], a[40:43], v[40:43]\n"
"s_waitcnt vmcnt(9) lgkmcnt(3)\n"
"ds_read_b64_tr_b16 v[178:179], v202 offset:4544\n"
"v_mfma_f32_16x16x32_bf16 v[96:99], a[248:251], a[160:163], v[96:99]\n"
"s_waitcnt vmcnt(7)\n"
"ds_read_b64_tr_b16 v[180:181], v202 offset:4320\n"
"buffer_load_dwordx4 v216, s[36:39], s66 offen lds\n"
"v_mfma_f32_16x16x32_bf16 v[100:103], a[248:251], a[164:167], v[100:103]\n"
"s_waitcnt lgkmcnt(3)\n"
"s_add_u32 s66, s79, 0x200\n"
"ds_read_b64_tr_b16 v[182:183], v202 offset:4576\n"
"v_mfma_f32_16x16x32_bf16 v[108:111], a[252:255], a[164:167], v[108:111]\n"
"s_add_u32 m0, s59, 0x8400\n"
"ds_read_b64_tr_b16 v[184:185], v202 offset:8448\n"
"v_mfma_f32_16x16x32_bf16 v[104:107], a[252:255], a[160:163], v[104:107]\n"
"s_waitcnt lgkmcnt(3)\n"
"ds_read_b64_tr_b16 v[186:187], v202 offset:8704\n"
"v_cvt_pk_bf16_f32 v248, v96, v97\n"
"v_mfma_f32_16x16x32_bf16 v[112:115], v[176:179], a[160:163], v[112:115]\n"
"ds_read_b64_tr_b16 v[188:189], v202 offset:8480\n"
"buffer_load_dwordx4 v216, s[36:39], s66 offen lds\n"
"v_mfma_f32_16x16x32_bf16 v[116:119], v[176:179], a[164:167], v[116:119]\n"
"s_waitcnt lgkmcnt(3)\n"
"s_add_u32 s66, s63, 0x12000\n"
"ds_read_b64_tr_b16 v[190:191], v202 offset:8736\n"
"v_mfma_f32_16x16x32_bf16 v[124:127], v[180:183], a[164:167], v[124:127]\n"
"s_add_u32 m0, s59, 0x9480\n"
"ds_read_b64_tr_b16 v[192:193], v202 offset:8512\n"
"v_mfma_f32_16x16x32_bf16 v[120:123], v[180:183], a[160:163], v[120:123]\n"
"s_waitcnt lgkmcnt(3)\n"
"ds_read_b64_tr_b16 v[194:195], v202 offset:8768\n"
"v_cvt_pk_bf16_f32 v240, v112, v113\n"
"v_mfma_f32_16x16x32_bf16 v[144:147], v[184:187], a[160:163], v[144:147]\n"
"ds_read_b64_tr_b16 v[196:197], v202 offset:8544\n"
"buffer_load_dwordx4 v211, s[24:27], s66 offen lds\n"
"v_mfma_f32_16x16x32_bf16 v[148:151], v[184:187], a[164:167], v[148:151]\n"
"s_waitcnt lgkmcnt(3)\n"
"s_add_u32 s66, s63, 0x1b000\n"
"ds_read_b64_tr_b16 v[198:199], v202 offset:8800\n"
"v_mfma_f32_16x16x32_bf16 v[156:159], v[188:191], a[164:167], v[156:159]\n"
"s_add_u32 m0, s59, 0xa500\n"
"ds_read_b64_tr_b16 a[168:169], v202 offset:12672\n"
"v_mfma_f32_16x16x32_bf16 v[152:155], v[188:191], a[160:163], v[152:155]\n"
"s_waitcnt lgkmcnt(3)\n"
"ds_read_b64_tr_b16 a[170:171], v202 offset:12928\n"
"v_cvt_pk_bf16_f32 v241, v114, v115\n"
"v_mfma_f32_16x16x32_bf16 v[160:163], v[192:195], a[160:163], v[160:163]\n"
"ds_read_b64_tr_b16 a[172:173], v202 offset:12704\n"
"v_cvt_pk_bf16_f32 v242, v120, v121\n"
"v_mfma_f32_16x16x32_bf16 v[164:167], v[192:195], a[164:167], v[164:167]\n"
"s_waitcnt lgkmcnt(3)\n"
"ds_read_b64_tr_b16 a[174:175], v202 offset:12960\n"
"buffer_load_dwordx4 v211, s[24:27], s66 offen lds\n"
"v_mfma_f32_16x16x32_bf16 v[172:175], v[196:199], a[164:167], v[172:175]\n"
"s_barrier\n"
"s_add_u32 s66, s63, 0x24000\n"
"ds_read_b64_tr_b16 a[240:241], v200 offset:12672\n"
"v_mfma_f32_16x16x32_bf16 v[168:171], v[196:199], a[160:163], v[168:171]\n"
"s_waitcnt lgkmcnt(3)\n"
"s_add_u32 m0, s59, 0xb580\n"
"ds_read_b64_tr_b16 a[242:243], v200 offset:12928\n"
"v_mfma_f32_16x16x32_bf16 v[32:35], a[168:171], a[48:51], v[32:35]\n"
"ds_read_b64_tr_b16 a[244:245], v200 offset:12704\n"
"v_cvt_pk_bf16_f32 v243, v122, v123\n"
"v_mfma_f32_16x16x32_bf16 v[36:39], a[168:171], a[52:55], v[36:39]\n"
"s_waitcnt lgkmcnt(3)\n"
"ds_read_b64_tr_b16 a[246:247], v200 offset:12960\n"
"v_cvt_pk_bf16_f32 v244, v116, v117\n"
"v_mfma_f32_16x16x32_bf16 v[44:47], a[172:175], a[52:55], v[44:47]\n"
"ds_read_b64_tr_b16 a[248:249], v201 offset:8448\n"
"buffer_store_dwordx4 v[240:243], v139, s[44:47], s84 offen offset:0 sc0 sc1\n"
"v_mfma_f32_16x16x32_bf16 v[40:43], a[172:175], a[48:51], v[40:43]\n"
"s_waitcnt lgkmcnt(3)\n"
"ds_read_b64_tr_b16 a[250:251], v201 offset:8704\n"
"v_cvt_pk_bf16_f32 v245, v118, v119\n"
"v_mfma_f32_16x16x32_bf16 v[32:35], a[240:243], a[56:59], v[32:35]\n"
"s_add_u32 s84, s95, 0\n"
"ds_read_b64_tr_b16 a[252:253], v201 offset:8480\n"
"v_cvt_pk_bf16_f32 v246, v124, v125\n"
"v_mfma_f32_16x16x32_bf16 v[36:39], a[240:243], a[60:63], v[36:39]\n"
"s_waitcnt lgkmcnt(3)\n"
"ds_read_b64_tr_b16 a[254:255], v201 offset:8736\n"
"v_cvt_pk_bf16_f32 v247, v126, v127\n"
"v_mfma_f32_16x16x32_bf16 v[44:47], a[244:247], a[60:63], v[44:47]\n"
"s_add_u32 s95, s69, s67\n"
"ds_read_b64_tr_b16 v[176:177], v201 offset:12672\n"
"buffer_load_dwordx4 v211, s[24:27], s66 offen lds\n"
"v_mfma_f32_16x16x32_bf16 v[40:43], a[244:247], a[56:59], v[40:43]\n"
"s_waitcnt lgkmcnt(3)\n"
"s_add_u32 s66, s63, 0x2d000\n"
"ds_read_b64_tr_b16 v[178:179], v201 offset:12928\n"
"v_mfma_f32_16x16x32_bf16 v[32:35], a[248:251], a[64:67], v[32:35]\n"
"s_add_u32 m0, s59, 0xc600\n"
"s_xor_b32 s67, s9, 0x2000\n"
"ds_read_b64_tr_b16 v[180:181], v201 offset:12704\n"
"v_mfma_f32_16x16x32_bf16 v[36:39], a[248:251], a[68:71], v[36:39]\n"
"s_waitcnt lgkmcnt(3)\n"
"ds_read_b64_tr_b16 v[182:183], v201 offset:12960\n"
"v_cvt_pk_bf16_f32 v249, v98, v99\n"
"v_mfma_f32_16x16x32_bf16 v[44:47], a[252:255], a[68:71], v[44:47]\n"
"ds_read_b64_tr_b16 v[184:185], v201 offset:16896\n"
"v_cvt_pk_bf16_f32 v250, v104, v105\n"
"v_mfma_f32_16x16x32_bf16 v[40:43], a[252:255], a[64:67], v[40:43]\n"
"s_waitcnt lgkmcnt(3)\n"
"ds_read_b64_tr_b16 v[186:187], v201 offset:17152\n"
"buffer_load_dwordx4 v211, s[24:27], s66 offen lds\n"
"v_mfma_f32_16x16x32_bf16 v[32:35], v[176:179], a[72:75], v[32:35]\n"
"s_add_u32 s66, s78, 0\n"
"s_add_u32 m0, s59, 0xd680\n"
"ds_read_b64_tr_b16 v[188:189], v201 offset:16928\n"
"v_mfma_f32_16x16x32_bf16 v[36:39], v[176:179], a[76:79], v[36:39]\n"
"s_waitcnt lgkmcnt(3)\n"
"ds_read_b64_tr_b16 v[190:191], v201 offset:17184\n"
"v_cvt_pk_bf16_f32 v251, v106, v107\n"
"v_mfma_f32_16x16x32_bf16 v[44:47], v[180:183], a[76:79], v[44:47]\n"
"ds_read_b64_tr_b16 v[192:193], v201 offset:16960\n"
"v_cvt_pk_bf16_f32 v252, v100, v101\n"
"v_mfma_f32_16x16x32_bf16 v[40:43], v[180:183], a[72:75], v[40:43]\n"
"s_waitcnt lgkmcnt(3)\n"
"ds_read_b64_tr_b16 v[194:195], v201 offset:17216\n"
"v_cvt_pk_bf16_f32 v253, v102, v103\n"
"v_mfma_f32_16x16x32_bf16 v[0:3], v[184:187], a[0:3], 0\n"
"ds_read_b64_tr_b16 v[196:197], v201 offset:16992\n"
"buffer_load_dwordx4 v211, s[24:27], s66 offen lds\n"
"v_mfma_f32_16x16x32_bf16 v[4:7], v[184:187], a[4:7], 0\n"
"s_waitcnt lgkmcnt(3)\n"
"s_add_u32 s66, s78, 0x9000\n"
"ds_read_b64_tr_b16 v[198:199], v201 offset:17248\n"
"v_mfma_f32_16x16x32_bf16 v[12:15], v[188:191], a[4:7], 0\n"
"s_add_u32 m0, s59, 0xe700\n"
"ds_read_b64_tr_b16 a[168:169], v201 offset:21120\n"
"v_mfma_f32_16x16x32_bf16 v[8:11], v[188:191], a[0:3], 0\n"
"s_waitcnt lgkmcnt(3)\n"
"ds_read_b64_tr_b16 a[170:171], v201 offset:21376\n"
"v_cvt_pk_bf16_f32 v254, v108, v109\n"
"v_mfma_f32_16x16x32_bf16 v[16:19], v[192:195], a[0:3], 0\n"
"ds_read_b64_tr_b16 a[172:173], v201 offset:21152\n"
"v_cvt_pk_bf16_f32 v255, v110, v111\n"
"v_mfma_f32_16x16x32_bf16 v[20:23], v[192:195], a[4:7], 0\n"
"s_waitcnt lgkmcnt(3)\n"
"ds_read_b64_tr_b16 a[174:175], v201 offset:21408\n"
"buffer_store_dwordx4 v[244:247], v139, s[44:47], s84 offen offset:0 sc0 sc1\n"
"v_mfma_f32_16x16x32_bf16 v[28:31], v[196:199], a[4:7], 0\n"
"s_add_u32 s84, s89, 0x100000\n"
"ds_read_b64_tr_b16 a[240:241], v201 offset:33792\n"
"v_mfma_f32_16x16x32_bf16 v[24:27], v[196:199], a[0:3], 0\n"
"s_waitcnt lgkmcnt(3)\n"
"ds_read_b64_tr_b16 a[242:243], v201 offset:34048\n"
"v_cvt_pk_bf16_f32 v240, v160, v161\n"
"v_mfma_f32_16x16x32_bf16 v[32:35], a[168:171], a[80:83], v[32:35]\n"
"ds_read_b64_tr_b16 a[244:245], v201 offset:33824\n"
"v_cvt_pk_bf16_f32 v241, v162, v163\n"
"v_mfma_f32_16x16x32_bf16 v[36:39], a[168:171], a[84:87], v[36:39]\n"
"s_waitcnt lgkmcnt(3)\n"
"ds_read_b64_tr_b16 a[246:247], v201 offset:34080\n"
"buffer_load_dwordx4 v211, s[24:27], s66 offen lds\n"
"v_mfma_f32_16x16x32_bf16 v[44:47], a[172:175], a[84:87], v[44:47]\n"
"s_add_u32 s66, s80, s76\n"
"s_add_u32 m0, s59, 0\n"
"ds_read_b64_tr_b16 a[248:249], v201 offset:38016\n"
"v_mfma_f32_16x16x32_bf16 v[40:43], a[172:175], a[80:83], v[40:43]\n"
"s_waitcnt lgkmcnt(3)\n"
"ds_read_b64_tr_b16 a[250:251], v201 offset:38272\n"
"v_cvt_pk_bf16_f32 v242, v168, v169\n"
"v_mfma_f32_16x16x32_bf16 v[32:35], a[240:243], a[88:91], v[32:35]\n"
"ds_read_b64_tr_b16 a[252:253], v201 offset:38048\n"
"v_cvt_pk_bf16_f32 v243, v170, v171\n"
"v_mfma_f32_16x16x32_bf16 v[36:39], a[240:243], a[92:95], v[36:39]\n"
"s_waitcnt vmcnt(11) lgkmcnt(3)\n"
"ds_read_b64_tr_b16 a[254:255], v201 offset:38304\n"
"v_add_u32 v209, 0x2a000, v209\n"
"v_mfma_f32_16x16x32_bf16 v[44:47], a[244:247], a[92:95], v[44:47]\n"
"s_barrier\n"
"ds_read_b64_tr_b16 v[176:177], v201 offset:42240\n"
"buffer_load_dwordx4 v212, s[40:43], s66 offen lds\n"
"v_mfma_f32_16x16x32_bf16 v[40:43], a[244:247], a[88:91], v[40:43]\n"
"s_waitcnt lgkmcnt(3)\n"
"s_add_u32 s66, s81, s76\n"
"ds_read_b64_tr_b16 v[178:179], v201 offset:42496\n"
"v_mfma_f32_16x16x32_bf16 v[32:35], a[248:251], a[96:99], v[32:35]\n"
"s_add_u32 m0, s59, 0x1080\n"
"ds_read_b64_tr_b16 v[180:181], v201 offset:42272\n"
"v_mfma_f32_16x16x32_bf16 v[36:39], a[248:251], a[100:103], v[36:39]\n"
"s_waitcnt lgkmcnt(3)\n"
"ds_read_b64_tr_b16 v[182:183], v201 offset:42528\n"
"v_cvt_pk_bf16_f32 v244, v164, v165\n"
"v_mfma_f32_16x16x32_bf16 v[44:47], a[252:255], a[100:103], v[44:47]\n"
"ds_read_b64_tr_b16 v[184:185], v201 offset:46464\n"
"v_cvt_pk_bf16_f32 v245, v166, v167\n"
"v_mfma_f32_16x16x32_bf16 v[40:43], a[252:255], a[96:99], v[40:43]\n"
"s_waitcnt lgkmcnt(3)\n"
"ds_read_b64_tr_b16 v[186:187], v201 offset:46720\n"
"buffer_load_dwordx4 v213, s[40:43], s66 offen lds\n"
"v_mfma_f32_16x16x32_bf16 v[32:35], v[176:179], a[104:107], v[32:35]\n"
"s_add_u32 s66, s78, 0x12000\n"
"s_add_u32 m0, s59, 0xf780\n"
"ds_read_b64_tr_b16 v[188:189], v201 offset:46496\n"
"v_mfma_f32_16x16x32_bf16 v[36:39], v[176:179], a[108:111], v[36:39]\n"
"s_waitcnt lgkmcnt(3)\n"
"ds_read_b64_tr_b16 v[190:191], v201 offset:46752\n"
"v_cvt_pk_bf16_f32 v246, v172, v173\n"
"v_mfma_f32_16x16x32_bf16 v[44:47], v[180:183], a[108:111], v[44:47]\n"
"ds_read_b64_tr_b16 v[192:193], v201 offset:46528\n"
"buffer_store_dwordx4 v[248:251], v139, s[44:47], s84 offen offset:0 sc0 sc1\n"
"v_mfma_f32_16x16x32_bf16 v[40:43], v[180:183], a[104:107], v[40:43]\n"
"s_waitcnt lgkmcnt(3)\n"
"s_add_u32 s84, s89, 0x900000\n"
"ds_read_b64_tr_b16 v[194:195], v201 offset:46784\n"
"v_mfma_f32_16x16x32_bf16 v[0:3], v[184:187], a[8:11], v[0:3]\n"
"s_add_u32 s89, s69, s9\n"
"ds_read_b64_tr_b16 v[196:197], v201 offset:46560\n"
"v_cvt_pk_bf16_f32 v247, v174, v175\n"
"v_mfma_f32_16x16x32_bf16 v[4:7], v[184:187], a[12:15], v[4:7]\n"
"s_waitcnt lgkmcnt(3)\n"
"ds_read_b64_tr_b16 v[198:199], v201 offset:46816\n"
"v_mov_b32 v130, v128\n"
"v_mfma_f32_16x16x32_bf16 v[12:15], v[188:191], a[12:15], v[12:15]\n"
"s_mul_i32 s69, s68, 0x80\n"
"ds_read_b64_tr_b16 a[168:169], v202 offset:16896\n"
"buffer_load_dwordx4 v211, s[24:27], s66 offen lds\n"
"v_mfma_f32_16x16x32_bf16 v[8:11], v[188:191], a[8:11], v[8:11]\n"
"s_waitcnt lgkmcnt(3)\n"
"s_add_u32 s66, s78, 0x1b000\n"
"ds_read_b64_tr_b16 a[170:171], v202 offset:17152\n"
"v_mfma_f32_16x16x32_bf16 v[16:19], v[192:195], a[8:11], v[16:19]\n"
"s_add_u32 m0, s59, 0x14a00\n"
"s_add_u32 s69, s69, s19\n"
"ds_read_b64_tr_b16 a[172:173], v202 offset:16928\n"
"v_mfma_f32_16x16x32_bf16 v[20:23], v[192:195], a[12:15], v[20:23]\n"
"s_waitcnt lgkmcnt(3)\n"
"ds_read_b64_tr_b16 a[174:175], v202 offset:17184\n"
"v_mov_b32 v131, v129\n"
"v_mfma_f32_16x16x32_bf16 v[28:31], v[196:199], a[12:15], v[28:31]\n"
"ds_read_b64_tr_b16 a[240:241], v202 offset:25344\n"
"v_cvt_pk_bf16_f32 v248, v144, v145\n"
"v_mfma_f32_16x16x32_bf16 v[24:27], v[196:199], a[8:11], v[24:27]\n"
"s_waitcnt lgkmcnt(3)\n"
"ds_read_b64_tr_b16 a[242:243], v202 offset:25600\n"
"buffer_load_dwordx4 v211, s[24:27], s66 offen lds\n"
"v_mfma_f32_16x16x32_bf16 v[32:35], a[168:171], a[112:115], v[32:35]\n"
"s_add_u32 m0, s62, 0x24180\n"
"s_add_u32 s66, s78, 0x24000\n"
"ds_read_b64_tr_b16 a[244:245], v202 offset:25376\n"
"v_mfma_f32_16x16x32_bf16 v[36:39], a[168:171], a[116:119], v[36:39]\n"
"s_waitcnt lgkmcnt(3)\n"
"ds_read_b64_tr_b16 a[246:247], v202 offset:25632\n"
"v_cvt_pk_bf16_f32 v249, v146, v147\n"
"v_mfma_f32_16x16x32_bf16 v[44:47], a[172:175], a[116:119], v[44:47]\n"
"ds_read_b64_tr_b16 a[248:249], v200 offset:16896\n"
"v_cvt_pk_bf16_f32 v250, v152, v153\n"
"v_mfma_f32_16x16x32_bf16 v[40:43], a[172:175], a[112:115], v[40:43]\n"
"s_waitcnt lgkmcnt(3)\n"
"ds_read_b64_tr_b16 a[250:251], v200 offset:17152\n"
"v_cvt_pk_bf16_f32 v251, v154, v155\n"
"v_mfma_f32_16x16x32_bf16 v[32:35], a[240:243], a[120:123], v[32:35]\n"
"ds_read_b64_tr_b16 a[252:253], v200 offset:16928\n"
"buffer_load_dwordx4 v132, s[20:23], s90 offen lds\n"
"v_mfma_f32_16x16x32_bf16 v[36:39], a[240:243], a[124:127], v[36:39]\n"
"s_waitcnt lgkmcnt(3)\n"
"s_add_u32 m0, s62, 0x24580\n"
"ds_read_b64_tr_b16 a[254:255], v200 offset:17184\n"
"v_mfma_f32_16x16x32_bf16 v[44:47], a[244:247], a[124:127], v[44:47]\n"
"ds_read_b64_tr_b16 v[176:177], v200 offset:21120\n"
"v_mfma_f32_16x16x32_bf16 v[40:43], a[244:247], a[120:123], v[40:43]\n"
"s_waitcnt lgkmcnt(3)\n"
"ds_read_b64_tr_b16 v[178:179], v200 offset:21376\n"
"v_mfma_f32_16x16x32_bf16 v[32:35], a[248:251], a[128:131], v[32:35]\n"
"ds_read_b64_tr_b16 v[180:181], v200 offset:21152\n"
"v_mfma_f32_16x16x32_bf16 v[36:39], a[248:251], a[132:135], v[36:39]\n"
"s_waitcnt lgkmcnt(3)\n"
"ds_read_b64_tr_b16 v[182:183], v200 offset:21408\n"
"buffer_store_dwordx4 v[252:255], v139, s[44:47], s84 offen offset:0 sc0 sc1\n"
"v_mfma_f32_16x16x32_bf16 v[44:47], a[252:255], a[132:135], v[44:47]\n"
"s_add_u32 s84, s95, 0xa00000\n"
"ds_read_b64_tr_b16 v[184:185], v200 offset:25344\n"
"v_mfma_f32_16x16x32_bf16 v[40:43], a[252:255], a[128:131], v[40:43]\n"
"s_waitcnt lgkmcnt(3)\n"
"ds_read_b64_tr_b16 v[186:187], v200 offset:25600\n"
"v_cvt_pk_bf16_f32 v252, v148, v149\n"
"v_mfma_f32_16x16x32_bf16 v[32:35], v[176:179], a[136:139], v[32:35]\n"
"ds_read_b64_tr_b16 v[188:189], v200 offset:25376\n"
"v_cvt_pk_bf16_f32 v253, v150, v151\n"
"v_mfma_f32_16x16x32_bf16 v[36:39], v[176:179], a[140:143], v[36:39]\n"
"s_waitcnt vmcnt(11) lgkmcnt(3)\n"
"ds_read_b64_tr_b16 v[190:191], v200 offset:25632\n"
"buffer_load_dwordx4 v132, s[20:23], s91 offen lds\n"
"v_mfma_f32_16x16x32_bf16 v[44:47], v[180:183], a[140:143], v[44:47]\n"
"s_barrier\n"
"s_add_u32 m0, s62, 0x24980\n"
"ds_read_b64_tr_b16 v[192:193], v200 offset:25408\n"
"v_mfma_f32_16x16x32_bf16 v[40:43], v[180:183], a[136:139], v[40:43]\n"
"s_waitcnt lgkmcnt(3)\n"
"ds_read_b64_tr_b16 v[194:195], v200 offset:25664\n"
"v_cvt_pk_bf16_f32 v254, v156, v157\n"
"v_mfma_f32_16x16x32_bf16 v[0:3], v[184:187], a[16:19], v[0:3]\n"
"ds_read_b64_tr_b16 v[196:197], v200 offset:25440\n"
"v_cvt_pk_bf16_f32 v255, v158, v159\n"
"v_mfma_f32_16x16x32_bf16 v[4:7], v[184:187], a[20:23], v[4:7]\n"
"s_waitcnt lgkmcnt(3)\n"
"ds_read_b64_tr_b16 v[198:199], v200 offset:25696\n"
"v_mfma_f32_16x16x32_bf16 v[12:15], v[188:191], a[20:23], v[12:15]\n"
"ds_read_b64_tr_b16 a[168:169], v202 offset:21120\n"
"buffer_load_dwordx4 v133, s[20:23], s92 offen lds\n"
"v_mfma_f32_16x16x32_bf16 v[8:11], v[188:191], a[16:19], v[8:11]\n"
"s_waitcnt lgkmcnt(3)\n"
"s_add_u32 m0, s59, 0x15a80\n"
"ds_read_b64_tr_b16 a[170:171], v202 offset:21376\n"
"v_mfma_f32_16x16x32_bf16 v[16:19], v[192:195], a[16:19], v[16:19]\n"
"ds_read_b64_tr_b16 a[172:173], v202 offset:21152\n"
"v_mfma_f32_16x16x32_bf16 v[20:23], v[192:195], a[20:23], v[20:23]\n"
"s_waitcnt lgkmcnt(3)\n"
"ds_read_b64_tr_b16 a[174:175], v202 offset:21408\n"
"v_mfma_f32_16x16x32_bf16 v[28:31], v[196:199], a[20:23], v[28:31]\n"
"ds_read_b64_tr_b16 a[240:241], v200 offset:29568\n"
"v_mfma_f32_16x16x32_bf16 v[24:27], v[196:199], a[16:19], v[24:27]\n"
"s_waitcnt lgkmcnt(3)\n"
"ds_read_b64_tr_b16 a[242:243], v200 offset:29824\n"
"buffer_load_dwordx4 v211, s[24:27], s66 offen lds\n"
"v_mfma_f32_16x16x32_bf16 v[32:35], a[168:171], a[144:147], v[32:35]\n"
"s_add_u32 s66, s78, 0x2d000\n"
"s_add_u32 m0, s59, 0x2100\n"
"ds_read_b64_tr_b16 a[244:245], v200 offset:29600\n"
"v_mfma_f32_16x16x32_bf16 v[36:39], a[168:171], a[148:151], v[36:39]\n"
"s_waitcnt lgkmcnt(3)\n"
"ds_read_b64_tr_b16 a[246:247], v200 offset:29856\n"
"v_mfma_f32_16x16x32_bf16 v[44:47], a[172:175], a[148:151], v[44:47]\n"
"ds_read_b64_tr_b16 a[248:249], v200 offset:33792\n"
"v_mfma_f32_16x16x32_bf16 v[40:43], a[172:175], a[144:147], v[40:43]\n"
"s_waitcnt lgkmcnt(3)\n"
"ds_read_b64_tr_b16 a[250:251], v200 offset:34048\n"
"v_mfma_f32_16x16x32_bf16 v[32:35], a[240:243], a[152:155], v[32:35]\n"
"ds_read_b64_tr_b16 a[252:253], v200 offset:33824\n"
"buffer_store_dwordx4 v[240:243], v139, s[44:47], s84 offen offset:0 sc0 sc1\n"
"v_mfma_f32_16x16x32_bf16 v[36:39], a[240:243], a[156:159], v[36:39]\n"
"s_waitcnt lgkmcnt(3)\n"
"s_add_u32 s84, s95, 0x200000\n"
"ds_read_b64_tr_b16 a[254:255], v200 offset:34080\n"
"v_mfma_f32_16x16x32_bf16 v[44:47], a[244:247], a[156:159], v[44:47]\n"
"s_add_u32 s95, s69, s67\n"
"s_xor_b32 s67, s9, 0x2000\n"
"ds_read_b64_tr_b16 v[176:177], v200 offset:38016\n"
"v_mfma_f32_16x16x32_bf16 v[40:43], a[244:247], a[152:155], v[40:43]\n"
"s_waitcnt lgkmcnt(3)\n"
"ds_read_b64_tr_b16 v[178:179], v200 offset:38272\n"
"v_mfma_f32_16x16x32_bf16 v[32:35], a[248:251], a[160:163], v[32:35]\n"
"ds_read_b64_tr_b16 v[180:181], v200 offset:38048\n"
"buffer_load_dwordx4 v211, s[24:27], s66 offen lds\n"
"v_mfma_f32_16x16x32_bf16 v[36:39], a[248:251], a[164:167], v[36:39]\n"
"s_waitcnt lgkmcnt(3)\n"
"s_add_u32 s66, s63, 0x36000\n"
"ds_read_b64_tr_b16 v[182:183], v200 offset:38304\n"
"v_mfma_f32_16x16x32_bf16 v[44:47], a[252:255], a[164:167], v[44:47]\n"
"s_add_u32 m0, s59, 0x1ad00\n"
"ds_read_b64_tr_b16 v[184:185], v200 offset:38080\n"
"v_mfma_f32_16x16x32_bf16 v[40:43], a[252:255], a[160:163], v[40:43]\n"
"s_waitcnt lgkmcnt(3)\n"
"ds_read_b64_tr_b16 v[186:187], v200 offset:38336\n"
"v_cvt_pk_bf16_f32 v240, v32, v33\n"
"v_mfma_f32_16x16x32_bf16 v[0:3], v[176:179], a[24:27], v[0:3]\n"
"ds_read_b64_tr_b16 v[188:189], v200 offset:38112\n"
"v_cvt_pk_bf16_f32 v241, v34, v35\n"
"v_mfma_f32_16x16x32_bf16 v[4:7], v[176:179], a[28:31], v[4:7]\n"
"s_waitcnt lgkmcnt(3)\n"
"ds_read_b64_tr_b16 v[190:191], v200 offset:38368\n"
"buffer_load_dwordx4 v211, s[24:27], s66 offen lds\n"
"v_mfma_f32_16x16x32_bf16 v[12:15], v[180:183], a[28:31], v[12:15]\n"
"s_add_u32 s66, s63, 0x3f000\n"
"s_add_u32 m0, s59, 0x1bd80\n"
"ds_read_b64_tr_b16 v[192:193], v200 offset:42240\n"
"v_mfma_f32_16x16x32_bf16 v[8:11], v[180:183], a[24:27], v[8:11]\n"
"s_waitcnt lgkmcnt(3)\n"
"ds_read_b64_tr_b16 v[194:195], v200 offset:42496\n"
"v_cvt_pk_bf16_f32 v242, v40, v41\n"
"v_mfma_f32_16x16x32_bf16 v[16:19], v[184:187], a[24:27], v[16:19]\n"
"ds_read_b64_tr_b16 v[196:197], v200 offset:42272\n"
"v_cvt_pk_bf16_f32 v243, v42, v43\n"
"v_mfma_f32_16x16x32_bf16 v[20:23], v[184:187], a[28:31], v[20:23]\n"
"s_waitcnt lgkmcnt(3)\n"
"ds_read_b64_tr_b16 v[198:199], v200 offset:42528\n"
"v_mfma_f32_16x16x32_bf16 v[28:31], v[188:191], a[28:31], v[28:31]\n"
"ds_read_b64_tr_b16 a[168:169], v200 offset:42304\n"
"buffer_store_dwordx4 v[244:247], v139, s[44:47], s84 offen offset:0 sc0 sc1\n"
"v_mfma_f32_16x16x32_bf16 v[24:27], v[188:191], a[24:27], v[24:27]\n"
"s_waitcnt lgkmcnt(3)\n"
"s_add_u32 s84, s89, 0x300000\n"
"ds_read_b64_tr_b16 a[170:171], v200 offset:42560\n"
"v_mfma_f32_16x16x32_bf16 v[0:3], v[192:195], a[32:35], v[0:3]\n"
"ds_read_b64_tr_b16 a[172:173], v200 offset:42336\n"
"v_cvt_pk_bf16_f32 v244, v36, v37\n"
"v_mfma_f32_16x16x32_bf16 v[4:7], v[192:195], a[36:39], v[4:7]\n"
"s_waitcnt vmcnt(13) lgkmcnt(3)\n"
"ds_read_b64_tr_b16 a[174:175], v200 offset:42592\n"
"v_cvt_pk_bf16_f32 v245, v38, v39\n"
"v_mfma_f32_16x16x32_bf16 v[12:15], v[196:199], a[36:39], v[12:15]\n"
"s_barrier\n"
"ds_read_b64_tr_b16 a[240:241], v200 offset:46464\n"
"buffer_load_dwordx4 v211, s[24:27], s66 offen lds\n"
"v_mfma_f32_16x16x32_bf16 v[8:11], v[196:199], a[32:35], v[8:11]\n"
"s_waitcnt lgkmcnt(3)\n"
"s_add_u32 s66, s64, 0\n"
"ds_read_b64_tr_b16 a[242:243], v200 offset:46720\n"
"v_mfma_f32_16x16x32_bf16 v[16:19], a[168:171], a[32:35], v[16:19]\n"
"s_add_u32 m0, s59, 0x1ce00\n"
"ds_read_b64_tr_b16 a[244:245], v200 offset:46496\n"
"v_mfma_f32_16x16x32_bf16 v[20:23], a[168:171], a[36:39], v[20:23]\n"
"s_waitcnt lgkmcnt(3)\n"
"ds_read_b64_tr_b16 a[246:247], v200 offset:46752\n"
"v_cvt_pk_bf16_f32 v246, v44, v45\n"
"v_mfma_f32_16x16x32_bf16 v[28:31], a[172:175], a[36:39], v[28:31]\n"
"ds_read_b64_tr_b16 a[248:249], v200 offset:46528\n"
"v_cvt_pk_bf16_f32 v247, v46, v47\n"
"v_mfma_f32_16x16x32_bf16 v[24:27], a[172:175], a[32:35], v[24:27]\n"
"s_waitcnt lgkmcnt(3)\n"
"ds_read_b64_tr_b16 a[250:251], v200 offset:46784\n"
"buffer_load_dwordx4 v211, s[24:27], s66 offen lds\n"
"v_mfma_f32_16x16x32_bf16 v[0:3], a[240:243], a[40:43], v[0:3]\n"
"s_add_u32 s66, s78, 0x36000\n"
"s_add_u32 m0, s59, 0x1de80\n"
"ds_read_b64_tr_b16 a[252:253], v200 offset:46560\n"
"v_mfma_f32_16x16x32_bf16 v[4:7], a[240:243], a[44:47], v[4:7]\n"
"s_waitcnt lgkmcnt(3)\n"
"ds_read_b64_tr_b16 a[254:255], v200 offset:46816\n"
"v_mfma_f32_16x16x32_bf16 v[12:15], a[244:247], a[44:47], v[12:15]\n"
"ds_read_b64_tr_b16 v[176:177], v200 offset:50688\n"
"v_mfma_f32_16x16x32_bf16 v[8:11], a[244:247], a[40:43], v[8:11]\n"
"s_waitcnt lgkmcnt(3)\n"
"ds_read_b64_tr_b16 v[178:179], v200 offset:50944\n"
"v_mfma_f32_16x16x32_bf16 v[16:19], a[248:251], a[40:43], v[16:19]\n"
"ds_read_b64_tr_b16 v[180:181], v200 offset:50720\n"
"buffer_load_dwordx4 v211, s[24:27], s66 offen lds\n"
"v_mfma_f32_16x16x32_bf16 v[20:23], a[248:251], a[44:47], v[20:23]\n"
"s_waitcnt lgkmcnt(3)\n"
"s_add_u32 s66, s78, 0x3f000\n"
"ds_read_b64_tr_b16 v[182:183], v200 offset:50976\n"
"v_mfma_f32_16x16x32_bf16 v[28:31], a[252:255], a[44:47], v[28:31]\n"
"s_add_u32 m0, s59, 0x1ef00\n"
"ds_read_b64_tr_b16 v[184:185], v200 offset:50752\n"
"v_mfma_f32_16x16x32_bf16 v[24:27], a[252:255], a[40:43], v[24:27]\n"
"s_waitcnt lgkmcnt(3)\n"
"ds_read_b64_tr_b16 v[186:187], v200 offset:51008\n"
"v_mfma_f32_16x16x32_bf16 v[0:3], v[176:179], a[48:51], v[0:3]\n"
"ds_read_b64_tr_b16 v[188:189], v200 offset:50784\n"
"v_mfma_f32_16x16x32_bf16 v[4:7], v[176:179], a[52:55], v[4:7]\n"
"s_waitcnt lgkmcnt(3)\n"
"ds_read_b64_tr_b16 v[190:191], v200 offset:51040\n"
"buffer_store_dwordx4 v[248:251], v139, s[44:47], s84 offen offset:0 sc0 sc1\n"
"v_mfma_f32_16x16x32_bf16 v[12:15], v[180:183], a[52:55], v[12:15]\n"
"s_add_u32 s84, s89, 0xb00000\n"
"s_add_u32 s89, s69, s9\n"
"ds_read_b64_tr_b16 v[192:193], v200 offset:54912\n"
"v_mfma_f32_16x16x32_bf16 v[8:11], v[180:183], a[48:51], v[8:11]\n"
"s_waitcnt lgkmcnt(3)\n"
"ds_read_b64_tr_b16 v[194:195], v200 offset:55168\n"
"v_mfma_f32_16x16x32_bf16 v[16:19], v[184:187], a[48:51], v[16:19]\n"
"ds_read_b64_tr_b16 v[196:197], v200 offset:54944\n"
"v_mfma_f32_16x16x32_bf16 v[20:23], v[184:187], a[52:55], v[20:23]\n"
"s_waitcnt lgkmcnt(3)\n"
"ds_read_b64_tr_b16 v[198:199], v200 offset:55200\n"
"buffer_load_dwordx4 v211, s[24:27], s66 offen lds\n"
"v_mfma_f32_16x16x32_bf16 v[28:31], v[188:191], a[52:55], v[28:31]\n"
"s_add_u32 s66, s79, 0\n"
"s_add_u32 m0, s59, 0x3180\n"
"ds_read_b64_tr_b16 a[168:169], v200 offset:54976\n"
"v_mfma_f32_16x16x32_bf16 v[24:27], v[188:191], a[48:51], v[24:27]\n"
"s_waitcnt lgkmcnt(3)\n"
"ds_read_b64_tr_b16 a[170:171], v200 offset:55232\n"
"v_mfma_f32_16x16x32_bf16 v[0:3], v[192:195], a[56:59], v[0:3]\n"
"ds_read_b64_tr_b16 a[172:173], v200 offset:55008\n"
"v_mfma_f32_16x16x32_bf16 v[4:7], v[192:195], a[60:63], v[4:7]\n"
"s_waitcnt lgkmcnt(3)\n"
"ds_read_b64_tr_b16 a[174:175], v200 offset:55264\n"
"v_mfma_f32_16x16x32_bf16 v[12:15], v[196:199], a[60:63], v[12:15]\n"
"ds_read_b64_tr_b16 a[240:241], v201 offset:0\n"
"buffer_load_dwordx4 v211, s[24:27], s66 offen lds\n"
"v_mfma_f32_16x16x32_bf16 v[8:11], v[196:199], a[56:59], v[8:11]\n"
"s_waitcnt lgkmcnt(3)\n"
"s_add_u32 s66, s63, 0\n"
"ds_read_b64_tr_b16 a[242:243], v201 offset:256\n"
"v_mfma_f32_16x16x32_bf16 v[16:19], a[168:171], a[56:59], v[16:19]\n"
"s_add_u32 m0, s59, 0x10800\n"
"ds_read_b64_tr_b16 a[244:245], v201 offset:32\n"
"v_mfma_f32_16x16x32_bf16 v[20:23], a[168:171], a[60:63], v[20:23]\n"
"s_waitcnt lgkmcnt(3)\n"
"ds_read_b64_tr_b16 a[246:247], v201 offset:288\n"
"v_mfma_f32_16x16x32_bf16 v[28:31], a[172:175], a[60:63], v[28:31]\n"
"ds_read_b64_tr_b16 a[248:249], v201 offset:64\n"
"v_mfma_f32_16x16x32_bf16 v[24:27], a[172:175], a[56:59], v[24:27]\n"
"s_waitcnt lgkmcnt(3)\n"
"ds_read_b64_tr_b16 a[250:251], v201 offset:320\n"
"buffer_store_dwordx4 v[252:255], v139, s[44:47], s84 offen offset:0 sc0 sc1\n"
"v_mfma_f32_16x16x32_bf16 v[0:3], a[240:243], a[64:67], v[0:3]\n"
"s_add_u32 s84, s95, 0xc00000\n"
"ds_read_b64_tr_b16 a[252:253], v201 offset:96\n"
"v_mfma_f32_16x16x32_bf16 v[4:7], a[240:243], a[68:71], v[4:7]\n"
"s_waitcnt vmcnt(11) lgkmcnt(3)\n"
"ds_read_b64_tr_b16 a[254:255], v201 offset:352\n"
"v_mfma_f32_16x16x32_bf16 v[12:15], a[244:247], a[68:71], v[12:15]\n"
"s_barrier\n"
"ds_read_b64_tr_b16 v[176:177], v200 offset:0\n"
"v_mfma_f32_16x16x32_bf16 v[8:11], a[244:247], a[64:67], v[8:11]\n"
"s_waitcnt lgkmcnt(3)\n"
"ds_read_b64_tr_b16 v[178:179], v200 offset:256\n"
"buffer_load_dwordx4 v211, s[28:31], s66 offen lds\n"
"v_mfma_f32_16x16x32_bf16 v[16:19], a[248:251], a[64:67], v[16:19]\n"
"s_add_u32 s66, s63, 0x9000\n"
"s_add_u32 m0, s59, 0x11880\n"
"ds_read_b64_tr_b16 v[180:181], v200 offset:32\n"
"v_mfma_f32_16x16x32_bf16 v[20:23], a[248:251], a[68:71], v[20:23]\n"
"s_waitcnt lgkmcnt(3)\n"
"ds_read_b64_tr_b16 v[182:183], v200 offset:288\n"
"v_mfma_f32_16x16x32_bf16 v[28:31], a[252:255], a[68:71], v[28:31]\n"
"ds_read_b64_tr_b16 v[184:185], v200 offset:64\n"
"v_mfma_f32_16x16x32_bf16 v[24:27], a[252:255], a[64:67], v[24:27]\n"
"s_waitcnt lgkmcnt(3)\n"
"ds_read_b64_tr_b16 v[186:187], v200 offset:320\n"
"v_mfma_f32_16x16x32_bf16 v[0:3], v[176:179], a[72:75], v[0:3]\n"
"ds_read_b64_tr_b16 v[188:189], v200 offset:96\n"
"buffer_load_dwordx4 v211, s[28:31], s66 offen lds\n"
"v_mfma_f32_16x16x32_bf16 v[4:7], v[176:179], a[76:79], v[4:7]\n"
"s_waitcnt lgkmcnt(3)\n"
"s_add_u32 s66, s65, s77\n"
"ds_read_b64_tr_b16 v[190:191], v200 offset:352\n"
"v_mfma_f32_16x16x32_bf16 v[12:15], v[180:183], a[76:79], v[12:15]\n"
"s_add_u32 m0, s59, 0x12900\n"
"ds_read_b64_tr_b16 v[192:193], v200 offset:4224\n"
"v_mfma_f32_16x16x32_bf16 v[8:11], v[180:183], a[72:75], v[8:11]\n"
"s_waitcnt lgkmcnt(3)\n"
"ds_read_b64_tr_b16 v[194:195], v200 offset:4480\n"
"v_mfma_f32_16x16x32_bf16 v[16:19], v[184:187], a[72:75], v[16:19]\n"
"ds_read_b64_tr_b16 v[196:197], v200 offset:4256\n"
"v_mfma_f32_16x16x32_bf16 v[20:23], v[184:187], a[76:79], v[20:23]\n"
"L_st12_%=:\n s_cbranch_execz L_ret_%=\n"
"s_waitcnt lgkmcnt(3)\n"
"ds_read_b64_tr_b16 v[198:199], v200 offset:4512\n"
"v_mfma_f32_16x16x32_bf16 v[28:31], v[188:191], a[76:79], v[28:31]\n"
"ds_read_b64_tr_b16 a[168:169], v200 offset:4288\n"
"buffer_store_dwordx2 v[236:237], v135, s[44:47], s60 offen offset:0 sc0 sc1\n"
"v_mfma_f32_16x16x32_bf16 v[24:27], v[188:191], a[72:75], v[24:27]\n"
"s_waitcnt lgkmcnt(3)\n"
"s_mul_i32 s60, s68, 0x210\n"
"ds_read_b64_tr_b16 a[170:171], v200 offset:4544\n"
"v_mfma_f32_16x16x32_bf16 v[0:3], v[192:195], a[80:83], v[0:3]\n"
"s_mul_i32 s68, s58, 0x280\n"
"ds_read_b64_tr_b16 a[172:173], v200 offset:4320\n"
"buffer_store_dwordx4 v[240:243], v139, s[44:47], s82 offen offset:0 sc0 sc1\n"
"v_mfma_f32_16x16x32_bf16 v[4:7], v[192:195], a[84:87], v[4:7]\n"
"s_waitcnt lgkmcnt(3)\n"
"s_sub_u32 s68, s77, s68\n"
"ds_read_b64_tr_b16 a[174:175], v200 offset:4576\n"
"v_mfma_f32_16x16x32_bf16 v[12:15], v[196:199], a[84:87], v[12:15]\n"
"s_mul_i32 s69, s68, 0x80\n"
"s_add_u32 s69, s69, s19\n"
"ds_read_b64_tr_b16 a[240:241], v201 offset:4224\n"
"v_mfma_f32_16x16x32_bf16 v[8:11], v[196:199], a[80:83], v[8:11]\n"
"s_waitcnt lgkmcnt(3)\n"
"s_add_u32 s60, s60, s88\n"
"ds_read_b64_tr_b16 a[242:243], v201 offset:4480\n"
"v_mfma_f32_16x16x32_bf16 v[16:19], a[168:171], a[80:83], v[16:19]\n"
"ds_read_b64_tr_b16 a[244:245], v201 offset:4256\n"
"buffer_load_dwordx4 v212, s[40:43], s66 offen lds\n"
"v_mfma_f32_16x16x32_bf16 v[20:23], a[168:171], a[84:87], v[20:23]\n"
"s_waitcnt lgkmcnt(3)\n"
"s_add_u32 s66, s63, 0x12000\n"
"ds_read_b64_tr_b16 a[246:247], v201 offset:4512\n"
"v_mfma_f32_16x16x32_bf16 v[28:31], a[172:175], a[84:87], v[28:31]\n"
"s_add_u32 m0, s59, 0x13980\n"
"ds_read_b64_tr_b16 a[248:249], v201 offset:4288\n"
"v_mfma_f32_16x16x32_bf16 v[24:27], a[172:175], a[80:83], v[24:27]\n"
"s_waitcnt lgkmcnt(3)\n"
"ds_read_b64_tr_b16 a[250:251], v201 offset:4544\n"
"v_mfma_f32_16x16x32_bf16 v[0:3], a[240:243], a[88:91], v[0:3]\n"
"ds_read_b64_tr_b16 a[252:253], v201 offset:4320\n"
"v_mfma_f32_16x16x32_bf16 v[4:7], a[240:243], a[92:95], v[4:7]\n"
"s_waitcnt lgkmcnt(3)\n"
"ds_read_b64_tr_b16 a[254:255], v201 offset:4576\n"
"buffer_load_dwordx4 v211, s[28:31], s66 offen lds\n"
"v_mfma_f32_16x16x32_bf16 v[12:15], a[244:247], a[92:95], v[12:15]\n"
"s_add_u32 s66, s63, 0x1b000\n"
"s_add_u32 m0, s59, 0x16b00\n"
"ds_read_b64_tr_b16 v[176:177], v201 offset:25344\n"
"v_mfma_f32_16x16x32_bf16 v[8:11], a[244:247], a[88:91], v[8:11]\n"
"s_waitcnt lgkmcnt(3)\n"
"ds_read_b64_tr_b16 v[178:179], v201 offset:25600\n"
"v_mfma_f32_16x16x32_bf16 v[16:19], a[248:251], a[88:91], v[16:19]\n"
"ds_read_b64_tr_b16 v[180:181], v201 offset:25376\n"
"v_mfma_f32_16x16x32_bf16 v[20:23], a[248:251], a[92:95], v[20:23]\n"
"s_waitcnt lgkmcnt(3)\n"
"ds_read_b64_tr_b16 v[182:183], v201 offset:25632\n"
"v_mfma_f32_16x16x32_bf16 v[28:31], a[252:255], a[92:95], v[28:31]\n"
"ds_read_b64_tr_b16 v[184:185], v201 offset:25408\n"
"buffer_store_dwordx4 v[244:247], v139, s[44:47], s83 offen offset:0 sc0 sc1\n"
"v_mfma_f32_16x16x32_bf16 v[24:27], a[252:255], a[88:91], v[24:27]\n"
"s_waitcnt lgkmcnt(3)\n"
"ds_read_b64_tr_b16 v[186:187], v201 offset:25664\n"
"v_mfma_f32_16x16x32_bf16 v[0:3], v[176:179], a[96:99], v[0:3]\n"
"ds_read_b64_tr_b16 v[188:189], v201 offset:25440\n"
"v_mfma_f32_16x16x32_bf16 v[4:7], v[176:179], a[100:103], v[4:7]\n"
"s_waitcnt vmcnt(12) lgkmcnt(3)\n"
"ds_read_b64_tr_b16 v[190:191], v201 offset:25696\n"
"v_mfma_f32_16x16x32_bf16 v[12:15], v[180:183], a[100:103], v[12:15]\n"
"s_barrier\n"
"ds_read_b64_tr_b16 v[192:193], v201 offset:29568\n"
"buffer_load_dwordx4 v211, s[28:31], s66 offen lds\n"
"v_mfma_f32_16x16x32_bf16 v[8:11], v[180:183], a[96:99], v[8:11]\n"
"s_waitcnt lgkmcnt(3)\n"
"s_add_u32 s66, s63, 0x24000\n"
"ds_read_b64_tr_b16 v[194:195], v201 offset:29824\n"
"v_mfma_f32_16x16x32_bf16 v[16:19], v[184:187], a[96:99], v[16:19]\n"
"s_add_u32 m0, s59, 0x1ff80\n"
"ds_read_b64_tr_b16 v[196:197], v201 offset:29600\n"
"v_mfma_f32_16x16x32_bf16 v[20:23], v[184:187], a[100:103], v[20:23]\n"
"s_waitcnt lgkmcnt(3)\n"
"ds_read_b64_tr_b16 v[198:199], v201 offset:29856\n"
"v_mfma_f32_16x16x32_bf16 v[28:31], v[188:191], a[100:103], v[28:31]\n"
"ds_read_b64_tr_b16 a[168:169], v201 offset:29632\n"
"v_mfma_f32_16x16x32_bf16 v[24:27], v[188:191], a[96:99], v[24:27]\n"
"s_waitcnt lgkmcnt(3)\n"
"ds_read_b64_tr_b16 a[170:171], v201 offset:29888\n"
"buffer_load_dwordx4 v211, s[28:31], s66 offen lds\n"
"v_mfma_f32_16x16x32_bf16 v[0:3], v[192:195], a[104:107], v[0:3]\n"
"s_add_u32 s66, s63, 0x2d000\n"
"s_add_u32 m0, s59, 0x4200\n"
"ds_read_b64_tr_b16 a[172:173], v201 offset:29664\n"
"v_mfma_f32_16x16x32_bf16 v[4:7], v[192:195], a[108:111], v[4:7]\n"
"s_waitcnt lgkmcnt(3)\n"
"ds_read_b64_tr_b16 a[174:175], v201 offset:29920\n"
"v_mfma_f32_16x16x32_bf16 v[12:15], v[196:199], a[108:111], v[12:15]\n"
"ds_read_b64_tr_b16 a[240:241], v200 offset:8448\n"
"v_mfma_f32_16x16x32_bf16 v[8:11], v[196:199], a[104:107], v[8:11]\n"
"s_waitcnt lgkmcnt(3)\n"
"ds_read_b64_tr_b16 a[242:243], v200 offset:8704\n"
"v_mfma_f32_16x16x32_bf16 v[16:19], a[168:171], a[104:107], v[16:19]\n"
"ds_read_b64_tr_b16 a[244:245], v200 offset:8480\n"
"buffer_load_dwordx4 v211, s[28:31], s66 offen lds\n"
"v_mfma_f32_16x16x32_bf16 v[20:23], a[168:171], a[108:111], v[20:23]\n"
"s_waitcnt lgkmcnt(3)\n"
"s_add_u32 s66, s78, 0\n"
"ds_read_b64_tr_b16 a[246:247], v200 offset:8736\n"
"v_mfma_f32_16x16x32_bf16 v[28:31], a[172:175], a[108:111], v[28:31]\n"
"s_add_u32 m0, s59, 0x5280\n"
"ds_read_b64_tr_b16 a[248:249], v200 offset:8512\n"
"v_mfma_f32_16x16x32_bf16 v[24:27], a[172:175], a[104:107], v[24:27]\n"
"s_waitcnt lgkmcnt(3)\n"
"ds_read_b64_tr_b16 a[250:251], v200 offset:8768\n"
"v_mfma_f32_16x16x32_bf16 v[0:3], a[240:243], a[112:115], v[0:3]\n"
"ds_read_b64_tr_b16 a[252:253], v200 offset:8544\n"
"v_mfma_f32_16x16x32_bf16 v[4:7], a[240:243], a[116:119], v[4:7]\n"
"s_waitcnt lgkmcnt(3)\n"
"ds_read_b64_tr_b16 a[254:255], v200 offset:8800\n"
"buffer_load_dwordx4 v211, s[28:31], s66 offen lds\n"
"v_mfma_f32_16x16x32_bf16 v[12:15], a[244:247], a[116:119], v[12:15]\n"
"s_add_u32 s66, s78, 0x9000\n"
"s_add_u32 m0, s59, 0x17b80\n"
"ds_read_b64_tr_b16 v[176:177], v201 offset:50688\n"
"v_mfma_f32_16x16x32_bf16 v[8:11], a[244:247], a[112:115], v[8:11]\n"
"s_waitcnt lgkmcnt(3)\n"
"ds_read_b64_tr_b16 v[178:179], v201 offset:50944\n"
"v_mfma_f32_16x16x32_bf16 v[16:19], a[248:251], a[112:115], v[16:19]\n"
"ds_read_b64_tr_b16 v[180:181], v201 offset:50720\n"
"v_mfma_f32_16x16x32_bf16 v[20:23], a[248:251], a[116:119], v[20:23]\n"
"s_waitcnt lgkmcnt(3)\n"
"ds_read_b64_tr_b16 v[182:183], v201 offset:50976\n"
"v_mfma_f32_16x16x32_bf16 v[28:31], a[252:255], a[116:119], v[28:31]\n"
"ds_read_b64_tr_b16 v[184:185], v201 offset:50752\n"
"buffer_load_dwordx4 v211, s[28:31], s66 offen lds\n"
"v_mfma_f32_16x16x32_bf16 v[24:27], a[252:255], a[112:115], v[24:27]\n"
"s_waitcnt lgkmcnt(3)\n"
"s_add_u32 s66, s80, s77\n"
"ds_read_b64_tr_b16 v[186:187], v201 offset:51008\n"
"v_mfma_f32_16x16x32_bf16 v[0:3], v[176:179], a[120:123], v[0:3]\n"
"s_add_u32 m0, s59, 0x18c00\n"
"ds_read_b64_tr_b16 v[188:189], v201 offset:50784\n"
"v_mfma_f32_16x16x32_bf16 v[4:7], v[176:179], a[124:127], v[4:7]\n"
"s_waitcnt lgkmcnt(3)\n"
"ds_read_b64_tr_b16 v[190:191], v201 offset:51040\n"
"v_mfma_f32_16x16x32_bf16 v[12:15], v[180:183], a[124:127], v[12:15]\n"
"ds_read_b64_tr_b16 v[192:193], v201 offset:54912\n"
"v_mfma_f32_16x16x32_bf16 v[8:11], v[180:183], a[120:123], v[8:11]\n"
"s_waitcnt lgkmcnt(3)\n"
"ds_read_b64_tr_b16 v[194:195], v201 offset:55168\n"
"buffer_load_dwordx4 v212, s[40:43], s66 offen lds\n"
"v_mfma_f32_16x16x32_bf16 v[16:19], v[184:187], a[120:123], v[16:19]\n"
"s_add_u32 s66, s81, s77\n"
"s_add_u32 m0, s59, 0x19c80\n"
"ds_read_b64_tr_b16 v[196:197], v201 offset:54944\n"
"v_mfma_f32_16x16x32_bf16 v[20:23], v[184:187], a[124:127], v[20:23]\n"
"s_waitcnt lgkmcnt(3)\n"
"ds_read_b64_tr_b16 v[198:199], v201 offset:55200\n"
"v_mfma_f32_16x16x32_bf16 v[28:31], v[188:191], a[124:127], v[28:31]\n"
"ds_read_b64_tr_b16 a[168:169], v201 offset:54976\n"
"v_mfma_f32_16x16x32_bf16 v[24:27], v[188:191], a[120:123], v[24:27]\n"
"s_waitcnt lgkmcnt(3)\n"
"ds_read_b64_tr_b16 a[170:171], v201 offset:55232\n"
"v_mfma_f32_16x16x32_bf16 v[0:3], v[192:195], a[128:131], v[0:3]\n"
"ds_read_b64_tr_b16 a[172:173], v201 offset:55008\n"
"buffer_load_dwordx4 v213, s[40:43], s66 offen lds\n"
"v_mfma_f32_16x16x32_bf16 v[4:7], v[192:195], a[132:135], v[4:7]\n"
"s_waitcnt vmcnt(13) lgkmcnt(3)\n"
"s_add_u32 s66, s78, 0x12000\n"
"ds_read_b64_tr_b16 a[174:175], v201 offset:55264\n"
"v_mfma_f32_16x16x32_bf16 v[12:15], v[196:199], a[132:135], v[12:15]\n"
"s_barrier\n"
"s_add_u32 m0, s59, 0x21000\n"
"ds_read_b64_tr_b16 a[240:241], v202 offset:0\n"
"v_mfma_f32_16x16x32_bf16 v[8:11], v[196:199], a[128:131], v[8:11]\n"
"s_waitcnt lgkmcnt(3)\n"
"ds_read_b64_tr_b16 a[242:243], v202 offset:256\n"
"v_mfma_f32_16x16x32_bf16 v[16:19], a[168:171], a[128:131], v[16:19]\n"
"ds_read_b64_tr_b16 a[244:245], v202 offset:32\n"
"buffer_load_dwordx4 v211, s[28:31], s66 offen lds\n"
"v_mfma_f32_16x16x32_bf16 v[20:23], a[168:171], a[132:135], v[20:23]\n"
"s_waitcnt lgkmcnt(3)\n"
"s_add_u32 s66, s78, 0x1b000\n"
"ds_read_b64_tr_b16 a[246:247], v202 offset:288\n"
"v_mfma_f32_16x16x32_bf16 v[28:31], a[172:175], a[132:135], v[28:31]\n"
"s_add_u32 m0, s59, 0x23100\n"
"ds_read_b64_tr_b16 a[248:249], v202 offset:64\n"
"v_mfma_f32_16x16x32_bf16 v[24:27], a[172:175], a[128:131], v[24:27]\n"
"s_waitcnt lgkmcnt(3)\n"
"ds_read_b64_tr_b16 a[250:251], v202 offset:320\n"
"v_mfma_f32_16x16x32_bf16 v[0:3], a[240:243], a[136:139], v[0:3]\n"
"ds_read_b64_tr_b16 a[252:253], v202 offset:96\n"
"v_mfma_f32_16x16x32_bf16 v[4:7], a[240:243], a[140:143], v[4:7]\n"
"s_waitcnt lgkmcnt(3)\n"
"ds_read_b64_tr_b16 a[254:255], v202 offset:352\n"
"buffer_load_dwordx4 v211, s[28:31], s66 offen lds\n"
"v_mfma_f32_16x16x32_bf16 v[12:15], a[244:247], a[140:143], v[12:15]\n"
"s_add_u32 s66, s78, 0x24000\n"
"s_add_u32 m0, s59, 0x6300\n"
"ds_read_b64_tr_b16 v[176:177], v202 offset:4224\n"
"v_mfma_f32_16x16x32_bf16 v[8:11], a[244:247], a[136:139], v[8:11]\n"
"s_waitcnt lgkmcnt(3)\n"
"ds_read_b64_tr_b16 v[178:179], v202 offset:4480\n"
"v_mfma_f32_16x16x32_bf16 v[16:19], a[248:251], a[136:139], v[16:19]\n"
"ds_read_b64_tr_b16 v[180:181], v202 offset:4256\n"
"v_mfma_f32_16x16x32_bf16 v[20:23], a[248:251], a[140:143], v[20:23]\n"
"s_waitcnt lgkmcnt(3)\n"
"ds_read_b64_tr_b16 v[182:183], v202 offset:4512\n"
"v_mfma_f32_16x16x32_bf16 v[28:31], a[252:255], a[140:143], v[28:31]\n"
"ds_read_b64_tr_b16 v[184:185], v202 offset:4288\n"
"buffer_load_dwordx4 v211, s[28:31], s66 offen lds\n"
"v_mfma_f32_16x16x32_bf16 v[24:27], a[252:255], a[136:139], v[24:27]\n"
"s_waitcnt lgkmcnt(3)\n"
"s_add_u32 s66, s78, 0x2d000\n"
"ds_read_b64_tr_b16 v[186:187], v202 offset:4544\n"
"v_mfma_f32_16x16x32_bf16 v[0:3], v[176:179], a[144:147], v[0:3]\n"
"s_add_u32 m0, s59, 0x7380\n"
"ds_read_b64_tr_b16 v[188:189], v202 offset:4320\n"
"v_mfma_f32_16x16x32_bf16 v[4:7], v[176:179], a[148:151], v[4:7]\n"
"s_waitcnt lgkmcnt(3)\n"
"ds_read_b64_tr_b16 v[190:191], v202 offset:4576\n"
"v_mfma_f32_16x16x32_bf16 v[12:15], v[180:183], a[148:151], v[12:15]\n"
"ds_read_b64_tr_b16 v[192:193], v202 offset:8448\n"
"v_mfma_f32_16x16x32_bf16 v[8:11], v[180:183], a[144:147], v[8:11]\n"
"s_waitcnt lgkmcnt(3)\n"
"ds_read_b64_tr_b16 v[194:195], v202 offset:8704\n"
"buffer_store_dwordx2 v[236:237], v135, s[44:47], s14 offen offset:256 sc0 sc1\n"
"v_mfma_f32_16x16x32_bf16 v[16:19], v[184:187], a[144:147], v[16:19]\n"
"s_mul_i32 s14, s68, 0x210\n"
"s_add_u32 s14, s14, s88\n"
"ds_read_b64_tr_b16 v[196:197], v202 offset:8480\n"
"v_mfma_f32_16x16x32_bf16 v[20:23], v[184:187], a[148:151], v[20:23]\n"
"s_waitcnt lgkmcnt(3)\n"
"ds_read_b64_tr_b16 v[198:199], v202 offset:8736\n"
"buffer_load_dwordx4 v211, s[28:31], s66 offen lds\n"
"v_mfma_f32_16x16x32_bf16 v[28:31], v[188:191], a[148:151], v[28:31]\n"
"s_add_u32 s66, s63, 0x36000\n"
"s_add_u32 m0, s59, 0x8400\n"
"ds_read_b64_tr_b16 a[168:169], v202 offset:8512\n"
"v_mfma_f32_16x16x32_bf16 v[24:27], v[188:191], a[144:147], v[24:27]\n"
"s_waitcnt lgkmcnt(3)\n"
"ds_read_b64_tr_b16 a[170:171], v202 offset:8768\n"
"v_mfma_f32_16x16x32_bf16 v[0:3], v[192:195], a[152:155], v[0:3]\n"
"ds_read_b64_tr_b16 a[172:173], v202 offset:8544\n"
"v_mfma_f32_16x16x32_bf16 v[4:7], v[192:195], a[156:159], v[4:7]\n"
"s_waitcnt lgkmcnt(3)\n"
"ds_read_b64_tr_b16 a[174:175], v202 offset:8800\n"
"v_mfma_f32_16x16x32_bf16 v[12:15], v[196:199], a[156:159], v[12:15]\n"
"ds_read_b64_tr_b16 a[240:241], v200 offset:12672\n"
"buffer_load_dwordx4 v211, s[28:31], s66 offen lds\n"
"v_mfma_f32_16x16x32_bf16 v[8:11], v[196:199], a[152:155], v[8:11]\n"
"s_waitcnt lgkmcnt(3)\n"
"s_add_u32 s66, s63, 0x3f000\n"
"ds_read_b64_tr_b16 a[242:243], v200 offset:12928\n"
"v_mfma_f32_16x16x32_bf16 v[16:19], a[168:171], a[152:155], v[16:19]\n"
"s_add_u32 m0, s59, 0x9480\n"
"ds_read_b64_tr_b16 a[244:245], v200 offset:12704\n"
"v_mfma_f32_16x16x32_bf16 v[20:23], a[168:171], a[156:159], v[20:23]\n"
"s_waitcnt lgkmcnt(3)\n"
"ds_read_b64_tr_b16 a[246:247], v200 offset:12960\n"
"v_mfma_f32_16x16x32_bf16 v[28:31], a[172:175], a[156:159], v[28:31]\n"
"ds_read_b64_tr_b16 a[248:249], v200 offset:12736\n"
"v_mfma_f32_16x16x32_bf16 v[24:27], a[172:175], a[152:155], v[24:27]\n"
"s_waitcnt lgkmcnt(3)\n"
"ds_read_b64_tr_b16 a[250:251], v200 offset:12992\n"
"buffer_load_dwordx4 v211, s[28:31], s66 offen lds\n"
"v_mfma_f32_16x16x32_bf16 v[0:3], a[240:243], a[160:163], v[0:3]\n"
"s_add_u32 s66, s64, 0\n"
"s_add_u32 m0, s59, 0x22080\n"
"ds_read_b64_tr_b16 a[252:253], v200 offset:12768\n"
"v_mfma_f32_16x16x32_bf16 v[4:7], a[240:243], a[164:167], v[4:7]\n"
"s_waitcnt vmcnt(9) lgkmcnt(3)\n"
"ds_read_b64_tr_b16 a[254:255], v200 offset:13024\n"
"v_mfma_f32_16x16x32_bf16 v[12:15], a[244:247], a[164:167], v[12:15]\n"
"s_barrier\n"
"ds_read_b64_tr_b16 v[176:177], v201 offset:8512\n"
"v_cvt_pk_bf16_f32 v240, v0, v1\n"
"v_mfma_f32_16x16x32_bf16 v[8:11], a[244:247], a[160:163], v[8:11]\n"
"s_waitcnt lgkmcnt(3)\n"
"ds_read_b64_tr_b16 v[178:179], v201 offset:8768\n"
"v_cvt_pk_bf16_f32 v241, v2, v3\n"
"v_mfma_f32_16x16x32_bf16 v[16:19], a[248:251], a[160:163], v[16:19]\n"
"ds_read_b64_tr_b16 v[180:181], v201 offset:8544\n"
"buffer_load_dwordx4 v211, s[28:31], s66 offen lds\n"
"v_mfma_f32_16x16x32_bf16 v[20:23], a[248:251], a[164:167], v[20:23]\n"
"s_waitcnt lgkmcnt(3)\n"
"s_add_u32 s66, s78, 0x36000\n"
"ds_read_b64_tr_b16 v[182:183], v201 offset:8800\n"
"v_mfma_f32_16x16x32_bf16 v[28:31], a[252:255], a[164:167], v[28:31]\n"
"s_add_u32 m0, s59, 0xa500\n"
"ds_read_b64_tr_b16 v[184:185], v201 offset:12736\n"
"v_mfma_f32_16x16x32_bf16 v[24:27], a[252:255], a[160:163], v[24:27]\n"
"s_waitcnt lgkmcnt(3)\n"
"ds_read_b64_tr_b16 v[186:187], v201 offset:12992\n"
"v_cvt_pk_bf16_f32 v248, v16, v17\n"
"v_mfma_f32_16x16x32_bf16 v[48:51], v[176:179], a[0:3], 0\n"
"ds_read_b64_tr_b16 v[188:189], v201 offset:12768\n"
"v_cvt_pk_bf16_f32 v249, v18, v19\n"
"v_mfma_f32_16x16x32_bf16 v[52:55], v[176:179], a[4:7], 0\n"
"s_waitcnt lgkmcnt(3)\n"
"ds_read_b64_tr_b16 v[190:191], v201 offset:13024\n"
"buffer_load_dwordx4 v211, s[28:31], s66 offen lds\n"
"v_mfma_f32_16x16x32_bf16 v[60:63], v[180:183], a[4:7], 0\n"
"s_add_u32 s66, s78, 0x3f000\n"
"s_add_u32 m0, s59, 0xb580\n"
"ds_read_b64_tr_b16 v[192:193], v201 offset:16960\n"
"v_mfma_f32_16x16x32_bf16 v[56:59], v[180:183], a[0:3], 0\n"
"s_waitcnt lgkmcnt(3)\n"
"ds_read_b64_tr_b16 v[194:195], v201 offset:17216\n"
"v_cvt_pk_bf16_f32 v250, v24, v25\n"
"v_mfma_f32_16x16x32_bf16 v[48:51], v[184:187], a[8:11], v[48:51]\n"
"ds_read_b64_tr_b16 v[196:197], v201 offset:16992\n"
"v_cvt_pk_bf16_f32 v251, v26, v27\n"
"v_mfma_f32_16x16x32_bf16 v[52:55], v[184:187], a[12:15], v[52:55]\n"
"s_waitcnt lgkmcnt(3)\n"
"ds_read_b64_tr_b16 v[198:199], v201 offset:17248\n"
"v_cvt_pk_bf16_f32 v252, v20, v21\n"
"v_mfma_f32_16x16x32_bf16 v[60:63], v[188:191], a[12:15], v[60:63]\n"
"ds_read_b64_tr_b16 a[168:169], v201 offset:21184\n"
"v_cvt_pk_bf16_f32 v253, v22, v23\n"
"v_mfma_f32_16x16x32_bf16 v[56:59], v[188:191], a[8:11], v[56:59]\n"
"s_waitcnt lgkmcnt(3)\n"
"ds_read_b64_tr_b16 a[170:171], v201 offset:21440\n"
"buffer_store_dwordx2 v[236:237], v137, s[44:47], s88 offen offset:1024 sc0 sc1\n"
"v_mfma_f32_16x16x32_bf16 v[48:51], v[192:195], a[16:19], v[48:51]\n"
"ds_read_b64_tr_b16 a[172:173], v201 offset:21216\n"
"v_cvt_pk_bf16_f32 v254, v28, v29\n"
"v_mfma_f32_16x16x32_bf16 v[52:55], v[192:195], a[20:23], v[52:55]\n"
"s_waitcnt lgkmcnt(3)\n"
"ds_read_b64_tr_b16 a[174:175], v201 offset:21472\n"
"buffer_store_dwordx4 v[248:251], v139, s[44:47], s84 offen offset:0 sc0 sc1\n"
"v_mfma_f32_16x16x32_bf16 v[60:63], v[196:199], a[20:23], v[60:63]\n"
"s_add_u32 s84, s95, 0x400000\n"
"ds_read_b64_tr_b16 a[240:241], v201 offset:33856\n"
"v_cvt_pk_bf16_f32 v255, v30, v31\n"
"v_mfma_f32_16x16x32_bf16 v[56:59], v[196:199], a[16:19], v[56:59]\n"
"s_waitcnt lgkmcnt(3)\n"
"ds_read_b64_tr_b16 a[242:243], v201 offset:34112\n"
"v_cvt_pk_bf16_f32 v242, v8, v9\n"
"v_mfma_f32_16x16x32_bf16 v[48:51], a[168:171], a[24:27], v[48:51]\n"
"s_add_u32 s95, s69, s67\n"
"ds_read_b64_tr_b16 a[244:245], v201 offset:33888\n"
"v_cvt_pk_bf16_f32 v243, v10, v11\n"
"v_mfma_f32_16x16x32_bf16 v[52:55], a[168:171], a[28:31], v[52:55]\n"
"s_waitcnt lgkmcnt(3)\n"
"ds_read_b64_tr_b16 a[246:247], v201 offset:34144\n"
"buffer_load_dwordx4 v211, s[28:31], s66 offen lds\n"
"v_mfma_f32_16x16x32_bf16 v[60:63], a[172:175], a[28:31], v[60:63]\n"
"s_add_u32 s66, s79, 0\n"
"s_add_u32 m0, s59, 0xc600\n"
"ds_read_b64_tr_b16 a[248:249], v202 offset:12736\n"
"v_mfma_f32_16x16x32_bf16 v[56:59], a[172:175], a[24:27], v[56:59]\n"
"s_waitcnt lgkmcnt(3)\n"
"ds_read_b64_tr_b16 a[250:251], v202 offset:12992\n"
"v_cvt_pk_bf16_f32 v244, v4, v5\n"
"v_mfma_f32_16x16x32_bf16 v[48:51], a[240:243], a[32:35], v[48:51]\n"
"ds_read_b64_tr_b16 a[252:253], v202 offset:12768\n"
"v_cvt_pk_bf16_f32 v245, v6, v7\n"
"v_mfma_f32_16x16x32_bf16 v[52:55], a[240:243], a[36:39], v[52:55]\n"
"s_waitcnt lgkmcnt(3)\n"
"ds_read_b64_tr_b16 a[254:255], v202 offset:13024\n"
"v_cvt_pk_bf16_f32 v246, v12, v13\n"
"v_mfma_f32_16x16x32_bf16 v[60:63], a[244:247], a[36:39], v[60:63]\n"
"ds_read_b64_tr_b16 v[176:177], v200 offset:16960\n"
"buffer_load_dwordx4 v211, s[28:31], s66 offen lds\n"
"v_mfma_f32_16x16x32_bf16 v[56:59], a[244:247], a[32:35], v[56:59]\n"
"s_waitcnt lgkmcnt(3)\n"
"ds_read_b64_tr_b16 v[178:179], v200 offset:17216\n"
"v_cvt_pk_bf16_f32 v247, v14, v15\n"
"v_mfma_f32_16x16x32_bf16 v[48:51], a[248:251], a[40:43], v[48:51]\n"
"ds_read_b64_tr_b16 v[180:181], v200 offset:16992\n"
"v_mfma_f32_16x16x32_bf16 v[52:55], a[248:251], a[44:47], v[52:55]\n"
"s_waitcnt lgkmcnt(3)\n"
"ds_read_b64_tr_b16 v[182:183], v200 offset:17248\n"
"v_mfma_f32_16x16x32_bf16 v[60:63], a[252:255], a[44:47], v[60:63]\n"
"ds_read_b64_tr_b16 v[184:185], v200 offset:21184\n"
"v_mfma_f32_16x16x32_bf16 v[56:59], a[252:255], a[40:43], v[56:59]\n"
"s_waitcnt lgkmcnt(3)\n"
"ds_read_b64_tr_b16 v[186:187], v200 offset:21440\n"
"v_mfma_f32_16x16x32_bf16 v[48:51], v[176:179], a[48:51], v[48:51]\n"
"ds_read_b64_tr_b16 v[188:189], v200 offset:21216\n"
"v_mfma_f32_16x16x32_bf16 v[52:55], v[176:179], a[52:55], v[52:55]\n"
"s_waitcnt vmcnt(6) lgkmcnt(3)\n"
"ds_read_b64_tr_b16 v[190:191], v200 offset:21472\n"
"buffer_store_dwordx4 v[252:255], v139, s[44:47], s84 offen offset:0 sc0 sc1\n"
"v_mfma_f32_16x16x32_bf16 v[60:63], v[180:183], a[52:55], v[60:63]\n"
"s_barrier\n"
"s_add_u32 s84, s89, 0x500000\n"
"ds_read_b64_tr_b16 v[192:193], v201 offset:38080\n"
"v_mfma_f32_16x16x32_bf16 v[56:59], v[180:183], a[48:51], v[56:59]\n"
"s_waitcnt lgkmcnt(3)\n"
"ds_read_b64_tr_b16 v[194:195], v201 offset:38336\n"
"v_mfma_f32_16x16x32_bf16 v[48:51], v[184:187], a[56:59], v[48:51]\n"
"ds_read_b64_tr_b16 v[196:197], v201 offset:38112\n"
"v_mfma_f32_16x16x32_bf16 v[52:55], v[184:187], a[60:63], v[52:55]\n"
"s_waitcnt lgkmcnt(3)\n"
"ds_read_b64_tr_b16 v[198:199], v201 offset:38368\n"
"v_mfma_f32_16x16x32_bf16 v[60:63], v[188:191], a[60:63], v[60:63]\n"
"ds_read_b64_tr_b16 a[168:169], v201 offset:42304\n"
"v_mfma_f32_16x16x32_bf16 v[56:59], v[188:191], a[56:59], v[56:59]\n"
"s_waitcnt lgkmcnt(3)\n"
"ds_read_b64_tr_b16 a[170:171], v201 offset:42560\n"
"v_mfma_f32_16x16x32_bf16 v[48:51], v[192:195], a[64:67], v[48:51]\n"
"ds_read_b64_tr_b16 a[172:173], v201 offset:42336\n"
"v_mfma_f32_16x16x32_bf16 v[52:55], v[192:195], a[68:71], v[52:55]\n"
"s_waitcnt lgkmcnt(3)\n"
"ds_read_b64_tr_b16 a[174:175], v201 offset:42592\n"
"v_mfma_f32_16x16x32_bf16 v[60:63], v[196:199], a[68:71], v[60:63]\n"
"ds_read_b64_tr_b16 a[240:241], v201 offset:46528\n"
"v_mfma_f32_16x16x32_bf16 v[56:59], v[196:199], a[64:67], v[56:59]\n"
"s_waitcnt lgkmcnt(3)\n"
"ds_read_b64_tr_b16 a[242:243], v201 offset:46784\n"
"v_mfma_f32_16x16x32_bf16 v[48:51], a[168:171], a[72:75], v[48:51]\n"
"ds_read_b64_tr_b16 a[244:245], v201 offset:46560\n"
"v_mfma_f32_16x16x32_bf16 v[52:55], a[168:171], a[76:79], v[52:55]\n"
"s_waitcnt lgkmcnt(3)\n"
"ds_read_b64_tr_b16 a[246:247], v201 offset:46816\n"
"v_mfma_f32_16x16x32_bf16 v[60:63], a[172:175], a[76:79], v[60:63]\n"
"ds_read_b64_tr_b16 a[248:249], v202 offset:16960\n"
"s_mov_b32 exec_hi, 0\n s_mov_b32 exec_lo, s93\n buffer_load_dwordx2 v[230:231], v138, s[44:47], s16 offen offset:0 sc0 sc1\n s_mov_b64 exec, -1\n"
"v_mfma_f32_16x16x32_bf16 v[56:59], a[172:175], a[72:75], v[56:59]\n"
"s_waitcnt lgkmcnt(3)\n"
"ds_read_b64_tr_b16 a[250:251], v202 offset:17216\n"
"v_mfma_f32_16x16x32_bf16 v[48:51], a[240:243], a[80:83], v[48:51]\n"
"ds_read_b64_tr_b16 a[252:253], v202 offset:16992\n"
"v_mfma_f32_16x16x32_bf16 v[52:55], a[240:243], a[84:87], v[52:55]\n"
"s_waitcnt lgkmcnt(3)\n"
"ds_read_b64_tr_b16 a[254:255], v202 offset:17248\n"
"buffer_store_dwordx4 v[240:243], v139, s[44:47], s84 offen offset:0 sc0 sc1\n"
"v_mfma_f32_16x16x32_bf16 v[60:63], a[244:247], a[84:87], v[60:63]\n"
"s_add_u32 s84, s89, 0xd00000\n"
"s_add_u32 s89, s69, s9\n"
"ds_read_b64_tr_b16 v[176:177], v202 offset:25408\n"
"v_mfma_f32_16x16x32_bf16 v[56:59], a[244:247], a[80:83], v[56:59]\n"
"s_waitcnt lgkmcnt(3)\n"
"ds_read_b64_tr_b16 v[178:179], v202 offset:25664\n"
"v_mfma_f32_16x16x32_bf16 v[48:51], a[248:251], a[88:91], v[48:51]\n"
"ds_read_b64_tr_b16 v[180:181], v202 offset:25440\n"
"v_mfma_f32_16x16x32_bf16 v[52:55], a[248:251], a[92:95], v[52:55]\n"
"s_waitcnt lgkmcnt(3)\n"
"ds_read_b64_tr_b16 v[182:183], v202 offset:25696\n"
"v_mfma_f32_16x16x32_bf16 v[60:63], a[252:255], a[92:95], v[60:63]\n"
"ds_read_b64_tr_b16 v[184:185], v200 offset:25408\n"
"v_mfma_f32_16x16x32_bf16 v[56:59], a[252:255], a[88:91], v[56:59]\n"
"s_waitcnt lgkmcnt(3)\n"
"ds_read_b64_tr_b16 v[186:187], v200 offset:25664\n"
"v_mfma_f32_16x16x32_bf16 v[48:51], v[176:179], a[96:99], v[48:51]\n"
"ds_read_b64_tr_b16 v[188:189], v200 offset:25440\n"
"v_mfma_f32_16x16x32_bf16 v[52:55], v[176:179], a[100:103], v[52:55]\n"
"s_waitcnt lgkmcnt(3)\n"
"ds_read_b64_tr_b16 v[190:191], v200 offset:25696\n"
"v_mfma_f32_16x16x32_bf16 v[60:63], v[180:183], a[100:103], v[60:63]\n"
"ds_read_b64_tr_b16 v[192:193], v200 offset:29632\n"
"v_mfma_f32_16x16x32_bf16 v[56:59], v[180:183], a[96:99], v[56:59]\n"
"s_waitcnt lgkmcnt(3)\n"
"ds_read_b64_tr_b16 v[194:195], v200 offset:29888\n"
"v_mfma_f32_16x16x32_bf16 v[48:51], v[184:187], a[104:107], v[48:51]\n"
"ds_read_b64_tr_b16 v[196:197], v200 offset:29664\n"
"v_mfma_f32_16x16x32_bf16 v[52:55], v[184:187], a[108:111], v[52:55]\n"
"s_waitcnt lgkmcnt(3)\n"
"ds_read_b64_tr_b16 v[198:199], v200 offset:29920\n"
"v_mfma_f32_16x16x32_bf16 v[60:63], v[188:191], a[108:111], v[60:63]\n"
"ds_read_b64_tr_b16 a[168:169], v200 offset:33856\n"
"v_mfma_f32_16x16x32_bf16 v[56:59], v[188:191], a[104:107], v[56:59]\n"
"s_waitcnt lgkmcnt(3)\n"
"ds_read_b64_tr_b16 a[170:171], v200 offset:34112\n"
"v_mfma_f32_16x16x32_bf16 v[48:51], v[192:195], a[112:115], v[48:51]\n"
"ds_read_b64_tr_b16 a[172:173], v200 offset:33888\n"
"v_mfma_f32_16x16x32_bf16 v[52:55], v[192:195], a[116:119], v[52:55]\n"
"s_waitcnt vmcnt(3) lgkmcnt(3)\n"
"ds_read_b64_tr_b16 a[174:175], v200 offset:34144\n"
"buffer_store_dwordx4 v[244:247], v139, s[44:47], s84 offen offset:0 sc0 sc1\n"
"v_mfma_f32_16x16x32_bf16 v[60:63], v[196:199], a[116:119], v[60:63]\n"
"s_barrier\n"
"s_add_u32 s84, s95, 0xe00000\n"
"ds_read_b64_tr_b16 a[240:241], v200 offset:38080\n"
"v_mfma_f32_16x16x32_bf16 v[56:59], v[196:199], a[112:115], v[56:59]\n"
"s_waitcnt lgkmcnt(3)\n"
"ds_read_b64_tr_b16 a[242:243], v200 offset:38336\n"
"v_mfma_f32_16x16x32_bf16 v[48:51], a[168:171], a[120:123], v[48:51]\n"
"ds_read_b64_tr_b16 a[244:245], v200 offset:38112\n"
"v_mfma_f32_16x16x32_bf16 v[52:55], a[168:171], a[124:127], v[52:55]\n"
"s_waitcnt lgkmcnt(3)\n"
"ds_read_b64_tr_b16 a[246:247], v200 offset:38368\n"
"v_mfma_f32_16x16x32_bf16 v[60:63], a[172:175], a[124:127], v[60:63]\n"
"ds_read_b64_tr_b16 a[248:249], v202 offset:21184\n"
"v_mfma_f32_16x16x32_bf16 v[56:59], a[172:175], a[120:123], v[56:59]\n"
"s_waitcnt lgkmcnt(3)\n"
"ds_read_b64_tr_b16 a[250:251], v202 offset:21440\n"
"v_mfma_f32_16x16x32_bf16 v[48:51], a[240:243], a[128:131], v[48:51]\n"
"ds_read_b64_tr_b16 a[252:253], v202 offset:21216\n"
"v_mfma_f32_16x16x32_bf16 v[52:55], a[240:243], a[132:135], v[52:55]\n"
"s_waitcnt lgkmcnt(3)\n"
"ds_read_b64_tr_b16 a[254:255], v202 offset:21472\n"
"v_mfma_f32_16x16x32_bf16 v[60:63], a[244:247], a[132:135], v[60:63]\n"
"ds_read_b64_tr_b16 v[176:177], v200 offset:42304\n"
"v_mfma_f32_16x16x32_bf16 v[56:59], a[244:247], a[128:131], v[56:59]\n"
"s_waitcnt lgkmcnt(3)\n"
"ds_read_b64_tr_b16 v[178:179], v200 offset:42560\n"
"v_mfma_f32_16x16x32_bf16 v[48:51], a[248:251], a[136:139], v[48:51]\n"
"ds_read_b64_tr_b16 v[180:181], v200 offset:42336\n"
"v_mfma_f32_16x16x32_bf16 v[52:55], a[248:251], a[140:143], v[52:55]\n"
"s_waitcnt lgkmcnt(3)\n"
"ds_read_b64_tr_b16 v[182:183], v200 offset:42592\n"
"v_mfma_f32_16x16x32_bf16 v[60:63], a[252:255], a[140:143], v[60:63]\n"
"ds_read_b64_tr_b16 v[184:185], v200 offset:46528\n"
"v_mfma_f32_16x16x32_bf16 v[56:59], a[252:255], a[136:139], v[56:59]\n"
"s_waitcnt lgkmcnt(3)\n"
"ds_read_b64_tr_b16 v[186:187], v200 offset:46784\n"
"v_mfma_f32_16x16x32_bf16 v[48:51], v[176:179], a[144:147], v[48:51]\n"
"ds_read_b64_tr_b16 v[188:189], v200 offset:46560\n"
"v_mfma_f32_16x16x32_bf16 v[52:55], v[176:179], a[148:151], v[52:55]\n"
"s_waitcnt lgkmcnt(3)\n"
"ds_read_b64_tr_b16 v[190:191], v200 offset:46816\n"
"v_mfma_f32_16x16x32_bf16 v[60:63], v[180:183], a[148:151], v[60:63]\n"
"ds_read_b64_tr_b16 v[192:193], v200 offset:50752\n"
"v_mfma_f32_16x16x32_bf16 v[56:59], v[180:183], a[144:147], v[56:59]\n"
"s_waitcnt lgkmcnt(3)\n"
"ds_read_b64_tr_b16 v[194:195], v200 offset:51008\n"
"v_mfma_f32_16x16x32_bf16 v[48:51], v[184:187], a[152:155], v[48:51]\n"
"ds_read_b64_tr_b16 v[196:197], v200 offset:50784\n"
"v_mfma_f32_16x16x32_bf16 v[52:55], v[184:187], a[156:159], v[52:55]\n"
"s_waitcnt lgkmcnt(3)\n"
"ds_read_b64_tr_b16 v[198:199], v200 offset:51040\n"
"v_mfma_f32_16x16x32_bf16 v[60:63], v[188:191], a[156:159], v[60:63]\n"
"ds_read_b64_tr_b16 a[168:169], v201 offset:8448\n"
"v_mfma_f32_16x16x32_bf16 v[56:59], v[188:191], a[152:155], v[56:59]\n"
"s_waitcnt lgkmcnt(3)\n"
"ds_read_b64_tr_b16 a[170:171], v201 offset:8704\n"
"v_mfma_f32_16x16x32_bf16 v[48:51], v[192:195], a[160:163], v[48:51]\n"
"ds_read_b64_tr_b16 a[172:173], v201 offset:8480\n"
"v_mfma_f32_16x16x32_bf16 v[52:55], v[192:195], a[164:167], v[52:55]\n"
"s_waitcnt lgkmcnt(3)\n"
"ds_read_b64_tr_b16 a[174:175], v201 offset:8736\n"
"v_mfma_f32_16x16x32_bf16 v[60:63], v[196:199], a[164:167], v[60:63]\n"
"ds_read_b64_tr_b16 a[240:241], v201 offset:12672\n"
"v_mfma_f32_16x16x32_bf16 v[56:59], v[196:199], a[160:163], v[56:59]\n"
"s_waitcnt lgkmcnt(3)\n"
"ds_read_b64_tr_b16 a[242:243], v201 offset:12928\n"
"v_cvt_pk_bf16_f32 v248, v48, v49\n"
"v_cvt_pk_bf16_f32 v249, v50, v51\n"
"v_mfma_f32_16x16x32_bf16 v[96:99], a[168:171], a[0:3], 0\n"
"ds_read_b64_tr_b16 a[244:245], v201 offset:12704\n"
"v_cvt_pk_bf16_f32 v252, v52, v53\n"
"v_mfma_f32_16x16x32_bf16 v[100:103], a[168:171], a[4:7], 0\n"
"s_waitcnt lgkmcnt(3)\n"
"ds_read_b64_tr_b16 a[246:247], v201 offset:12960\n"
"v_cvt_pk_bf16_f32 v250, v56, v57\n"
"v_cvt_pk_bf16_f32 v251, v58, v59\n"
"v_mfma_f32_16x16x32_bf16 v[108:111], a[172:175], a[4:7], 0\n"
"ds_read_b64_tr_b16 a[248:249], v201 offset:16896\n"
"v_cvt_pk_bf16_f32 v253, v54, v55\n"
"v_mfma_f32_16x16x32_bf16 v[104:107], a[172:175], a[0:3], 0\n"
"s_waitcnt lgkmcnt(3)\n"
"ds_read_b64_tr_b16 a[250:251], v201 offset:17152\n"
"v_cvt_pk_bf16_f32 v254, v60, v61\n"
"v_mfma_f32_16x16x32_bf16 v[96:99], a[240:243], a[8:11], v[96:99]\n"
"ds_read_b64_tr_b16 a[252:253], v201 offset:16928\n"
"v_cvt_pk_bf16_f32 v255, v62, v63\n"
"v_mfma_f32_16x16x32_bf16 v[100:103], a[240:243], a[12:15], v[100:103]\n"
"s_waitcnt vmcnt(2) lgkmcnt(3)\n"
"ds_read_b64_tr_b16 a[254:255], v201 offset:17184\n"
"v_xor_b32 v239, v230, v231\n"
"v_mfma_f32_16x16x32_bf16 v[108:111], a[244:247], a[12:15], v[108:111]\n"
"ds_read_b64_tr_b16 v[176:177], v201 offset:21120\n"
"v_subrev_u32 v238, s18, v230\n"
"v_mfma_f32_16x16x32_bf16 v[104:107], a[244:247], a[8:11], v[104:107]\n"
"L_st13_%=:\n s_cbranch_execz L_ret_%=\n"
"s_waitcnt lgkmcnt(3)\n"
"ds_read_b64_tr_b16 v[178:179], v201 offset:21376\n"
"buffer_store_dwordx4 v[248:251], v139, s[44:47], s84 offen offset:0 sc0 sc1\n"
"v_mfma_f32_16x16x32_bf16 v[96:99], a[248:251], a[16:19], v[96:99]\n"
"s_add_u32 s84, s95, 0x600000\n"
"ds_read_b64_tr_b16 v[180:181], v201 offset:21152\n"
"v_mfma_f32_16x16x32_bf16 v[100:103], a[248:251], a[20:23], v[100:103]\n"
"s_waitcnt lgkmcnt(3)\n"
"ds_read_b64_tr_b16 v[182:183], v201 offset:21408\n"
"v_xor_b32 v239, 0x5bd1e995, v239\n"
"v_mfma_f32_16x16x32_bf16 v[108:111], a[252:255], a[20:23], v[108:111]\n"
"ds_read_b64_tr_b16 v[184:185], v201 offset:33792\n"
"ds_read_b64_tr_b16 v[186:187], v201 offset:34048\n"
"v_max_u32 v238, v238, v239\n"
"v_mfma_f32_16x16x32_bf16 v[104:107], a[252:255], a[16:19], v[104:107]\n"
"s_waitcnt lgkmcnt(4)\n"
"ds_read_b64_tr_b16 v[188:189], v201 offset:33824\n"
"ds_read_b64_tr_b16 v[190:191], v201 offset:34080\n"
"v_cmp_ge_u32 s[10:11], v228, v238\n"
"v_mfma_f32_16x16x32_bf16 v[96:99], v[176:179], a[24:27], v[96:99]\n"
"ds_read_b64_tr_b16 v[192:193], v202 offset:12672\n"
"ds_read_b64_tr_b16 v[194:195], v202 offset:12928\n"
"v_mfma_f32_16x16x32_bf16 v[100:103], v[176:179], a[28:31], v[100:103]\n"
"s_waitcnt lgkmcnt(6)\n"
"ds_read_b64_tr_b16 v[196:197], v202 offset:12704\n"
"buffer_store_dwordx4 v[252:255], v139, s[44:47], s84 offen offset:0 sc0 sc1\n"
"v_mfma_f32_16x16x32_bf16 v[108:111], v[180:183], a[28:31], v[108:111]\n"
"ds_read_b64_tr_b16 v[198:199], v202 offset:12960\n"
"ds_read_b64_tr_b16 a[168:169], v200 offset:16896\n"
"v_mfma_f32_16x16x32_bf16 v[104:107], v[180:183], a[24:27], v[104:107]\n"
"s_waitcnt lgkmcnt(7)\n"
"ds_read_b64_tr_b16 a[170:171], v200 offset:17152\n"
"ds_read_b64_tr_b16 a[172:173], v200 offset:16928\n"
"v_mfma_f32_16x16x32_bf16 v[96:99], v[184:187], a[32:35], v[96:99]\n"
"ds_read_b64_tr_b16 a[174:175], v200 offset:17184\n"
"ds_read_b64_tr_b16 a[240:241], v200 offset:21120\n"
"v_mfma_f32_16x16x32_bf16 v[100:103], v[184:187], a[36:39], v[100:103]\n"
"s_waitcnt lgkmcnt(9)\n"
"ds_read_b64_tr_b16 a[242:243], v200 offset:21376\n"
"v_mfma_f32_16x16x32_bf16 v[108:111], v[188:191], a[36:39], v[108:111]\n"
"ds_read_b64 a[176:177], v229 offset:32\n"
"v_mfma_f32_16x16x32_bf16 v[104:107], v[188:191], a[32:35], v[104:107]\n"
"s_waitcnt lgkmcnt(8)\n"
"s_andn2_b32 s10, s93, s10\n s_cbranch_scc1 L_slow4_%=\n L_back4_%=:\n"
"ds_read_b64 a[206:207], v229 offset:0\n"
"v_mfma_f32_16x16x32_bf16 v[96:99], v[192:195], a[40:43], v[96:99]\n"
"ds_read_b64 a[208:209], v229 offset:1056\n"
"v_mfma_f32_16x16x32_bf16 v[100:103], v[192:195], a[44:47], v[100:103]\n"
"s_waitcnt lgkmcnt(6)\n"
"ds_read_b64 a[238:239], v229 offset:1024\n"
"buffer_load_dwordx4 a[178:181], v139, s[44:47], s94 offen offset:0 sc0 sc1\n"
"v_mfma_f32_16x16x32_bf16 v[108:111], v[196:199], a[44:47], v[108:111]\n"
"s_add_u32 s94, s86, 0x800000\n"
"ds_read_b64 v[64:65], v229 offset:2080\n"
"v_mfma_f32_16x16x32_bf16 v[104:107], v[196:199], a[40:43], v[104:107]\n"
"ds_read_b64 v[94:95], v229 offset:2048\n"
"v_mfma_f32_16x16x32_bf16 v[96:99], a[168:171], a[48:51], v[96:99]\n"
"ds_read_b64_tr_b16 a[244:245], v200 offset:21152\n"
"v_mfma_f32_16x16x32_bf16 v[100:103], a[168:171], a[52:55], v[100:103]\n"
"s_waitcnt lgkmcnt(5)\n"
"ds_read_b64_tr_b16 a[246:247], v200 offset:21408\n"
"v_mfma_f32_16x16x32_bf16 v[108:111], a[172:175], a[52:55], v[108:111]\n"
"ds_read_b64_tr_b16 a[248:249], v201 offset:38016\n"
"buffer_load_dwordx4 a[210:213], v139, s[44:47], s94 offen offset:0 sc0 sc1\n"
"v_mfma_f32_16x16x32_bf16 v[104:107], a[172:175], a[48:51], v[104:107]\n"
"s_add_u32 s94, s86, 0x100000\n"
"ds_read_b64_tr_b16 a[250:251], v201 offset:38272\n"
"v_mfma_f32_16x16x32_bf16 v[96:99], a[240:243], a[56:59], v[96:99]\n"
"ds_read_b64_tr_b16 a[252:253], v201 offset:38048\n"
"v_mfma_f32_16x16x32_bf16 v[100:103], a[240:243], a[60:63], v[100:103]\n"
"s_waitcnt lgkmcnt(3)\n"
"ds_read_b64_tr_b16 a[254:255], v201 offset:38304\n"
"v_mfma_f32_16x16x32_bf16 v[108:111], a[244:247], a[60:63], v[108:111]\n"
"ds_read_b64_tr_b16 v[176:177], v201 offset:42240\n"
"v_mfma_f32_16x16x32_bf16 v[104:107], a[244:247], a[56:59], v[104:107]\n"
"s_waitcnt lgkmcnt(3)\n"
"ds_read_b64_tr_b16 v[178:179], v201 offset:42496\n"
"buffer_load_dwordx4 a[182:185], v139, s[44:47], s94 offen offset:0 sc0 sc1\n"
"v_mfma_f32_16x16x32_bf16 v[96:99], a[248:251], a[64:67], v[96:99]\n"
"s_add_u32 s94, s86, 0x900000\n"
"ds_read_b64_tr_b16 v[180:181], v201 offset:42272\n"
"v_mfma_f32_16x16x32_bf16 v[100:103], a[248:251], a[68:71], v[100:103]\n"
"s_waitcnt lgkmcnt(3)\n"
"ds_read_b64_tr_b16 v[182:183], v201 offset:42528\n"
"v_mfma_f32_16x16x32_bf16 v[108:111], a[252:255], a[68:71], v[108:111]\n"
"ds_read_b64_tr_b16 v[184:185], v201 offset:46464\n"
"v_mfma_f32_16x16x32_bf16 v[104:107], a[252:255], a[64:67], v[104:107]\n"
"s_waitcnt lgkmcnt(3)\n"
"ds_read_b64_tr_b16 v[186:187], v201 offset:46720\n"
"v_mfma_f32_16x16x32_bf16 v[96:99], v[176:179], a[72:75], v[96:99]\n"
"ds_read_b64_tr_b16 v[188:189], v201 offset:46496\n"
"s_mov_b64 exec, 0x70\n buffer_load_dwordx2 v[230:231], v138, s[44:47], s16 offen offset:0 sc0 sc1\n s_mov_b64 exec, -1\n"
"v_mfma_f32_16x16x32_bf16 v[100:103], v[176:179], a[76:79], v[100:103]\n"
"s_waitcnt lgkmcnt(3)\n"
"ds_read_b64_tr_b16 v[190:191], v201 offset:46752\n"
"v_mfma_f32_16x16x32_bf16 v[108:111], v[180:183], a[76:79], v[108:111]\n"
"ds_read_b64_tr_b16 v[192:193], v202 offset:16896\n"
"buffer_load_dwordx4 a[214:217], v139, s[44:47], s94 offen offset:0 sc0 sc1\n"
"v_mfma_f32_16x16x32_bf16 v[104:107], v[180:183], a[72:75], v[104:107]\n"
"s_waitcnt lgkmcnt(3)\n"
"s_add_u32 s94, s86, 0x200000\n"
"ds_read_b64_tr_b16 v[194:195], v202 offset:17152\n"
"v_mfma_f32_16x16x32_bf16 v[96:99], v[184:187], a[80:83], v[96:99]\n"
"ds_read_b64_tr_b16 v[196:197], v202 offset:16928\n"
"v_mfma_f32_16x16x32_bf16 v[100:103], v[184:187], a[84:87], v[100:103]\n"
"s_waitcnt lgkmcnt(3)\n"
"ds_read_b64_tr_b16 v[198:199], v202 offset:17184\n"
"v_mfma_f32_16x16x32_bf16 v[108:111], v[188:191], a[84:87], v[108:111]\n"
"ds_read_b64_tr_b16 a[168:169], v202 offset:25344\n"
"v_mfma_f32_16x16x32_bf16 v[104:107], v[188:191], a[80:83], v[104:107]\n"
"s_waitcnt lgkmcnt(3)\n"
"ds_read_b64_tr_b16 a[170:171], v202 offset:25600\n"
"buffer_load_dwordx4 a[186:189], v139, s[44:47], s94 offen offset:0 sc0 sc1\n"
"v_mfma_f32_16x16x32_bf16 v[96:99], v[192:195], a[88:91], v[96:99]\n"
"s_add_u32 s94, s86, 0xa00000\n"
"ds_read_b64_tr_b16 a[172:173], v202 offset:25376\n"
"v_mfma_f32_16x16x32_bf16 v[100:103], v[192:195], a[92:95], v[100:103]\n"
"s_waitcnt lgkmcnt(3)\n"
"ds_read_b64_tr_b16 a[174:175], v202 offset:25632\n"
"v_mfma_f32_16x16x32_bf16 v[108:111], v[196:199], a[92:95], v[108:111]\n"
"ds_read_b64_tr_b16 a[240:241], v200 offset:25344\n"
"v_mfma_f32_16x16x32_bf16 v[104:107], v[196:199], a[88:91], v[104:107]\n"
"s_waitcnt lgkmcnt(3)\n"
"ds_read_b64_tr_b16 a[242:243], v200 offset:25600\n"
"v_mfma_f32_16x16x32_bf16 v[96:99], a[168:171], a[96:99], v[96:99]\n"
"ds_read_b64_tr_b16 a[244:245], v200 offset:25376\n"
"buffer_load_dwordx4 a[218:221], v139, s[44:47], s94 offen offset:0 sc0 sc1\n"
"v_mfma_f32_16x16x32_bf16 v[100:103], a[168:171], a[100:103], v[100:103]\n"
"s_waitcnt lgkmcnt(3)\n"
"s_add_u32 s94, s86, 0x300000\n"
"ds_read_b64_tr_b16 a[246:247], v200 offset:25632\n"
"v_mfma_f32_16x16x32_bf16 v[108:111], a[172:175], a[100:103], v[108:111]\n"
"ds_read_b64_tr_b16 a[248:249], v200 offset:29568\n"
"v_mfma_f32_16x16x32_bf16 v[104:107], a[172:175], a[96:99], v[104:107]\n"
"s_waitcnt lgkmcnt(3)\n"
"ds_read_b64_tr_b16 a[250:251], v200 offset:29824\n"
"v_mfma_f32_16x16x32_bf16 v[96:99], a[240:243], a[104:107], v[96:99]\n"
"ds_read_b64_tr_b16 a[252:253], v200 offset:29600\n"
"v_mfma_f32_16x16x32_bf16 v[100:103], a[240:243], a[108:111], v[100:103]\n"
"s_waitcnt lgkmcnt(3)\n"
"ds_read_b64_tr_b16 a[254:255], v200 offset:29856\n"
"v_mfma_f32_16x16x32_bf16 v[108:111], a[244:247], a[108:111], v[108:111]\n"
"s_waitcnt vmcnt(9)\n"
"ds_read_b64_tr_b16 v[176:177], v200 offset:33792\n"
"buffer_store_dwordx2 v[236:237], v135, s[44:47], s60 offen offset:512 sc0 sc1\n"
"v_mfma_f32_16x16x32_bf16 v[104:107], a[244:247], a[104:107], v[104:107]\n"
"s_waitcnt lgkmcnt(3)\n"
"ds_read_b64_tr_b16 v[178:179], v200 offset:34048\n"
"v_mfma_f32_16x16x32_bf16 v[96:99], a[248:251], a[112:115], v[96:99]\n"
"ds_read_b64_tr_b16 v[180:181], v200 offset:33824\n"
"buffer_load_dwordx4 a[190:193], v139, s[44:47], s94 offen offset:0 sc0 sc1\n"
"v_mfma_f32_16x16x32_bf16 v[100:103], a[248:251], a[116:119], v[100:103]\n"
"s_waitcnt lgkmcnt(3)\n"
"s_add_u32 s94, s86, 0xb00000\n"
"ds_read_b64_tr_b16 v[182:183], v200 offset:34080\n"
"v_mfma_f32_16x16x32_bf16 v[108:111], a[252:255], a[116:119], v[108:111]\n"
"ds_read_b64_tr_b16 v[184:185], v200 offset:38016\n"
"v_mfma_f32_16x16x32_bf16 v[104:107], a[252:255], a[112:115], v[104:107]\n"
"s_waitcnt lgkmcnt(3)\n"
"ds_read_b64_tr_b16 v[186:187], v200 offset:38272\n"
"v_mfma_f32_16x16x32_bf16 v[96:99], v[176:179], a[120:123], v[96:99]\n"
"ds_read_b64_tr_b16 v[188:189], v200 offset:38048\n"
"v_mfma_f32_16x16x32_bf16 v[100:103], v[176:179], a[124:127], v[100:103]\n"
"s_waitcnt vmcnt(9) lgkmcnt(3)\n"
"ds_read_b64_tr_b16 v[190:191], v200 offset:38304\n"
"buffer_store_dwordx2 v[236:237], v135, s[44:47], s14 offen offset:768 sc0 sc1\n"
"v_mfma_f32_16x16x32_bf16 v[108:111], v[180:183], a[124:127], v[108:111]\n"
"ds_read_b64_tr_b16 v[192:193], v202 offset:21120\n"
"v_mfma_f32_16x16x32_bf16 v[104:107], v[180:183], a[120:123], v[104:107]\n"
"s_waitcnt lgkmcnt(3)\n"
"ds_read_b64_tr_b16 v[194:195], v202 offset:21376\n"
"buffer_load_dwordx4 a[222:225], v139, s[44:47], s94 offen offset:0 sc0 sc1\n"
"v_mfma_f32_16x16x32_bf16 v[96:99], v[184:187], a[128:131], v[96:99]\n"
"ds_read_b64_tr_b16 v[196:197], v202 offset:21152\n"
"v_mfma_f32_16x16x32_bf16 v[100:103], v[184:187], a[132:135], v[100:103]\n"
"s_waitcnt lgkmcnt(3)\n"
"ds_read_b64_tr_b16 v[198:199], v202 offset:21408\n"
"v_mfma_f32_16x16x32_bf16 v[108:111], v[188:191], a[132:135], v[108:111]\n"
"ds_read_b64_tr_b16 a[168:169], v200 offset:42240\n"
"v_mfma_f32_16x16x32_bf16 v[104:107], v[188:191], a[128:131], v[104:107]\n"
"s_waitcnt lgkmcnt(3)\n"
"ds_read_b64_tr_b16 a[170:171], v200 offset:42496\n"
"v_mfma_f32_16x16x32_bf16 v[96:99], v[192:195], a[136:139], v[96:99]\n"
"ds_read_b64_tr_b16 a[172:173], v200 offset:42272\n"
"s_cmp_lt_u32 s93, 0x100\n s_cbranch_scc1 L_pskip_%=\n s_add_u32 s94, s87, 0x0\n buffer_load_dwordx4 v[66:69], v134, s[44:47], s94 offen offset:0 sc0 sc1\n s_add_u32 s94, s87, 0x40000\n buffer_load_dwordx4 v[70:73], v134, s[44:47], s94 offen offset:0 sc0 sc1\n s_add_u32 s94, s87, 0x80000\n buffer_load_dwordx4 v[74:77], v134, s[44:47], s94 offen offset:0 sc0 sc1\n s_add_u32 s94, s87, 0xc0000\n buffer_load_dwordx4 v[78:81], v134, s[44:47], s94 offen offset:0 sc0 sc1\n s_add_u32 s94, s87, 0x100000\n buffer_load_dwordx4 v[82:85], v134, s[44:47], s94 offen offset:0 sc0 sc1\n s_add_u32 s94, s87, 0x140000\n buffer_load_dwordx4 v[86:89], v134, s[44:47], s94 offen offset:0 sc0 sc1\n L_pskip_%=:\n s_add_u32 s94, s87, 0x180000\n buffer_load_dwordx4 v[90:93], v134, s[44:47], s94 offen offset:0 sc0 sc1\n"
"v_mfma_f32_16x16x32_bf16 v[100:103], v[192:195], a[140:143], v[100:103]\n"
"s_waitcnt lgkmcnt(3)\n"
"s_add_u32 s94, s86, 0x400000\n"
"ds_read_b64_tr_b16 a[174:175], v200 offset:42528\n"
"v_mfma_f32_16x16x32_bf16 v[108:111], v[196:199], a[140:143], v[108:111]\n"
"ds_read_b64_tr_b16 a[240:241], v200 offset:46464\n"
"v_mfma_f32_16x16x32_bf16 v[104:107], v[196:199], a[136:139], v[104:107]\n"
"s_waitcnt lgkmcnt(3)\n"
"ds_read_b64_tr_b16 a[242:243], v200 offset:46720\n"
"v_mfma_f32_16x16x32_bf16 v[96:99], a[168:171], a[144:147], v[96:99]\n"
"ds_read_b64_tr_b16 a[244:245], v200 offset:46496\n"
"v_mfma_f32_16x16x32_bf16 v[100:103], a[168:171], a[148:151], v[100:103]\n"
"s_waitcnt lgkmcnt(3)\n"
"ds_read_b64_tr_b16 a[246:247], v200 offset:46752\n"
"v_mfma_f32_16x16x32_bf16 v[108:111], a[172:175], a[148:151], v[108:111]\n"
"ds_read_b64_tr_b16 a[248:249], v200 offset:50688\n"
"v_mfma_f32_16x16x32_bf16 v[104:107], a[172:175], a[144:147], v[104:107]\n"
"s_waitcnt lgkmcnt(3)\n"
"ds_read_b64_tr_b16 a[250:251], v200 offset:50944\n"
"v_mfma_f32_16x16x32_bf16 v[96:99], a[240:243], a[152:155], v[96:99]\n"
"ds_read_b64_tr_b16 a[252:253], v200 offset:50720\n"
"v_mfma_f32_16x16x32_bf16 v[100:103], a[240:243], a[156:159], v[100:103]\n"
"s_waitcnt lgkmcnt(3)\n"
"ds_read_b64_tr_b16 a[254:255], v200 offset:50976\n"
"v_mfma_f32_16x16x32_bf16 v[108:111], a[244:247], a[156:159], v[108:111]\n"
"v_mfma_f32_16x16x32_bf16 v[104:107], a[244:247], a[152:155], v[104:107]\n"
"s_waitcnt lgkmcnt(2)\n"
"v_mfma_f32_16x16x32_bf16 v[96:99], a[248:251], a[160:163], v[96:99]\n"
"v_mfma_f32_16x16x32_bf16 v[100:103], a[248:251], a[164:167], v[100:103]\n"
"s_waitcnt lgkmcnt(0)\n"
"v_mfma_f32_16x16x32_bf16 v[108:111], a[252:255], a[164:167], v[108:111]\n"
"v_mfma_f32_16x16x32_bf16 v[104:107], a[252:255], a[160:163], v[104:107]\n"
"s_waitcnt vmcnt(8)\n"
"v_subrev_u32 v238, s18, v230\n"
"v_xor_b32 v239, v230, v231\n"
"v_xor_b32 v239, 0x5bd1e995, v239\n"
"v_max_u32 v238, v238, v239\n"
"v_cmp_ge_u32 s[10:11], v228, v238\n"
"s_nop 0\n"
"s_andn2_b32 s10, 0x70, s10\n s_cbranch_scc0 L_fin_%=\n L_spin_%=:\n s_sub_u32 s8, s8, 1\n s_cbranch_scc1 L_fin_%=\n s_sleep 0\n s_mov_b64 exec, 0x70\n buffer_load_dwordx2 v[230:231], v138, s[44:47], s16 offen offset:0 sc0 sc1\n s_mov_b64 exec, -1\n s_waitcnt vmcnt(0)\n v_subrev_u32 v238, s18, v230\n v_xor_b32 v239, v230, v231\n v_xor_b32 v239, 0x5bd1e995, v239\n v_max_u32 v238, v238, v239\n v_cmp_ge_u32 s[10:11], v228, v238\n s_nop 1\n s_andn2_b32 s10, 0x70, s10\n s_cbranch_scc1 L_spin_%=\n L_fin_%=:\n"
"buffer_load_dwordx4 a[194:197], v139, s[44:47], s94 offen offset:0 sc0 sc1\n"
"s_add_u32 s94, s86, 0xc00000\n"
"buffer_load_dwordx4 a[226:229], v139, s[44:47], s94 offen offset:0 sc0 sc1\n"
"s_add_u32 s94, s86, 0x500000\n"
"buffer_load_dwordx4 a[198:201], v139, s[44:47], s94 offen offset:0 sc0 sc1\n"
"s_add_u32 s94, s86, 0xd00000\n"
"buffer_load_dwordx4 a[230:233], v139, s[44:47], s94 offen offset:0 sc0 sc1\n"
"s_add_u32 s94, s86, 0x600000\n"
"buffer_load_dwordx4 a[202:205], v139, s[44:47], s94 offen offset:0 sc0 sc1\n"
"s_add_u32 s94, s86, 0xe00000\n"
"buffer_load_dwordx4 a[234:237], v139, s[44:47], s94 offen offset:0 sc0 sc1\n"
"v_mfma_f32_16x16x32_bf16 v[104:107], v[128:131], a[176:179], v[104:107]\n"
"v_mfma_f32_16x16x32_bf16 v[108:111], v[128:131], a[208:211], v[108:111]\n"
"v_mfma_f32_16x16x32_bf16 v[96:99], v[128:131], a[180:183], v[96:99]\n"
"s_waitcnt vmcnt(13)\n"
"v_mfma_f32_16x16x32_bf16 v[100:103], v[128:131], a[212:215], v[100:103]\n"
"s_waitcnt vmcnt(12)\n"
"v_mfma_f32_16x16x32_bf16 v[104:107], v[128:131], a[184:187], v[104:107]\n"
"s_waitcnt vmcnt(11)\n"
"v_mfma_f32_16x16x32_bf16 v[108:111], v[128:131], a[216:219], v[108:111]\n"
"s_waitcnt vmcnt(9)\n"
"v_mfma_f32_16x16x32_bf16 v[96:99], v[128:131], a[188:191], v[96:99]\n"
"s_waitcnt vmcnt(7)\n"
"v_mfma_f32_16x16x32_bf16 v[100:103], v[128:131], a[220:223], v[100:103]\n"
"s_waitcnt vmcnt(6)\n"
"v_mfma_f32_16x16x32_bf16 v[40:43], v[128:131], v[64:67], v[40:43]\n"
"v_mfma_f32_16x16x32_bf16 v[32:35], v[128:131], v[68:71], v[32:35]\n"
"v_mfma_f32_16x16x32_bf16 v[40:43], v[128:131], v[72:75], v[40:43]\n"
"v_mfma_f32_16x16x32_bf16 v[32:35], v[128:131], v[76:79], v[32:35]\n"
"v_mfma_f32_16x16x32_bf16 v[40:43], v[128:131], v[80:83], v[40:43]\n"
"v_mfma_f32_16x16x32_bf16 v[32:35], v[128:131], v[84:87], v[32:35]\n"
"v_mfma_f32_16x16x32_bf16 v[40:43], v[128:131], v[88:91], v[40:43]\n"
"v_mfma_f32_16x16x32_bf16 v[32:35], v[128:131], v[92:95], v[32:35]\n"
"s_nop 7\n"
"v_cvt_pk_bf16_f32 v248, v32, v33\n"
"v_cvt_pk_bf16_f32 v249, v34, v35\n"
"ds_write_b64 v229, v[248:249] offset:2048\n"
"v_cvt_pk_bf16_f32 v250, v40, v41\n"
"v_cvt_pk_bf16_f32 v251, v42, v43\n"
"ds_write_b64 v229, v[250:251] offset:2080\n"
"s_nop 1\n"
"ds_read_b128 v[248:251], v232 offset:2048\n"
"s_waitcnt lgkmcnt(0)\n"
"buffer_store_dwordx4 v[248:251], v133, s[96:99], s92 offen offset:0 sc0 sc1\n"
"v_mov_b32 v238, 0x100\n"
"v_mov_b32 v239, 0\n"
"s_mov_b64 exec, 1\n"
"buffer_atomic_add v238, v239, s[44:47], s72 offen sc1\n"
"s_mov_b64 exec, -1\n"
"s_nop 4\n"
"s_waitcnt vmcnt(7)\n"
"v_mfma_f32_16x16x32_bf16 v[104:107], v[128:131], a[192:195], v[104:107]\n"
"s_waitcnt vmcnt(6)\n"
"v_mfma_f32_16x16x32_bf16 v[108:111], v[128:131], a[224:227], v[108:111]\n"
"s_waitcnt vmcnt(5)\n"
"v_mfma_f32_16x16x32_bf16 v[96:99], v[128:131], a[196:199], v[96:99]\n"
"s_waitcnt vmcnt(4)\n"
"v_mfma_f32_16x16x32_bf16 v[100:103], v[128:131], a[228:231], v[100:103]\n"
"s_waitcnt vmcnt(3)\n"
"v_mfma_f32_16x16x32_bf16 v[104:107], v[128:131], a[200:203], v[104:107]\n"
"s_waitcnt vmcnt(2)\n"
"v_mfma_f32_16x16x32_bf16 v[108:111], v[128:131], a[232:235], v[108:111]\n"
"v_mfma_f32_16x16x32_bf16 v[96:99], v[128:131], a[204:207], v[96:99]\n"
"v_mfma_f32_16x16x32_bf16 v[100:103], v[128:131], a[236:239], v[100:103]\n"
"s_nop 6\n"
"v_cvt_pk_bf16_f32 v240, v96, v97\n"
"v_cvt_pk_bf16_f32 v241, v98, v99\n"
"ds_write_b64 v229, v[240:241] offset:0\n"
"v_cvt_pk_bf16_f32 v242, v104, v105\n"
"v_cvt_pk_bf16_f32 v243, v106, v107\n"
"ds_write_b64 v229, v[242:243] offset:32\n"
"s_nop 1\n"
"ds_read_b128 v[240:243], v232 offset:0\n"
"v_cvt_pk_bf16_f32 v244, v100, v101\n"
"v_cvt_pk_bf16_f32 v245, v102, v103\n"
"ds_write_b64 v229, v[244:245] offset:1024\n"
"v_cvt_pk_bf16_f32 v246, v108, v109\n"
"v_cvt_pk_bf16_f32 v247, v110, v111\n"
"ds_write_b64 v229, v[246:247] offset:1056\n"
"s_nop 1\n"
"ds_read_b128 v[244:247], v232 offset:1024\n"
"s_waitcnt lgkmcnt(3)\n"
"buffer_store_dwordx4 v[240:243], v132, s[96:99], s90 offen offset:0 sc0 sc1\n"
"s_waitcnt lgkmcnt(0)\n"
"buffer_store_dwordx4 v[244:247], v132, s[96:99], s91 offen offset:0 sc0 sc1\n"
"s_branch L_end_%=\n"
"L_slow0_%=:\n s_sub_u32 s8, s8, 1\n s_cbranch_scc1 L_give0_%=\n s_sleep 1\n s_mov_b64 exec, 0x30000\n buffer_load_dwordx2 v[230:231], v138, s[44:47], s16 offen offset:0 sc0 sc1\n s_mov_b64 exec, -1\n s_waitcnt vmcnt(0)\n v_subrev_u32 v238, s18, v230\n v_xor_b32 v239, v230, v231\n v_xor_b32 v239, 0x5bd1e995, v239\n v_max_u32 v238, v238, v239\n v_cmp_ge_u32 s[10:11], v228, v238\n s_nop 4\n s_andn2_b32 s10, 0x30000, s10\n s_cbranch_scc1 L_slow0_%=\n s_branch L_back0_%=\n L_give0_%=:\n s_mov_b32 s8, 0\n s_branch L_back0_%=\n"
"L_slow1_%=:\n s_sub_u32 s8, s8, 1\n s_cbranch_scc1 L_give1_%=\n s_sleep 1\n s_mov_b64 exec, 0xc0000\n buffer_load_dwordx2 v[230:231], v138, s[44:47], s16 offen offset:0 sc0 sc1\n s_mov_b64 exec, -1\n s_waitcnt vmcnt(0)\n v_subrev_u32 v238, s18, v230\n v_xor_b32 v239, v230, v231\n v_xor_b32 v239, 0x5bd1e995, v239\n v_max_u32 v238, v238, v239\n v_cmp_ge_u32 s[10:11], v228, v238\n s_nop 4\n s_andn2_b32 s10, 0xc0000, s10\n s_cbranch_scc1 L_slow1_%=\n s_branch L_back1_%=\n L_give1_%=:\n s_mov_b32 s8, 0\n s_branch L_back1_%=\n"
"L_slow2_%=:\n s_sub_u32 s8, s8, 1\n s_cbranch_scc1 L_give2_%=\n s_sleep 1\n s_mov_b64 exec, 0x300000\n buffer_load_dwordx2 v[230:231], v138, s[44:47], s16 offen offset:0 sc0 sc1\n s_mov_b64 exec, -1\n s_waitcnt vmcnt(0)\n v_subrev_u32 v238, s18, v230\n v_xor_b32 v239, v230, v231\n v_xor_b32 v239, 0x5bd1e995, v239\n v_max_u32 v238, v238, v239\n v_cmp_ge_u32 s[10:11], v228, v238\n s_nop 4\n s_andn2_b32 s10, 0x300000, s10\n s_cbranch_scc1 L_slow2_%=\n s_branch L_back2_%=\n L_give2_%=:\n s_mov_b32 s8, 0\n s_branch L_back2_%=\n"
"L_slow3_%=:\n s_sub_u32 s8, s8, 1\n s_cbranch_scc1 L_give3_%=\n s_sleep 1\n s_mov_b64 exec, 0xc00000\n buffer_load_dwordx2 v[230:231], v138, s[44:47], s16 offen offset:0 sc0 sc1\n s_mov_b64 exec, -1\n s_waitcnt vmcnt(0)\n v_subrev_u32 v238, s18, v230\n v_xor_b32 v239, v230, v231\n v_xor_b32 v239, 0x5bd1e995, v239\n v_max_u32 v238, v238, v239\n v_cmp_ge_u32 s[10:11], v228, v238\n s_nop 4\n s_andn2_b32 s10, 0xc00000, s10\n s_cbranch_scc1 L_slow3_%=\n s_branch L_back3_%=\n L_give3_%=:\n s_mov_b32 s8, 0\n s_branch L_back3_%=\n"
"L_slow4_%=:\n s_sub_u32 s8, s8, 1\n s_cbranch_scc1 L_give4_%=\n s_sleep 1\n s_mov_b32 exec_hi, 0\n s_mov_b32 exec_lo, s93\n buffer_load_dwordx2 v[230:231], v138, s[44:47], s16 offen offset:0 sc0 sc1\n s_mov_b64 exec, -1\n s_waitcnt vmcnt(0)\n v_subrev_u32 v238, s18, v230\n v_xor_b32 v239, v230, v231\n v_xor_b32 v239, 0x5bd1e995, v239\n v_max_u32 v238, v238, v239\n v_cmp_ge_u32 s[10:11], v228, v238\n s_nop 4\n s_andn2_b32 s10, s93, s10\n s_cbranch_scc1 L_slow4_%=\n s_branch L_back4_%=\n L_give4_%=:\n s_mov_b32 s8, 0\n s_branch L_back4_%=\n"
".fill 512, 4, 0xbf800000\n"
"L_end_%=:\n"
  :: "s"(__builtin_amdgcn_kernarg_segment_ptr()), "s"(blockIdx.x), "s"(wid), "s"((unsigned)(size_t)smem)
  : "v0","v1","v2","v3","v4","v5","v6","v7","v8","v9","v10","v11","v12","v13","v14","v15","v16","v17","v18","v19","v20","v21","v22","v23","v24","v25","v26","v27","v28","v29","v30","v31","v32","v33","v34","v35","v36","v37","v38","v39","v40","v41","v42","v43","v44","v45","v46","v47","v48","v49","v50","v51","v52","v53","v54","v55","v56","v57","v58","v59","v60","v61","v62","v63","v64","v65","v66","v67","v68","v69","v70","v71","v72","v73","v74","v75","v76","v77","v78","v79","v80","v81","v82","v83","v84","v85","v86","v87","v88","v89","v90","v91","v92","v93","v94","v95","v96","v97","v98","v99","v100","v101","v102","v103","v104","v105","v106","v107","v108","v109","v110","v111","v112","v113","v114","v115","v116","v117","v118","v119","v120","v121","v122","v123","v124","v125","v126","v127","v128","v129","v130","v131","v132","v133","v134","v135","v136","v137","v138","v139","v140","v141","v142","v143","v144","v145","v146","v147","v148","v149","v150","v151","v152","v153","v154","v155","v156","v157","v158","v159","v160","v161","v162","v163","v164","v165","v166","v167","v168","v169","v170","v171","v172","v173","v174","v175","v176","v177","v178","v179","v180","v181","v182","v183","v184","v185","v186","v187","v188","v189","v190","v191","v192","v193","v194","v195","v196","v197","v198","v199","v200","v201","v202","v203","v204","v205","v206","v207","v208","v209","v210","v211","v212","v213","v214","v215","v216","v217","v218","v219","v220","v221","v222","v223","v224","v225","v226","v227","v228","v229","v230","v231","v232","v233","v234","v235","v236","v237","v238","v239","v240","v241","v242","v243","v244","v245","v246","v247","v248","v249","v250","v251","v252","v253","v254","v255","a0","a1","a2","a3","a4","a5","a6","a7","a8","a9","a10","a11","a12","a13","a14","a15","a16","a17","a18","a19","a20","a21","a22","a23","a24","a25","a26","a27","a28","a29","a30","a31","a32","a33","a34","a35","a36","a37","a38","a39","a40","a41","a42","a43","a44","a45","a46","a47","a48","a49","a50","a51","a52","a53","a54","a55","a56","a57","a58","a59","a60","a61","a62","a63","a64","a65","a66","a67","a68","a69","a70","a71","a72","a73","a74","a75","a76","a77","a78","a79","a80","a81","a82","a83","a84","a85","a86","a87","a88","a89","a90","a91","a92","a93","a94","a95","a96","a97","a98","a99","a100","a101","a102","a103","a104","a105","a106","a107","a108","a109","a110","a111","a112","a113","a114","a115","a116","a117","a118","a119","a120","a121","a122","a123","a124","a125","a126","a127","a128","a129","a130","a131","a132","a133","a134","a135","a136","a137","a138","a139","a140","a141","a142","a143","a144","a145","a146","a147","a148","a149","a150","a151","a152","a153","a154","a155","a156","a157","a158","a159","a160","a161","a162","a163","a164","a165","a166","a167","a168","a169","a170","a171","a172","a173","a174","a175","a176","a177","a178","a179","a180","a181","a182","a183","a184","a185","a186","a187","a188","a189","a190","a191","a192","a193","a194","a195","a196","a197","a198","a199","a200","a201","a202","a203","a204","a205","a206","a207","a208","a209","a210","a211","a212","a213","a214","a215","a216","a217","a218","a219","a220","a221","a222","a223","a224","a225","a226","a227","a228","a229","a230","a231","a232","a233","a234","a235","a236","a237","a238","a239","a240","a241","a242","a243","a244","a245","a246","a247","a248","a249","a250","a251","a252","a253","a254","a255","s8","s9","s10","s11","s12","s13","s14","s15","s16","s17","s18","s19","s20","s21","s22","s23","s24","s25","s26","s27","s28","s29","s30","s31","s32","s33","s34","s35","s36","s37","s38","s39","s40","s41","s42","s43","s44","s45","s46","s47","s48","s49","s50","s51","s52","s53","s54","s55","s56","s57","s58","s59","s60","s61","s62","s63","s64","s65","s66","s67","s68","s69","s70","s71","s72","s73","s74","s75","s76","s77","s78","s79","s80","s81","s82","s83","s84","s85","s86","s87","s88","s89","s90","s91","s92","s93","s94","s95","s96","s97","s98","s99","vcc","scc","memory");
}

extern "C" void kernel_launch(void* const* d_in, const int* in_sizes, int n_in, void* d_out, int out_size, void* d_ws,
                              size_t ws_size, hipStream_t stream) {
  fused_main<<<256, 256, 0, stream>>>(d_in[0], d_in[1], d_in[2], d_in[4], d_in[5], d_in[6], d_ws, d_out);
}
```
